```python
import torch

def _softmax(x, axis=-1):
    return torch.softmax(x, dim=axis)

def _gelu(x):
    return torch.nn.functional.gelu(x, approximate='tanh')

def _silu(x):
    return torch.nn.functional.silu(x)

def _sigmoid(x):
    return torch.sigmoid(x)

def _layernorm(x, axis=-1, eps=1e-05):
    m = x.mean(dim=axis, keepdim=True)
    v = x.var(dim=axis, keepdim=True, unbiased=False)
    return (x - m) / torch.sqrt(v + eps)

def _scatter_add(tgt, idx, src, axis=0):
    return tgt.clone().index_add_(axis, idx, src)

def _segment_sum(src, seg, n):
    out = torch.zeros((n,) + src.shape[1:], dtype=src.dtype)
    return out.index_add_(0, seg, src)

def _pad(x, pw):
    flat = tuple((v for pair in reversed(pw) for v in pair))
    return torch.nn.functional.pad(x, flat)
kernel_name = 'gated_mix__compressed_kv_cla__mixer_gmlp__prompt'

def setup_inputs():

    _w = lambda a: (lambda t: t.to(torch.bfloat16) if t.is_floating_point() else t)(torch.as_tensor(a))

    def _A_setup():
        B, H, Sq, Sk, D, M = (1, 16, 2048, 2048, 256, 448)
        Dc = 4 * D
        return {'x': _w((lambda *s: torch.randn(*s, dtype=torch.bfloat16) * 0.05)(B, Sq, M)), 'c_kv': _w((lambda *s: torch.randn(*s, dtype=torch.bfloat16) * 0.8)(B, Sk, Dc)), 'w_q': _w((lambda *s: torch.randn(*s, dtype=torch.bfloat16) * 6.4)(M, H, D) * M ** (-0.5)), 'w_uk': _w((lambda *s: torch.randn(*s, dtype=torch.bfloat16) * 6.4)(Dc, H, D) * Dc ** (-0.5)), 'w_uv': _w((lambda *s: torch.randn(*s, dtype=torch.bfloat16) * 0.8)(Dc, H, D) * Dc ** (-0.5)), 'w_o': _w((lambda *s: torch.randn(*s, dtype=torch.bfloat16) * 0.8)(H, D, M) * (H * D) ** (-0.5))}
    _A_in = _A_setup()

    def _B_setup():
        B, S, D, F = (1, 2048, 448, 1792)
        Fh = F // 2
        return {'x': _w((lambda *s: torch.randn(*s, dtype=torch.bfloat16) * 0.05)(B, S, D)), 'w_in': _w((lambda *s: torch.randn(*s, dtype=torch.bfloat16) * 0.4)(D, 2 * Fh)), 'w_sp': _w(0.01 * (lambda *s: torch.randn(*s, dtype=torch.bfloat16) * 0.4)(S, S)), 'b_sp': _w(1.0 + 0.01 * (lambda *s: torch.randn(*s, dtype=torch.bfloat16) * 0.05)(S)), 'w_out': _w((lambda *s: torch.randn(*s, dtype=torch.bfloat16) * 0.05)(Fh, D))}
    _B_in = _B_setup()
    return {'A_x': _A_in['x'], 'A_c_kv': _A_in['c_kv'], 'A_w_q': _A_in['w_q'], 'A_w_uk': _A_in['w_uk'], 'A_w_uv': _A_in['w_uv'], 'A_w_o': _A_in['w_o'], 'B_x': _B_in['x'], 'B_w_in': _B_in['w_in'], 'B_w_sp': _B_in['w_sp'], 'B_b_sp': _B_in['b_sp'], 'B_w_out': _B_in['w_out'], 'gate_w': _w((lambda *s: torch.randn(*s, dtype=torch.bfloat16) * 0.05)(448) * 16.0)}

def reference(A_x, A_c_kv, A_w_q, A_w_uk, A_w_uv, A_w_o, B_x, B_w_in, B_w_sp, B_b_sp, B_w_out, gate_w):

    def _A_ref(x, c_kv, w_q, w_uk, w_uv, w_o):
        B, Sq, M = (x.shape[0], x.shape[1], x.shape[2])
        Sk = c_kv.shape[1]
        H, D = (w_q.shape[1], w_q.shape[2])
        q = torch.einsum('bqm,mhd->bhqd', x, w_q)
        k = torch.einsum('bkc,chd->bhkd', c_kv, w_uk)
        v = torch.einsum('bkc,chd->bhkd', c_kv, w_uv)
        scores = torch.einsum('bhqd,bhkd->bhqk', q, k) * D ** (-0.5)
        off = Sk - Sq
        qi = torch.arange(Sq)[:, None]
        kj = torch.arange(Sk)[None, :]
        cmask = kj <= qi + off
        scores = torch.where(cmask[None, None], scores, -1000000000.0)
        p = _softmax(scores, axis=-1)
        ctx = torch.einsum('bhqk,bhkd->bhqd', p, v)
        return torch.einsum('bhqd,hdm->bqm', ctx, w_o)

    def _B_ref(x, w_in, w_sp, b_sp, w_out):
        B, S, D = x.shape
        F = w_in.shape[-1]
        Fh = F // 2
        z = _gelu(torch.einsum('bsd,df->bsf', x, w_in))
        z1 = z[..., :Fh].clone()
        z2 = _layernorm(z[..., Fh:], axis=-1)
        g = torch.einsum('ts,bsf->btf', w_sp, z2) + b_sp[None, :, None]
        return torch.einsum('bsf,fd->bsd', z1 * g, w_out)
    x = A_x
    g = _sigmoid((x * gate_w).sum(-1, keepdims=True))
    a = _A_ref(x, A_c_kv, A_w_q, A_w_uk, A_w_uv, A_w_o)
    b = _B_ref(x, B_w_in, B_w_sp, B_b_sp, B_w_out)
    return g * a + (1 - g) * b
```

```cpp
#include <hip/hip_runtime.h>
#include <hip/hip_bf16.h>
#include <stdint.h>
typedef unsigned u32x4 __attribute__((ext_vector_type(4)));
typedef unsigned u32x2 __attribute__((ext_vector_type(2)));
typedef float f32x4 __attribute__((ext_vector_type(4)));
#define DI __device__ __forceinline__
DI u32x4 mk_srd(const void* p, unsigned bytes) {
  unsigned long long a = (unsigned long long)p;
  u32x4 r; r[0] = (unsigned)a; r[1] = (unsigned)(a >> 32) & 0xffffu; r[2] = bytes; r[3] = 0x00020000u; return r;
}
DI unsigned lane_fresh() { unsigned l; asm volatile("v_mbcnt_lo_u32_b32 %0, -1, 0\n\tv_mbcnt_hi_u32_b32 %0, -1, %0" : "=v"(l)); return l; }
DI unsigned rfl(unsigned x) { return __builtin_amdgcn_readfirstlane(x); }
DI float bf2f(unsigned short v) { return __uint_as_float(((unsigned)v) << 16); }
DI float bflo(unsigned v) { return __uint_as_float(v << 16); }
DI float bfhi(unsigned v) { return __uint_as_float(v & 0xffff0000u); }
DI unsigned pk2(float lo, float hi) {
  typedef float f2 __attribute__((ext_vector_type(2))); typedef __bf16 b2 __attribute__((ext_vector_type(2)));
  f2 v = {lo, hi}; b2 b = __builtin_convertvector(v, b2); return __builtin_bit_cast(unsigned, b);
}

struct Params {
  const char *x, *ckv, *wq, *wuk, *wuv, *wo, *win, *wsp, *bsp, *wout, *gatew;
  char *out, *ws;
};
typedef const __attribute__((address_space(4))) Params* KP;
DI KP kp_fresh() { KP p = (KP)__builtin_amdgcn_kernarg_segment_ptr(); asm volatile("" : "+s"(p)); return p; }

struct GS { unsigned vDA0, vDA1, vDB0, vDB1, vRA0, vRA1, vRB[8], sWA, sWB; };
template <int BM, int BN, int LDA, int LDB, bool AF, int NSUB, int LDSA, int LDSB>
DI GS gs_init(unsigned lane, unsigned w) {
  GS s;
  const unsigned wm = w >> 1, wn = w & 1, g = lane >> 4, c = lane & 15, q = c >> 2, p4 = lane & 3;
  const unsigned r8 = lane >> 3, ch8 = lane & 7, r4 = lane >> 4, ch16 = lane & 15;
  constexpr int MT = BM / 32, NT = BN / 32;
  if (!AF) {
    s.vDA0 = (w * (BM / 4) + r8) * LDA + ((ch8 ^ (r8 >> 1)) << 4);
    s.vDA1 = (w * (BM / 4) + r8) * LDA + ((ch8 ^ (4u + (r8 >> 1))) << 4);
    s.vRA0 = LDSA + (wm * (BM / 2) + c) * 128u + ((g ^ (c >> 1)) << 4);
    s.vRA1 = LDSA + (wm * (BM / 2) + c) * 128u + (((4u + g) ^ (c >> 1)) << 4);
    s.sWA = w * (BM * 32);
  } else {
    s.vDA0 = s.vDA1 = w * (BM / 64) * LDA + lane * 16u;
    s.vRA0 = s.vRA1 = LDSA + wm * MT * (2 * NSUB) * 1024u + lane * 16u;
    s.sWA = w * (BM * 32 * NSUB);
  }
  if (BN >= 128) {
    const unsigned bw = (BN == 256) ? ((w >> 1) * 256u + ((w & 1) * 32u + r4) * LDB) : ((w * 16u + r4) * LDB);
    s.vDB0 = bw + (((((ch16 >> 1) ^ r4) << 1) | (ch16 & 1)) << 4);
    s.vDB1 = bw + (((((ch16 >> 1) ^ (r4 | 4u)) << 1) | (ch16 & 1)) << 4);
    const unsigned f = q | ((g & 1) << 2);
    #pragma unroll
    for (int ni = 0; ni < 8; ++ni) {
      const unsigned seg = (BN == 256) ? ni : (4 * wn + (ni & 3));
      s.vRB[ni] = LDSB + ((BN == 256) ? wn * 16384u : 0u) + (8u * g + q) * 256u + ((seg ^ f) << 5) + 8u * p4;
    }
  } else {
    const unsigned bw = (w * 16u + r8) * LDB;
    const unsigned f0 = AF ? ((r8 >> 1) & 3u) : ((r8 >> 1) & 1u), f1 = AF ? f0 : (f0 | 2u);
    s.vDB0 = bw + (((((ch8 >> 1) ^ f0) << 1) | (ch8 & 1)) << 4);
    s.vDB1 = bw + (((((ch8 >> 1) ^ f1) << 1) | (ch8 & 1)) << 4);
    const unsigned row = AF ? (4u * g + q) : (8u * g + q);
    const unsigned f = AF ? ((row >> 1) & 3u) : ((q >> 1) | ((g & 1) << 1));
    #pragma unroll
    for (int ni = 0; ni < 8; ++ni) {
      const unsigned seg = 2 * wn + (ni & 1);
      s.vRB[ni] = LDSB + row * 128u + ((seg ^ f) << 5) + 8u * p4;
    }
  }
  s.sWB = w * (BN * 32);
  return s;
}

__global__ __launch_bounds__(256, 1) void fused_block(Params prm) {
  unsigned long long ka = (unsigned long long)__builtin_amdgcn_kernarg_segment_ptr();
  unsigned bid = blockIdx.x, tid = threadIdx.x;
  asm volatile(
".macro xad d,a,b\nv_add_f32 v\\d,\\a,v\\b\n.endm\n.macro xam s,c\ns_add_u32 m0,s\\s,\\c\n.endm\n.macro xar d,a\nv_accvgpr_read_b32 v\\d,a\\a\n.endm\n"
".macro xcv d,a,b\nv_cvt_pk_bf16_f32 v\\d,v\\a,v\\b\n.endm\n.macro xdl v,s,o,i\nbuffer_load_dwordx4 v\\v,s[\\s:\\s+3],\\o offen offset:\\i lds\n.endm\n"
".macro xdl1 v,s,o,i\nbuffer_load_dwordx4 v\\v,s[\\s:\\s+3],\\o offen offset:\\i sc1 lds\n.endm\n.macro xex d,a\nv_exp_f32 v\\d,v\\a\n.endm\n"
".macro xfm d,a,b,c\nv_fma_f32 v\\d,\\a,\\b,\\c\n.endm\n.macro xm16 d,a,b\n"
"v_mfma_f32_16x16x32_bf16 a[\\d:\\d+3],v[\\a:\\a+3],v[\\b:\\b+3],a[\\d:\\d+3]\n.endm\n.macro xm16z d,a,b\n"
"v_mfma_f32_16x16x32_bf16 a[\\d:\\d+3],v[\\a:\\a+3],v[\\b:\\b+3],0\n.endm\n.macro xm32 d,a,b\n"
"v_mfma_f32_32x32x16_bf16 a[\\d:\\d+15],v[\\a:\\a+3],v[\\b:\\b+3],a[\\d:\\d+15]\n.endm\n.macro xm32v d,a,b,c\n"
"v_mfma_f32_32x32x16_bf16 v[\\d:\\d+15],v[\\a:\\a+3],v[\\b:\\b+3],v[\\c:\\c+15]\n.endm\n.macro xmu d,a,b\nv_mul_f32 v\\d,\\a,v\\b\n.endm\n"
".macro xrb d,a,o\nds_read_b128 v[\\d:\\d+3],v\\a offset:\\o\n.endm\n.macro xrc d,a\nv_rcp_f32 v\\d,v\\a\n.endm\n.macro xrt d,a,o\n"
"ds_read_b64_tr_b16 v[\\d:\\d+1],v\\a offset:\\o\n.endm\n.macro xst1 d,v,s,o,i\n"
"buffer_store_dwordx4 v[\\d:\\d+3],v\\v,s[\\s:\\s+3],\\o offen offset:\\i sc1\n.endm\n.macro xwl n\ns_waitcnt lgkmcnt(\\n)\n.endm\n.macro xx3 d,a,b,c\n"
"v_max3_f32 v\\d,v\\a,v\\b,v\\c\n.endm\ns_nop 3\nv_readfirstlane_b32 s3,v0\ns_nop 3\ns_lshr_b32 s3,s3,6\ns_load_dwordx2 s[4:5],s[0:1],96\n"
"s_lshr_b32 s88,s2,3\ns_mul_i32 s88,s88,37\ns_lshr_b32 s88,s88,8\ns_lshr_b32 s89,s88,2\ns_xor_b32 s89,s89,1\ns_and_b32 s100,s2,7\n"
"s_lshl_b32 s100,s100,1\ns_lshr_b32 s99,s2,3\ns_and_b32 s99,s99,1\ns_add_u32 s100,s100,s99\ns_lshr_b32 s98,s2,3\ns_lshr_b32 s98,s98,2\n"
"s_sub_u32 s98,15,s98\ns_lshr_b32 s97,s2,3\ns_lshr_b32 s97,s97,2\ns_lshr_b32 s96,s2,3\ns_lshr_b32 s96,s96,1\ns_and_b32 s96,s96,1\ns_cmp_lg_u32 s96,0\n"
"s_cselect_b32 s99,s98,s97\ns_lshl_b32 s99,s99,4\ns_or_b32 s100,s100,s99\ns_lshr_b32 s99,s2,3\ns_lshr_b32 s99,s99,2\ns_lshl_b32 s99,s99,8\n"
"s_or_b32 s100,s100,s99\ns_and_b32 s99,s2,7\ns_lshl_b32 s99,s99,1\ns_lshr_b32 s96,s2,3\ns_and_b32 s96,s96,1\ns_add_u32 s99,s99,s96\n"
"s_lshl_b32 s99,s99,12\ns_or_b32 s100,s100,s99\ns_lshr_b32 s99,s2,3\ns_and_b32 s99,s99,3\ns_lshr_b32 s99,s99,1\ns_lshl_b32 s99,s99,16\n"
"s_or_b32 s100,s100,s99\ns_and_b32 s87,s2,7\ns_lshr_b32 s87,s87,1\ns_lshl_b32 s87,s87,2\ns_cmp_lg_u32 s89,0\ns_cselect_b32 s99,s88,0\n"
"s_add_u32 s87,s87,s99\ns_lshl_b32 s87,s87,17\ns_and_b32 s99,s2,7\ns_and_b32 s99,s99,1\ns_mul_i32 s99,s99,7\ns_lshr_b32 s96,s2,3\n"
"s_add_u32 s99,s99,s96\ns_mul_i32 s96,s88,7\ns_sub_u32 s99,s99,s96\ns_lshl_b32 s99,s99,21\ns_or_b32 s87,s87,s99\ns_lshr_b32 s99,s2,3\n"
"s_lshr_b32 s99,s99,2\ns_lshl_b32 s99,s99,25\ns_or_b32 s87,s87,s99\ns_lshr_b32 s99,s2,3\ns_lshr_b32 s99,s99,1\ns_and_b32 s99,s99,1\n"
"s_lshl_b32 s99,s99,28\ns_or_b32 s87,s87,s99\ns_lshl_b32 s99,s89,29\ns_or_b32 s87,s87,s99\ns_or_b32 s100,s100,s87\nxwl 0\ns_and_b32 s98,s100,15\n"
"s_add_u32 s98,s98,9\ns_lshr_b32 s98,s98,4\ns_cmp_lg_u32 s98,0\ns_cbranch_scc1 Lqz2_1_%=x\ns_nop 4\nv_mbcnt_lo_u32_b32 v252,-1,0\n"
"v_mbcnt_hi_u32_b32 v252,-1,v252\ns_load_dwordx2 s[76:77],s[0:1],0\ns_load_dwordx2 s[78:79],s[0:1],16\ns_load_dwordx2 s[80:81],s[0:1],48\nxwl 0\n"
"s_lshr_b32 s99,s100,4\ns_and_b32 s99,s99,15\ns_mul_i32 s99,s99,0x1c000\ns_add_u32 s16,s76,s99\ns_addc_u32 s17,s77,0\ns_and_b32 s17,s17,0xffff\n"
"s_lshr_b32 s18,s100,4\ns_and_b32 s18,s18,15\ns_mul_i32 s18,s18,0x1c000\ns_sub_u32 s18,0x1c0000,s18\ns_mov_b32 s19,0x20000\ns_and_b32 s99,s100,15\n"
"s_lshl_b32 s99,s99,9\ns_add_u32 s20,s78,s99\ns_addc_u32 s21,s79,0\ns_and_b32 s21,s21,0xffff\ns_and_b32 s22,s100,15\ns_lshl_b32 s22,s22,9\n"
"s_sub_u32 s22,0x380000,s22\ns_mov_b32 s23,0x20000\ns_and_b32 s99,s100,15\ns_lshl_b32 s99,s99,8\ns_add_u32 s24,s80,s99\ns_addc_u32 s25,s81,0\n"
"s_and_b32 s25,s25,0xffff\ns_and_b32 s26,s100,15\ns_lshl_b32 s26,s26,8\ns_sub_u32 s26,0x188000,s26\ns_mov_b32 s27,0x20000\nv_lshrrev_b32 v0,3,v252\n"
"s_lshl_b32 s99,s3,5\nv_add_u32 v0,s99,v0\nv_mul_u32_u24 v0,0x380,v0\nv_and_b32 v247,7,v252\nv_lshrrev_b32 v246,3,v252\nv_lshrrev_b32 v246,1,v246\n"
"v_xor_b32 v247,v247,v246\nv_lshlrev_b32 v247,4,v247\nv_add_u32 v0,v0,v247\nv_xor_b32 v1,64,v0\nv_lshrrev_b32 v2,4,v252\ns_and_b32 s99,s3,1\n"
"s_lshl_b32 s99,s99,5\nv_add_u32 v2,s99,v2\nv_lshlrev_b32 v2,13,v2\ns_lshr_b32 s99,s3,1\ns_lshl_b32 s99,s99,8\nv_add_u32 v2,s99,v2\n"
"v_and_b32 v247,15,v252\nv_lshrrev_b32 v247,1,v247\nv_lshrrev_b32 v246,4,v252\nv_xor_b32 v247,v247,v246\nv_lshlrev_b32 v247,1,v247\n"
"v_and_b32 v246,15,v252\nv_and_b32 v246,1,v246\nv_or_b32 v247,v247,v246\nv_lshlrev_b32 v247,4,v247\nv_add_u32 v2,v2,v247\nv_xor_b32 v3,0x80,v2\n"
"v_lshrrev_b32 v4,4,v252\ns_lshl_b32 s99,s3,4\nv_add_u32 v4,s99,v4\nv_mul_u32_u24 v4,0xe00,v4\nv_and_b32 v247,15,v252\nv_lshrrev_b32 v247,1,v247\n"
"v_lshrrev_b32 v246,4,v252\nv_xor_b32 v247,v247,v246\nv_lshlrev_b32 v247,1,v247\nv_and_b32 v246,15,v252\nv_and_b32 v246,1,v246\n"
"v_or_b32 v247,v247,v246\nv_lshlrev_b32 v247,4,v247\nv_add_u32 v4,v4,v247\nv_xor_b32 v5,0x80,v4\nv_and_b32 v6,15,v252\ns_lshr_b32 s99,s3,1\n"
"s_lshl_b32 s99,s99,6\nv_add_u32 v6,s99,v6\nv_lshlrev_b32 v6,7,v6\nv_lshrrev_b32 v247,4,v252\nv_and_b32 v246,15,v252\nv_lshrrev_b32 v246,1,v246\n"
"v_xor_b32 v247,v247,v246\nv_lshlrev_b32 v247,4,v247\nv_add_u32 v6,v6,v247\nv_add_u32 v6,0x10000,v6\nv_xor_b32 v7,64,v6\nv_lshrrev_b32 v250,4,v252\n"
"v_lshlrev_b32 v250,3,v250\nv_and_b32 v247,15,v252\nv_lshrrev_b32 v247,2,v247\nv_add_u32 v250,v250,v247\nv_lshlrev_b32 v250,8,v250\n"
"s_and_b32 s99,s3,1\ns_lshl_b32 s99,s99,14\nv_add_u32 v250,s99,v250\nv_and_b32 v247,3,v252\nv_lshlrev_b32 v247,3,v247\nv_add_u32 v250,v250,v247\n"
"v_and_b32 v251,15,v252\nv_lshrrev_b32 v251,2,v251\nv_lshrrev_b32 v247,4,v252\nv_and_b32 v247,1,v247\nv_lshlrev_b32 v247,2,v247\n"
"v_or_b32 v251,v251,v247\nv_lshlrev_b32 v251,5,v251\nv_xor_b32 v8,0,v251\nv_add_u32 v8,v8,v250\nv_xor_b32 v9,32,v251\nv_add_u32 v9,v9,v250\n"
"v_xor_b32 v10,64,v251\nv_add_u32 v10,v10,v250\nv_xor_b32 v11,0x60,v251\nv_add_u32 v11,v11,v250\nv_xor_b32 v12,0x80,v251\nv_add_u32 v12,v12,v250\n"
"v_xor_b32 v13,0xa0,v251\nv_add_u32 v13,v13,v250\nv_xor_b32 v14,0xc0,v251\nv_add_u32 v14,v14,v250\nv_xor_b32 v15,0xe0,v251\nv_add_u32 v15,v15,v250\n"
"v_lshrrev_b32 v250,4,v252\nv_lshlrev_b32 v250,3,v250\nv_and_b32 v247,15,v252\nv_lshrrev_b32 v247,2,v247\nv_add_u32 v250,v250,v247\n"
"v_lshlrev_b32 v250,8,v250\nv_and_b32 v247,3,v252\nv_lshlrev_b32 v247,3,v247\nv_add_u32 v250,v250,v247\nv_add_u32 v250,0x1c000,v250\n"
"v_and_b32 v251,15,v252\nv_lshrrev_b32 v251,2,v251\nv_lshrrev_b32 v247,4,v252\nv_and_b32 v247,1,v247\nv_lshlrev_b32 v247,2,v247\n"
"v_or_b32 v251,v251,v247\ns_and_b32 s99,s3,1\ns_lshl_b32 s99,s99,2\nv_xor_b32 v251,s99,v251\nv_lshlrev_b32 v251,5,v251\nv_xor_b32 v16,0,v251\n"
"v_add_u32 v16,v16,v250\nv_xor_b32 v17,32,v251\nv_add_u32 v17,v17,v250\nv_xor_b32 v18,64,v251\nv_add_u32 v18,v18,v250\nv_xor_b32 v19,0x60,v251\n"
"v_add_u32 v19,v19,v250\ns_load_dwordx2 s[82:83],s[0:1],8\ns_load_dwordx2 s[84:85],s[0:1],24\ns_load_dwordx2 s[86:87],s[0:1],32\nxwl 0\n"
"s_lshr_b32 s99,s100,8\ns_and_b32 s99,s99,15\ns_lshl_b32 s99,s99,19\ns_add_u32 s28,s82,s99\ns_addc_u32 s29,s83,0\ns_and_b32 s29,s29,0xffff\n"
"s_lshr_b32 s30,s100,8\ns_and_b32 s30,s30,15\ns_lshl_b32 s30,s30,19\ns_sub_u32 s30,0x400000,s30\ns_mov_b32 s31,0x20000\ns_lshr_b32 s99,s100,16\n"
"s_and_b32 s99,s99,1\ns_cmp_lg_u32 s99,0\ns_cselect_b32 s74,s86,s84\ns_lshr_b32 s99,s100,16\ns_and_b32 s99,s99,1\ns_cmp_lg_u32 s99,0\n"
"s_cselect_b32 s75,s87,s85\ns_lshr_b32 s99,s100,12\ns_and_b32 s99,s99,15\ns_lshl_b32 s99,s99,9\ns_add_u32 s32,s74,s99\ns_addc_u32 s33,s75,0\n"
"s_and_b32 s33,s33,0xffff\ns_lshr_b32 s34,s100,12\ns_and_b32 s34,s34,15\ns_lshl_b32 s34,s34,9\ns_sub_u32 s34,0x800000,s34\ns_mov_b32 s35,0x20000\n"
"v_lshrrev_b32 v160,3,v252\ns_lshl_b32 s99,s3,6\nv_add_u32 v160,s99,v160\nv_lshlrev_b32 v160,11,v160\nv_and_b32 v247,7,v252\n"
"v_lshrrev_b32 v246,3,v252\nv_lshrrev_b32 v246,1,v246\nv_xor_b32 v247,v247,v246\nv_lshlrev_b32 v247,4,v247\nv_add_u32 v160,v160,v247\n"
"v_xor_b32 v161,64,v160\nv_lshrrev_b32 v162,4,v252\ns_and_b32 s99,s3,1\ns_lshl_b32 s99,s99,5\nv_add_u32 v162,s99,v162\nv_lshlrev_b32 v162,13,v162\n"
"s_lshr_b32 s99,s3,1\ns_lshl_b32 s99,s99,8\nv_add_u32 v162,s99,v162\nv_and_b32 v247,15,v252\nv_lshrrev_b32 v247,1,v247\nv_lshrrev_b32 v246,4,v252\n"
"v_xor_b32 v247,v247,v246\nv_lshlrev_b32 v247,1,v247\nv_and_b32 v246,15,v252\nv_and_b32 v246,1,v246\nv_or_b32 v247,v247,v246\n"
"v_lshlrev_b32 v247,4,v247\nv_add_u32 v162,v162,v247\nv_xor_b32 v163,0x80,v162\ns_lshl_b32 s99,s2,14\ns_add_u32 s53,s82,s99\ns_addc_u32 s54,s83,0\n"
"s_and_b32 s54,s54,0xffff\ns_lshl_b32 s99,s2,15\ns_add_u32 s55,s84,s99\ns_addc_u32 s56,s85,0\ns_and_b32 s56,s56,0xffff\ns_lshl_b32 s99,s2,15\n"
"s_add_u32 s57,s86,s99\ns_addc_u32 s58,s87,0\ns_and_b32 s58,s58,0xffff\ns_lshr_b32 s52,s100,16\ns_and_b32 s52,s52,1\nv_lshlrev_b32 v164,6,v252\n"
"s_lshl_b32 s99,s3,12\nv_add_u32 v164,s99,v164\nv_lshrrev_b32 v30,4,v252\nv_lshrrev_b32 v30,1,v30\nv_lshlrev_b32 v30,10,v30\ns_lshr_b32 s99,s3,1\n"
"s_lshl_b32 s99,s99,15\ns_and_b32 s98,s3,1\ns_lshl_b32 s98,s98,13\ns_add_u32 s99,s99,s98\nv_add_u32 v30,s99,v30\nv_lshrrev_b32 v247,4,v252\n"
"v_and_b32 v247,1,v247\nv_lshlrev_b32 v247,9,v247\nv_add_u32 v30,v30,v247\nv_and_b32 v247,15,v252\nv_lshlrev_b32 v247,4,v247\nv_add_u32 v30,v30,v247\n"
"s_and_b32 s99,s100,15\ns_lshl_b32 s99,s99,20\ns_add_u32 s99,0x2000000,s99\ns_lshr_b32 s98,s100,4\ns_and_b32 s98,s98,15\ns_lshl_b32 s98,s98,16\n"
"s_add_u32 s99,s99,s98\ns_add_u32 s40,s4,s99\ns_addc_u32 s41,s5,0\ns_and_b32 s41,s41,0xffff\ns_mov_b32 s42,0x10000\ns_mov_b32 s43,0x20000\n"
"s_add_u32 s44,s4,0x6c00000\ns_addc_u32 s45,s5,0\ns_and_b32 s45,s45,0xffff\ns_mov_b32 s46,0x8000\ns_mov_b32 s47,0x20000\nv_lshlrev_b32 v31,4,v252\n"
"s_lshr_b32 s99,s3,1\ns_mul_i32 s99,s99,0x1c000\ns_and_b32 s98,s3,1\ns_lshl_b32 s98,s98,11\ns_add_u32 s99,s99,s98\nv_add_u32 v31,s99,v31\n"
"s_lshr_b32 s99,s100,4\ns_and_b32 s99,s99,15\ns_mul_i32 s99,s99,0x38000\ns_add_u32 s99,0x4800000,s99\ns_and_b32 s98,s100,15\ns_lshl_b32 s98,s98,12\n"
"s_add_u32 s99,s99,s98\ns_add_u32 s48,s4,s99\ns_addc_u32 s49,s5,0\ns_and_b32 s49,s49,0xffff\ns_and_b32 s50,s100,15\ns_lshl_b32 s50,s50,12\n"
"s_sub_u32 s50,0x38000,s50\ns_mov_b32 s51,0x20000\ns_mov_b32 s74,0x1c00\ns_mov_b32 s75,0x3800\ns_mov_b32 s76,0x5400\ns_mul_i32 s62,s3,0x1000\n"
"s_mov_b32 s77,0x8000\ns_mov_b32 s78,0x10000\ns_mov_b32 s79,0x18000\ns_mov_b32 s80,0x20000\ns_mov_b32 s81,0x28000\ns_mov_b32 s82,0x30000\n"
"s_mov_b32 s83,0x38000\ns_mul_i32 s63,s3,0x2000\ns_mov_b32 s84,0x7000\ns_mov_b32 s85,0xa800\ns_mul_i32 s64,s3,0x1000\nxam 62,0x10000\ns_nop 0\n"
"xdl 0,16,0,0\nxam 62,0x10400\ns_nop 0\nxdl 1,16,s74,0\nxam 62,0x10800\ns_nop 0\nxdl 0,16,s75,0\nxam 62,0x10c00\ns_nop 0\nxdl 1,16,s76,0\n"
"s_add_u32 s16,s16,0x80\ns_addc_u32 s17,s17,0\ns_sub_u32 s18,s18,0x80\nxam 63,0\ns_nop 0\nxdl 2,20,0,0\nxam 63,0x400\ns_nop 0\nxdl 2,20,s77,0\n"
"xam 63,0x800\ns_nop 0\nxdl 3,20,s78,0\nxam 63,0xc00\ns_nop 0\nxdl 3,20,s79,0\nxam 63,0x1000\ns_nop 0\nxdl 2,20,s80,0\nxam 63,0x1400\ns_nop 0\n"
"xdl 2,20,s81,0\nxam 63,0x1800\ns_nop 0\nxdl 3,20,s82,0\nxam 63,0x1c00\ns_nop 0\nxdl 3,20,s83,0\ns_add_u32 s20,s20,0x80000\ns_addc_u32 s21,s21,0\n"
"s_sub_u32 s22,s22,0x80000\nxam 64,0x1c000\ns_nop 0\nxdl 4,24,0,0\nxam 64,0x1c400\ns_nop 0\nxdl 4,24,s75,0\nxam 64,0x1c800\ns_nop 0\nxdl 5,24,s84,0\n"
"xam 64,0x1cc00\ns_nop 0\nxdl 5,24,s85,0\ns_add_u32 s24,s24,0x38000\ns_addc_u32 s25,s25,0\ns_sub_u32 s26,s26,0x38000\nxam 62,0x14000\ns_nop 0\n"
"xdl 0,16,0,0\nxam 62,0x14400\ns_nop 0\nxdl 1,16,s74,0\nxam 62,0x14800\ns_nop 0\nxdl 0,16,s75,0\nxam 62,0x14c00\ns_nop 0\nxdl 1,16,s76,0\n"
"s_add_u32 s16,s16,0x80\ns_addc_u32 s17,s17,0\ns_sub_u32 s18,s18,0x80\nxam 63,0x8000\ns_nop 0\nxdl 2,20,0,0\nxam 63,0x8400\ns_nop 0\nxdl 2,20,s77,0\n"
"xam 63,0x8800\ns_nop 0\nxdl 3,20,s78,0\nxam 63,0x8c00\ns_nop 0\nxdl 3,20,s79,0\nxam 63,0x9000\ns_nop 0\nxdl 2,20,s80,0\nxam 63,0x9400\ns_nop 0\n"
"xdl 2,20,s81,0\nxam 63,0x9800\ns_nop 0\nxdl 3,20,s82,0\nxam 63,0x9c00\ns_nop 0\nxdl 3,20,s83,0\ns_add_u32 s20,s20,0x80000\ns_addc_u32 s21,s21,0\n"
"s_sub_u32 s22,s22,0x80000\nxam 64,0x20000\ns_nop 0\nxdl 4,24,0,0\nxam 64,0x20400\ns_nop 0\nxdl 4,24,s75,0\nxam 64,0x20800\ns_nop 0\nxdl 5,24,s84,0\n"
"xam 64,0x20c00\ns_nop 0\nxdl 5,24,s85,0\ns_add_u32 s24,s24,0x38000\ns_addc_u32 s25,s25,0\ns_sub_u32 s26,s26,0x38000\nxam 62,0x18000\ns_nop 0\n"
"xdl 0,16,0,0\nxam 62,0x18400\ns_nop 0\nxdl 1,16,s74,0\nxam 62,0x18800\ns_nop 0\nxdl 0,16,s75,0\nxam 62,0x18c00\ns_nop 0\nxdl 1,16,s76,0\n"
"s_add_u32 s16,s16,0x80\ns_addc_u32 s17,s17,0\ns_sub_u32 s18,s18,0x80\nxam 64,0x24000\ns_nop 0\nxdl 4,24,0,0\nxam 64,0x24400\ns_nop 0\nxdl 4,24,s75,0\n"
"xam 64,0x24800\ns_nop 0\nxdl 5,24,s84,0\nxam 64,0x24c00\ns_nop 0\nxdl 5,24,s85,0\ns_add_u32 s24,s24,0x38000\ns_addc_u32 s25,s25,0\n"
"s_sub_u32 s26,s26,0x38000\nv_mov_b32 v174,0\nbuffer_load_dwordx2 v[172:173],v174,s[44:47],0 offen offset:0 sc1\ns_mov_b32 s39,0x20000\n"
"s_mov_b32 s36,s53\ns_mov_b32 s37,s54\ns_mov_b32 s38,0x4000\nbuffer_load_dword v171,v164,s[36:39],0 offen offset:0\ns_mov_b32 s36,s55\n"
"s_mov_b32 s37,s56\ns_mov_b32 s38,0x8000\nbuffer_load_dword v171,v164,s[36:39],0 offen offset:0\ns_mov_b32 s68,0x4000\n"
"buffer_load_dword v171,v164,s[36:39],s68 offen offset:0\ns_mov_b32 s36,s57\ns_mov_b32 s37,s58\ns_mov_b32 s38,0x8000\n"
"buffer_load_dword v171,v164,s[36:39],0 offen offset:0\ns_mov_b32 s68,0x4000\nbuffer_load_dword v171,v164,s[36:39],s68 offen offset:0\n"
"s_mov_b32 s86,0x4000\ns_cmp_eq_u32 s52,0\ns_cselect_b32 s86,s86,0x8000\ns_mov_b32 s87,0x8000\ns_cmp_eq_u32 s52,0\ns_cselect_b32 s87,s87,0x4000\n"
"s_mov_b32 s88,0xc000\ns_mov_b32 s89,0x10000\ns_mov_b32 s90,0x14000\ns_cmp_eq_u32 s52,0\ns_cselect_b32 s90,s90,0x18000\ns_mov_b32 s91,0x18000\n"
"s_cmp_eq_u32 s52,0\ns_cselect_b32 s91,s91,0x14000\ns_mov_b32 s92,0x1c000\ns_mul_i32 s65,s3,0x2000\ns_mov_b32 s93,0x8000\ns_mov_b32 s94,0x10000\n"
"s_mov_b32 s95,0x18000\ns_mov_b32 s96,0x20000\ns_mov_b32 s97,0x28000\ns_mov_b32 s98,0x30000\ns_mov_b32 s99,0x38000\ns_mul_i32 s66,s3,0x2000\n"
"s_waitcnt vmcnt(30)\ns_barrier\nxrt 48,8,0\nxrt 50,8,1024\nxrt 52,9,0\nxrt 54,9,1024\nxrt 56,10,0\nxrt 58,10,1024\nxrt 60,11,0\nxrt 62,11,1024\n"
"xrt 64,12,0\nxrt 66,12,1024\nxrt 68,13,0\nxrt 70,13,1024\nxrt 72,14,0\nxrt 74,14,1024\nxrt 76,15,0\nxrt 78,15,1024\nxrt 80,16,0\nxrt 82,16,1024\n"
"xrt 84,17,0\nxrt 86,17,1024\nxrt 88,18,0\nxrt 90,18,1024\nxrt 92,19,0\nxrt 94,19,1024\nxrb 32,6,0\nxrb 36,6,2048\nxrb 40,6,4096\nxrb 44,6,6144\n"
"xwl 3\nxm16z 0,48,32\nxrt 112,8,8192\nxm16z 4,52,32\nxrt 114,8,9216\nxm16z 8,56,32\nxrt 116,9,8192\nxm16z 12,60,32\nxrt 118,9,9216\nxm16z 16,64,32\n"
"xm16z 20,68,32\nxrt 120,10,8192\nxm16z 24,72,32\nxrt 122,10,9216\nxm16z 28,76,32\nxrt 124,11,8192\nxm16z 32,80,32\nxm16z 36,84,32\nxrt 126,11,9216\n"
"xm16z 40,88,32\nxrt 128,12,8192\nxm16z 44,92,32\nxrt 130,12,9216\nxwl 12\nxm16z 48,48,36\nxrt 132,13,8192\nxm16z 52,52,36\nxm16z 56,56,36\n"
"xrt 134,13,9216\nxm16z 60,60,36\nxrt 136,14,8192\nxm16z 64,64,36\nxrt 138,14,9216\nxm16z 68,68,36\nxm16z 72,72,36\nxrt 140,15,8192\nxm16z 76,76,36\n"
"xrt 142,15,9216\nxm16z 80,80,36\nxrt 144,16,8192\nxm16z 84,84,36\nxrt 146,16,9216\nxm16z 88,88,36\nxm16z 92,92,36\nxrt 148,17,8192\nxwl 15\n"
"xm16z 96,48,40\nxrt 150,17,9216\nxm16z 100,52,40\nxrt 152,18,8192\nxm16z 104,56,40\nxm16z 108,60,40\nxrt 154,18,9216\nxm16z 112,64,40\n"
"xrt 156,19,8192\nxm16z 116,68,40\nxrt 158,19,9216\nxm16z 120,72,40\nxrb 96,7,0\nxm16z 124,76,40\nxm16z 128,80,40\nxrb 100,7,2048\nxm16z 132,84,40\n"
"xrb 104,7,4096\nxm16z 136,88,40\nxrb 108,7,6144\nxm16z 140,92,40\nxm16z 144,48,44\nxm16z 148,52,44\nxm16z 152,56,44\nxm16z 156,60,44\n"
"xm16z 160,64,44\nxm16z 164,68,44\nxm16z 168,72,44\nxm16z 172,76,44\nxm16z 176,80,44\nxm16z 180,84,44\nxm16z 184,88,44\nxm16z 188,92,44\n"
"s_waitcnt vmcnt(14) lgkmcnt(0)\ns_barrier\nxam 63,0\nxm16 0,112,96\nxdl 2,20,0,0\nxrt 48,8,32768\nxm16 4,116,96\nxrt 50,8,33792\nxm16 8,120,96\n"
"xrt 52,9,32768\nxm16 12,124,96\nxrt 54,9,33792\nxm16 16,128,96\nxam 63,0x400\nxm16 20,132,96\nxdl 2,20,s77,0\nxrt 56,10,32768\nxm16 24,136,96\n"
"xrt 58,10,33792\nxm16 28,140,96\nxrt 60,11,32768\nxm16 32,144,96\nxm16 36,148,96\nxrt 62,11,33792\nxam 63,0x800\nxm16 40,152,96\nxdl 3,20,s78,0\n"
"xrt 64,12,32768\nxm16 44,156,96\nxrt 66,12,33792\nxm16 48,112,100\nxrt 68,13,32768\nxm16 52,116,100\nxm16 56,120,100\nxrt 70,13,33792\n"
"xm16 60,124,100\nxrt 72,14,32768\nxam 63,0xc00\nxm16 64,128,100\nxdl 3,20,s79,0\nxrt 74,14,33792\nxm16 68,132,100\nxm16 72,136,100\nxrt 76,15,32768\n"
"xm16 76,140,100\nxrt 78,15,33792\nxm16 80,144,100\nxrt 80,16,16384\nxam 63,0x1000\nxm16 84,148,100\nxdl 2,20,s80,0\nxrt 82,16,17408\nxm16 88,152,100\n"
"xm16 92,156,100\nxrt 84,17,16384\nxm16 96,112,104\nxrt 86,17,17408\nxm16 100,116,104\nxrt 88,18,16384\nxam 63,0x1400\nxm16 104,120,104\n"
"xdl 2,20,s81,0\nxm16 108,124,104\nxrt 90,18,17408\nxm16 112,128,104\nxrt 92,19,16384\nxm16 116,132,104\nxrt 94,19,17408\nxm16 120,136,104\n"
"xrb 32,6,16384\nxm16 124,140,104\nxam 63,0x1800\nxm16 128,144,104\nxdl 3,20,s82,0\nxrb 36,6,18432\nxm16 132,148,104\nxrb 40,6,20480\n"
"xm16 136,152,104\nxrb 44,6,22528\nxm16 140,156,104\nxm16 144,112,108\nxam 63,0x1c00\nxm16 148,116,108\nxdl 3,20,s83,0\ns_add_u32 s20,s20,0x80000\n"
"s_addc_u32 s21,s21,0\ns_sub_u32 s22,s22,0x80000\nxm16 152,120,108\nxm16 156,124,108\nxm16 160,128,108\nxm16 164,132,108\nxm16 168,136,108\n"
"xm16 172,140,108\nxm16 176,144,108\nxm16 180,148,108\nxm16 184,152,108\nxm16 188,156,108\nxam 62,0x10000\nxwl 3\nxm16 0,48,32\nxdl 0,16,0,0\n"
"xrt 112,8,40960\nxm16 4,52,32\nxrt 114,8,41984\nxm16 8,56,32\nxrt 116,9,40960\nxm16 12,60,32\nxrt 118,9,41984\nxm16 16,64,32\nxam 62,0x10400\n"
"xm16 20,68,32\nxdl 1,16,s74,0\nxrt 120,10,40960\nxm16 24,72,32\nxrt 122,10,41984\nxm16 28,76,32\nxrt 124,11,40960\nxm16 32,80,32\nxm16 36,84,32\n"
"xrt 126,11,41984\nxam 62,0x10800\nxm16 40,88,32\nxdl 0,16,s75,0\nxrt 128,12,40960\nxm16 44,92,32\nxrt 130,12,41984\nxwl 12\nxm16 48,48,36\n"
"xrt 132,13,40960\nxm16 52,52,36\nxm16 56,56,36\nxrt 134,13,41984\nxam 62,0x10c00\nxm16 60,60,36\nxdl 1,16,s76,0\ns_add_u32 s16,s16,0x80\n"
"s_addc_u32 s17,s17,0\ns_sub_u32 s18,s18,0x80\nxrt 136,14,40960\nxm16 64,64,36\nxrt 138,14,41984\nxm16 68,68,36\nxm16 72,72,36\nxrt 140,15,40960\n"
"xm16 76,76,36\nxrt 142,15,41984\nxm16 80,80,36\nxrt 144,16,24576\nxam 64,0x1c000\nxm16 84,84,36\nxdl 4,24,0,0\nxrt 146,16,25600\nxm16 88,88,36\n"
"xm16 92,92,36\nxrt 148,17,24576\nxwl 15\nxm16 96,48,40\nxrt 150,17,25600\nxm16 100,52,40\nxrt 152,18,24576\nxam 64,0x1c400\nxm16 104,56,40\n"
"xdl 4,24,s75,0\nxm16 108,60,40\nxrt 154,18,25600\nxm16 112,64,40\nxrt 156,19,24576\nxm16 116,68,40\nxrt 158,19,25600\nxm16 120,72,40\nxrb 96,7,16384\n"
"xam 64,0x1c800\nxm16 124,76,40\nxdl 5,24,s84,0\nxm16 128,80,40\nxrb 100,7,18432\nxm16 132,84,40\nxrb 104,7,20480\nxm16 136,88,40\nxrb 108,7,22528\n"
"xm16 140,92,40\nxam 64,0x1cc00\nxm16 144,48,44\nxdl 5,24,s85,0\ns_add_u32 s24,s24,0x38000\ns_addc_u32 s25,s25,0\ns_sub_u32 s26,s26,0x38000\n"
"xm16 148,52,44\nxm16 152,56,44\nxm16 156,60,44\nxm16 160,64,44\nxm16 164,68,44\nxm16 168,72,44\nxm16 172,76,44\nxm16 176,80,44\nxm16 180,84,44\n"
"xm16 184,88,44\nxm16 188,92,44\ns_waitcnt vmcnt(8) lgkmcnt(0)\ns_barrier\nxam 63,0x8000\nxm16 0,112,96\nxdl 2,20,0,0\nxrt 48,8,0\nxm16 4,116,96\n"
"xrt 50,8,1024\nxm16 8,120,96\nxrt 52,9,0\nxm16 12,124,96\nxrt 54,9,1024\nxm16 16,128,96\nxam 63,0x8400\nxm16 20,132,96\nxdl 2,20,s77,0\nxrt 56,10,0\n"
"xm16 24,136,96\nxrt 58,10,1024\nxm16 28,140,96\nxrt 60,11,0\nxm16 32,144,96\nxm16 36,148,96\nxrt 62,11,1024\nxam 63,0x8800\nxm16 40,152,96\n"
"xdl 3,20,s78,0\nxrt 64,12,0\nxm16 44,156,96\nxrt 66,12,1024\nxm16 48,112,100\nxrt 68,13,0\nxm16 52,116,100\nxm16 56,120,100\nxrt 70,13,1024\n"
"xm16 60,124,100\nxrt 72,14,0\nxam 63,0x8c00\nxm16 64,128,100\nxdl 3,20,s79,0\nxrt 74,14,1024\nxm16 68,132,100\nxm16 72,136,100\nxrt 76,15,0\n"
"xm16 76,140,100\nxrt 78,15,1024\nxm16 80,144,100\nxrt 80,16,32768\nxam 63,0x9000\nxm16 84,148,100\nxdl 2,20,s80,0\nxrt 82,16,33792\nxm16 88,152,100\n"
"xm16 92,156,100\nxrt 84,17,32768\nxm16 96,112,104\nxrt 86,17,33792\nxm16 100,116,104\nxrt 88,18,32768\nxam 63,0x9400\nxm16 104,120,104\n"
"xdl 2,20,s81,0\nxm16 108,124,104\nxrt 90,18,33792\nxm16 112,128,104\nxrt 92,19,32768\nxm16 116,132,104\nxrt 94,19,33792\nxm16 120,136,104\n"
"xrb 32,6,32768\nxm16 124,140,104\nxam 63,0x9800\nxm16 128,144,104\nxdl 3,20,s82,0\nxrb 36,6,34816\nxm16 132,148,104\nxrb 40,6,36864\n"
"xm16 136,152,104\nxrb 44,6,38912\nxm16 140,156,104\nxm16 144,112,108\nxam 63,0x9c00\nxm16 148,116,108\nxdl 3,20,s83,0\ns_add_u32 s20,s20,0x80000\n"
"s_addc_u32 s21,s21,0\ns_sub_u32 s22,s22,0x80000\nxm16 152,120,108\nxm16 156,124,108\nxm16 160,128,108\nxm16 164,132,108\nxm16 168,136,108\n"
"xm16 172,140,108\nxm16 176,144,108\nxm16 180,148,108\nxm16 184,152,108\nxm16 188,156,108\nxam 62,0x14000\nxwl 3\nxm16 0,48,32\nxdl 0,16,0,0\n"
"xrt 112,8,8192\nxm16 4,52,32\nxrt 114,8,9216\nxm16 8,56,32\nxrt 116,9,8192\nxm16 12,60,32\nxrt 118,9,9216\nxm16 16,64,32\nxam 62,0x14400\n"
"xm16 20,68,32\nxdl 1,16,s74,0\nxrt 120,10,8192\nxm16 24,72,32\nxrt 122,10,9216\nxm16 28,76,32\nxrt 124,11,8192\nxm16 32,80,32\nxm16 36,84,32\n"
"xrt 126,11,9216\nxam 62,0x14800\nxm16 40,88,32\nxdl 0,16,s75,0\nxrt 128,12,8192\nxm16 44,92,32\nxrt 130,12,9216\nxwl 12\nxm16 48,48,36\n"
"xrt 132,13,8192\nxm16 52,52,36\nxm16 56,56,36\nxrt 134,13,9216\nxam 62,0x14c00\nxm16 60,60,36\nxdl 1,16,s76,0\ns_add_u32 s16,s16,0x80\n"
"s_addc_u32 s17,s17,0\ns_sub_u32 s18,s18,0x80\nxrt 136,14,8192\nxm16 64,64,36\nxrt 138,14,9216\nxm16 68,68,36\nxm16 72,72,36\nxrt 140,15,8192\n"
"xm16 76,76,36\nxrt 142,15,9216\nxm16 80,80,36\nxrt 144,16,40960\nxam 64,0x20000\nxm16 84,84,36\nxdl 4,24,0,0\nxrt 146,16,41984\nxm16 88,88,36\n"
"xm16 92,92,36\nxrt 148,17,40960\nxwl 15\nxm16 96,48,40\nxrt 150,17,41984\nxm16 100,52,40\nxrt 152,18,40960\nxam 64,0x20400\nxm16 104,56,40\n"
"xdl 4,24,s75,0\nxm16 108,60,40\nxrt 154,18,41984\nxm16 112,64,40\nxrt 156,19,40960\nxm16 116,68,40\nxrt 158,19,41984\nxm16 120,72,40\nxrb 96,7,32768\n"
"xam 64,0x20800\nxm16 124,76,40\nxdl 5,24,s84,0\nxm16 128,80,40\nxrb 100,7,34816\nxm16 132,84,40\nxrb 104,7,36864\nxm16 136,88,40\nxrb 108,7,38912\n"
"xm16 140,92,40\nxam 64,0x20c00\nxm16 144,48,44\nxdl 5,24,s85,0\ns_add_u32 s24,s24,0x38000\ns_addc_u32 s25,s25,0\ns_sub_u32 s26,s26,0x38000\n"
"xm16 148,52,44\nxm16 152,56,44\nxm16 156,60,44\nxm16 160,64,44\nxm16 164,68,44\nxm16 168,72,44\nxm16 172,76,44\nxm16 176,80,44\nxm16 180,84,44\n"
"xm16 184,88,44\nxm16 188,92,44\ns_waitcnt vmcnt(8) lgkmcnt(0)\ns_barrier\nxam 63,0\nxm16 0,112,96\nxdl 2,20,0,0\nxrt 48,8,32768\nxm16 4,116,96\n"
"xrt 50,8,33792\nxm16 8,120,96\nxrt 52,9,32768\nxm16 12,124,96\nxrt 54,9,33792\nxm16 16,128,96\nxam 63,0x400\nxm16 20,132,96\nxdl 2,20,s77,0\n"
"xrt 56,10,32768\nxm16 24,136,96\nxrt 58,10,33792\nxm16 28,140,96\nxrt 60,11,32768\nxm16 32,144,96\nxm16 36,148,96\nxrt 62,11,33792\nxam 63,0x800\n"
"xm16 40,152,96\nxdl 3,20,s78,0\nxrt 64,12,32768\nxm16 44,156,96\nxrt 66,12,33792\nxm16 48,112,100\nxrt 68,13,32768\nxm16 52,116,100\nxm16 56,120,100\n"
"xrt 70,13,33792\nxm16 60,124,100\nxrt 72,14,32768\nxam 63,0xc00\nxm16 64,128,100\nxdl 3,20,s79,0\nxrt 74,14,33792\nxm16 68,132,100\nxm16 72,136,100\n"
"xrt 76,15,32768\nxm16 76,140,100\nxrt 78,15,33792\nxm16 80,144,100\nxrt 80,16,0\nxam 63,0x1000\nxm16 84,148,100\nxdl 2,20,s80,0\nxrt 82,16,1024\n"
"xm16 88,152,100\nxm16 92,156,100\nxrt 84,17,0\nxm16 96,112,104\nxrt 86,17,1024\nxm16 100,116,104\nxrt 88,18,0\nxam 63,0x1400\nxm16 104,120,104\n"
"xdl 2,20,s81,0\nxm16 108,124,104\nxrt 90,18,1024\nxm16 112,128,104\nxrt 92,19,0\nxm16 116,132,104\nxrt 94,19,1024\nxm16 120,136,104\nxrb 32,6,0\n"
"xm16 124,140,104\nxam 63,0x1800\nxm16 128,144,104\nxdl 3,20,s82,0\nxrb 36,6,2048\nxm16 132,148,104\nxrb 40,6,4096\nxm16 136,152,104\nxrb 44,6,6144\n"
"xm16 140,156,104\nxm16 144,112,108\nxam 63,0x1c00\nxm16 148,116,108\nxdl 3,20,s83,0\ns_add_u32 s20,s20,0x80000\ns_addc_u32 s21,s21,0\n"
"s_sub_u32 s22,s22,0x80000\nxm16 152,120,108\nxm16 156,124,108\nxm16 160,128,108\nxm16 164,132,108\nxm16 168,136,108\nxm16 172,140,108\n"
"xm16 176,144,108\nxm16 180,148,108\nxm16 184,152,108\nxm16 188,156,108\nxam 62,0x18000\nxwl 3\nxm16 0,48,32\nxdl 0,16,0,0\nxrt 112,8,40960\n"
"xm16 4,52,32\nxrt 114,8,41984\nxm16 8,56,32\nxrt 116,9,40960\nxm16 12,60,32\nxrt 118,9,41984\nxm16 16,64,32\nxam 62,0x18400\nxm16 20,68,32\n"
"xdl 1,16,s74,0\nxrt 120,10,40960\nxm16 24,72,32\nxrt 122,10,41984\nxm16 28,76,32\nxrt 124,11,40960\nxm16 32,80,32\nxm16 36,84,32\nxrt 126,11,41984\n"
"xam 62,0x18800\nxm16 40,88,32\nxdl 0,16,s75,0\nxrt 128,12,40960\nxm16 44,92,32\nxrt 130,12,41984\nxwl 12\nxm16 48,48,36\nxrt 132,13,40960\n"
"xm16 52,52,36\nxm16 56,56,36\nxrt 134,13,41984\nxam 62,0x18c00\nxm16 60,60,36\nxdl 1,16,s76,0\ns_add_u32 s16,s16,0x80\ns_addc_u32 s17,s17,0\n"
"s_sub_u32 s18,s18,0x80\nxrt 136,14,40960\nxm16 64,64,36\nxrt 138,14,41984\nxm16 68,68,36\nxm16 72,72,36\nxrt 140,15,40960\nxm16 76,76,36\n"
"xrt 142,15,41984\nxm16 80,80,36\nxrt 144,16,8192\nxam 64,0x24000\nxm16 84,84,36\nxdl 4,24,0,0\nxrt 146,16,9216\nxm16 88,88,36\nxm16 92,92,36\n"
"xrt 148,17,8192\nxwl 15\nxm16 96,48,40\nxrt 150,17,9216\nxm16 100,52,40\nxrt 152,18,8192\nxam 64,0x24400\nxm16 104,56,40\nxdl 4,24,s75,0\n"
"xm16 108,60,40\nxrt 154,18,9216\nxm16 112,64,40\nxrt 156,19,8192\nxm16 116,68,40\nxrt 158,19,9216\nxm16 120,72,40\nxrb 96,7,0\nxam 64,0x24800\n"
"xm16 124,76,40\nxdl 5,24,s84,0\nxm16 128,80,40\nxrb 100,7,2048\nxm16 132,84,40\nxrb 104,7,4096\nxm16 136,88,40\nxrb 108,7,6144\nxm16 140,92,40\n"
"xam 64,0x24c00\nxm16 144,48,44\nxdl 5,24,s85,0\ns_add_u32 s24,s24,0x38000\ns_addc_u32 s25,s25,0\ns_sub_u32 s26,s26,0x38000\nxm16 148,52,44\n"
"xm16 152,56,44\nxm16 156,60,44\nxm16 160,64,44\nxm16 164,68,44\nxm16 168,72,44\nxm16 172,76,44\nxm16 176,80,44\nxm16 180,84,44\nxm16 184,88,44\n"
"xm16 188,92,44\ns_waitcnt vmcnt(8) lgkmcnt(0)\ns_barrier\nxam 63,0x8000\nxm16 0,112,96\nxdl 2,20,0,0\nxrt 48,8,0\nxm16 4,116,96\nxrt 50,8,1024\n"
"xm16 8,120,96\nxrt 52,9,0\nxm16 12,124,96\nxrt 54,9,1024\nxm16 16,128,96\nxam 63,0x8400\nxm16 20,132,96\nxdl 2,20,s77,0\nxrt 56,10,0\nxm16 24,136,96\n"
"xrt 58,10,1024\nxm16 28,140,96\nxrt 60,11,0\nxm16 32,144,96\nxm16 36,148,96\nxrt 62,11,1024\nxam 63,0x8800\nxm16 40,152,96\nxdl 3,20,s78,0\n"
"xrt 64,12,0\nxm16 44,156,96\nxrt 66,12,1024\nxm16 48,112,100\nxrt 68,13,0\nxm16 52,116,100\nxm16 56,120,100\nxrt 70,13,1024\nxm16 60,124,100\n"
"xrt 72,14,0\nxam 63,0x8c00\nxm16 64,128,100\nxdl 3,20,s79,0\nxrt 74,14,1024\nxm16 68,132,100\nxm16 72,136,100\nxrt 76,15,0\nxm16 76,140,100\n"
"xrt 78,15,1024\nxm16 80,144,100\nxrt 80,16,16384\nxam 63,0x9000\nxm16 84,148,100\nxdl 2,20,s80,0\nxrt 82,16,17408\nxm16 88,152,100\nxm16 92,156,100\n"
"xrt 84,17,16384\nxm16 96,112,104\nxrt 86,17,17408\nxm16 100,116,104\nxrt 88,18,16384\nxam 63,0x9400\nxm16 104,120,104\nxdl 2,20,s81,0\n"
"xm16 108,124,104\nxrt 90,18,17408\nxm16 112,128,104\nxrt 92,19,16384\nxm16 116,132,104\nxrt 94,19,17408\nxm16 120,136,104\nxrb 32,6,16384\n"
"xm16 124,140,104\nxam 63,0x9800\nxm16 128,144,104\nxdl 3,20,s82,0\nxrb 36,6,18432\nxm16 132,148,104\nxrb 40,6,20480\nxm16 136,152,104\n"
"xrb 44,6,22528\nxm16 140,156,104\nxm16 144,112,108\nxam 63,0x9c00\nxm16 148,116,108\nxdl 3,20,s83,0\ns_add_u32 s20,s20,0x80000\ns_addc_u32 s21,s21,0\n"
"s_sub_u32 s22,s22,0x80000\nxm16 152,120,108\nxm16 156,124,108\nxm16 160,128,108\nxm16 164,132,108\nxm16 168,136,108\nxm16 172,140,108\n"
"xm16 176,144,108\nxm16 180,148,108\nxm16 184,152,108\nxm16 188,156,108\nxam 62,0x10000\nxwl 3\nxm16 0,48,32\nxdl 0,16,0,0\nxrt 112,8,8192\n"
"xm16 4,52,32\nxrt 114,8,9216\nxm16 8,56,32\nxrt 116,9,8192\nxm16 12,60,32\nxrt 118,9,9216\nxm16 16,64,32\nxam 62,0x10400\nxm16 20,68,32\n"
"xdl 1,16,s74,0\nxrt 120,10,8192\nxm16 24,72,32\nxrt 122,10,9216\nxm16 28,76,32\nxrt 124,11,8192\nxm16 32,80,32\nxm16 36,84,32\nxrt 126,11,9216\n"
"xam 62,0x10800\nxm16 40,88,32\nxdl 0,16,s75,0\nxrt 128,12,8192\nxm16 44,92,32\nxrt 130,12,9216\nxwl 12\nxm16 48,48,36\nxrt 132,13,8192\n"
"xm16 52,52,36\nxm16 56,56,36\nxrt 134,13,9216\nxam 62,0x10c00\nxm16 60,60,36\nxdl 1,16,s76,0\ns_add_u32 s16,s16,0x80\ns_addc_u32 s17,s17,0\n"
"s_sub_u32 s18,s18,0x80\nxrt 136,14,8192\nxm16 64,64,36\nxrt 138,14,9216\nxm16 68,68,36\nxm16 72,72,36\nxrt 140,15,8192\nxm16 76,76,36\n"
"xrt 142,15,9216\nxm16 80,80,36\nxrt 144,16,24576\nxam 64,0x1c000\nxm16 84,84,36\nxdl 4,24,0,0\nxrt 146,16,25600\nxm16 88,88,36\nxm16 92,92,36\n"
"xrt 148,17,24576\nxwl 15\nxm16 96,48,40\nxrt 150,17,25600\nxm16 100,52,40\nxrt 152,18,24576\nxam 64,0x1c400\nxm16 104,56,40\nxdl 4,24,s75,0\n"
"xm16 108,60,40\nxrt 154,18,25600\nxm16 112,64,40\nxrt 156,19,24576\nxm16 116,68,40\nxrt 158,19,25600\nxm16 120,72,40\nxrb 96,7,16384\nxam 64,0x1c800\n"
"xm16 124,76,40\nxdl 5,24,s84,0\nxm16 128,80,40\nxrb 100,7,18432\nxm16 132,84,40\nxrb 104,7,20480\nxm16 136,88,40\nxrb 108,7,22528\nxm16 140,92,40\n"
"xam 64,0x1cc00\nxm16 144,48,44\nxdl 5,24,s85,0\ns_add_u32 s24,s24,0x38000\ns_addc_u32 s25,s25,0\ns_sub_u32 s26,s26,0x38000\nxm16 148,52,44\n"
"xm16 152,56,44\nxm16 156,60,44\nxm16 160,64,44\nxm16 164,68,44\nxm16 168,72,44\nxm16 172,76,44\nxm16 176,80,44\nxm16 180,84,44\nxm16 184,88,44\n"
"xm16 188,92,44\ns_waitcnt vmcnt(8) lgkmcnt(0)\ns_barrier\nxam 63,0\nxm16 0,112,96\nxdl 2,20,0,0\nxrt 48,8,32768\nxm16 4,116,96\nxrt 50,8,33792\n"
"xm16 8,120,96\nxrt 52,9,32768\nxm16 12,124,96\nxrt 54,9,33792\nxm16 16,128,96\nxam 63,0x400\nxm16 20,132,96\nxdl 2,20,s77,0\nxrt 56,10,32768\n"
"xm16 24,136,96\nxrt 58,10,33792\nxm16 28,140,96\nxrt 60,11,32768\nxm16 32,144,96\nxm16 36,148,96\nxrt 62,11,33792\nxam 63,0x800\nxm16 40,152,96\n"
"xdl 3,20,s78,0\nxrt 64,12,32768\nxm16 44,156,96\nxrt 66,12,33792\nxm16 48,112,100\nxrt 68,13,32768\nxm16 52,116,100\nxm16 56,120,100\n"
"xrt 70,13,33792\nxm16 60,124,100\nxrt 72,14,32768\nxam 63,0xc00\nxm16 64,128,100\nxdl 3,20,s79,0\nxrt 74,14,33792\nxm16 68,132,100\nxm16 72,136,100\n"
"xrt 76,15,32768\nxm16 76,140,100\nxrt 78,15,33792\nxm16 80,144,100\nxrt 80,16,32768\nxam 63,0x1000\nxm16 84,148,100\nxdl 2,20,s80,0\nxrt 82,16,33792\n"
"xm16 88,152,100\nxm16 92,156,100\nxrt 84,17,32768\nxm16 96,112,104\nxrt 86,17,33792\nxm16 100,116,104\nxrt 88,18,32768\nxam 63,0x1400\n"
"xm16 104,120,104\nxdl 2,20,s81,0\nxm16 108,124,104\nxrt 90,18,33792\nxm16 112,128,104\nxrt 92,19,32768\nxm16 116,132,104\nxrt 94,19,33792\n"
"xm16 120,136,104\nxrb 32,6,32768\nxm16 124,140,104\nxam 63,0x1800\nxm16 128,144,104\nxdl 3,20,s82,0\nxrb 36,6,34816\nxm16 132,148,104\n"
"xrb 40,6,36864\nxm16 136,152,104\nxrb 44,6,38912\nxm16 140,156,104\nxm16 144,112,108\nxam 63,0x1c00\nxm16 148,116,108\nxdl 3,20,s83,0\n"
"s_add_u32 s20,s20,0x80000\ns_addc_u32 s21,s21,0\ns_sub_u32 s22,s22,0x80000\nxm16 152,120,108\nxm16 156,124,108\nxm16 160,128,108\nxm16 164,132,108\n"
"xm16 168,136,108\nxm16 172,140,108\nxm16 176,144,108\nxm16 180,148,108\nxm16 184,152,108\nxm16 188,156,108\nxwl 3\nxm16 0,48,32\nxrt 112,8,40960\n"
"xm16 4,52,32\nxrt 114,8,41984\nxm16 8,56,32\nxrt 116,9,40960\nxm16 12,60,32\nxrt 118,9,41984\nxm16 16,64,32\nxm16 20,68,32\nxrt 120,10,40960\n"
"xm16 24,72,32\nxrt 122,10,41984\nxm16 28,76,32\nxrt 124,11,40960\nxm16 32,80,32\nxm16 36,84,32\nxrt 126,11,41984\nxm16 40,88,32\nxrt 128,12,40960\n"
"xm16 44,92,32\nxrt 130,12,41984\nxwl 12\nxm16 48,48,36\nxrt 132,13,40960\nxm16 52,52,36\nxm16 56,56,36\nxrt 134,13,41984\nxm16 60,60,36\n"
"xrt 136,14,40960\nxm16 64,64,36\nxrt 138,14,41984\nxm16 68,68,36\nxm16 72,72,36\nxrt 140,15,40960\nxm16 76,76,36\nxrt 142,15,41984\nxm16 80,80,36\n"
"xrt 144,16,40960\nxm16 84,84,36\nxrt 146,16,41984\nxm16 88,88,36\nxm16 92,92,36\nxrt 148,17,40960\nxwl 15\nxm16 96,48,40\nxrt 150,17,41984\n"
"xm16 100,52,40\nxrt 152,18,40960\nxm16 104,56,40\nxm16 108,60,40\nxrt 154,18,41984\nxm16 112,64,40\nxrt 156,19,40960\nxm16 116,68,40\n"
"xrt 158,19,41984\nxm16 120,72,40\nxrb 96,7,32768\nxm16 124,76,40\nxm16 128,80,40\nxrb 100,7,34816\nxm16 132,84,40\nxrb 104,7,36864\nxm16 136,88,40\n"
"xrb 108,7,38912\nxm16 140,92,40\nxm16 144,48,44\nxm16 148,52,44\nxm16 152,56,44\nxm16 156,60,44\nxm16 160,64,44\nxm16 164,68,44\nxm16 168,72,44\n"
"xm16 172,76,44\nxm16 176,80,44\nxm16 180,84,44\nxm16 184,88,44\nxm16 188,92,44\ns_waitcnt vmcnt(0) lgkmcnt(0)\ns_barrier\nxm16 0,112,96\nxrt 48,8,0\n"
"xm16 4,116,96\nxrt 50,8,1024\nxm16 8,120,96\nxrt 52,9,0\nxm16 12,124,96\nxrt 54,9,1024\nxm16 16,128,96\nxm16 20,132,96\nxrt 56,10,0\nxm16 24,136,96\n"
"xrt 58,10,1024\nxm16 28,140,96\nxrt 60,11,0\nxm16 32,144,96\nxm16 36,148,96\nxrt 62,11,1024\nxm16 40,152,96\nxrt 64,12,0\nxm16 44,156,96\n"
"xrt 66,12,1024\nxm16 48,112,100\nxrt 68,13,0\nxm16 52,116,100\nxm16 56,120,100\nxrt 70,13,1024\nxm16 60,124,100\nxrt 72,14,0\nxm16 64,128,100\n"
"xrt 74,14,1024\nxm16 68,132,100\nxm16 72,136,100\nxrt 76,15,0\nxm16 76,140,100\nxrt 78,15,1024\nxm16 80,144,100\nxrt 80,16,0\nxm16 84,148,100\n"
"xrt 82,16,1024\nxm16 88,152,100\nxm16 92,156,100\nxrt 84,17,0\nxm16 96,112,104\nxrt 86,17,1024\nxm16 100,116,104\nxrt 88,18,0\nxm16 104,120,104\n"
"xm16 108,124,104\nxrt 90,18,1024\nxm16 112,128,104\nxrt 92,19,0\nxm16 116,132,104\nxrt 94,19,1024\nxm16 120,136,104\nxrb 32,6,0\nxm16 124,140,104\n"
"xm16 128,144,104\nxrb 36,6,2048\nxm16 132,148,104\nxrb 40,6,4096\nxm16 136,152,104\nxrb 44,6,6144\nxm16 140,156,104\nxm16 144,112,108\n"
"xm16 148,116,108\nxm16 152,120,108\nxm16 156,124,108\nxm16 160,128,108\nxm16 164,132,108\nxm16 168,136,108\nxm16 172,140,108\nxm16 176,144,108\n"
"xm16 180,148,108\nxm16 184,152,108\nxm16 188,156,108\nxwl 3\nxm16 0,48,32\nxrt 112,8,8192\nxm16 4,52,32\nxrt 114,8,9216\nxm16 8,56,32\n"
"xrt 116,9,8192\nxm16 12,60,32\nxrt 118,9,9216\nxm16 16,64,32\nxm16 20,68,32\nxrt 120,10,8192\nxm16 24,72,32\nxrt 122,10,9216\nxm16 28,76,32\n"
"xrt 124,11,8192\nxm16 32,80,32\nxm16 36,84,32\nxrt 126,11,9216\nxm16 40,88,32\nxrt 128,12,8192\nxm16 44,92,32\nxrt 130,12,9216\nxwl 12\n"
"xm16 48,48,36\nxrt 132,13,8192\nxm16 52,52,36\nxm16 56,56,36\nxrt 134,13,9216\nxm16 60,60,36\nxrt 136,14,8192\nxm16 64,64,36\nxrt 138,14,9216\n"
"xm16 68,68,36\nxm16 72,72,36\nxrt 140,15,8192\nxm16 76,76,36\nxrt 142,15,9216\nxm16 80,80,36\nxrt 144,16,8192\nxm16 84,84,36\nxrt 146,16,9216\n"
"xm16 88,88,36\nxm16 92,92,36\nxrt 148,17,8192\nxwl 15\nxm16 96,48,40\nxrt 150,17,9216\nxm16 100,52,40\nxrt 152,18,8192\nxm16 104,56,40\n"
"xm16 108,60,40\nxrt 154,18,9216\nxm16 112,64,40\nxrt 156,19,8192\nxm16 116,68,40\nxrt 158,19,9216\nxm16 120,72,40\nxrb 96,7,0\nxm16 124,76,40\n"
"xm16 128,80,40\nxrb 100,7,2048\nxm16 132,84,40\nxrb 104,7,4096\nxm16 136,88,40\nxrb 108,7,6144\nxm16 140,92,40\nxm16 144,48,44\nxm16 148,52,44\n"
"xm16 152,56,44\nxm16 156,60,44\nxm16 160,64,44\nxm16 164,68,44\nxm16 168,72,44\nxm16 172,76,44\nxm16 176,80,44\nxm16 180,84,44\nxm16 184,88,44\n"
"xm16 188,92,44\nxwl 0\ns_barrier\nxam 65,0x18000\nxm16 0,112,96\nxdl 160,28,0,0\nxam 65,0x18400\nxm16 4,116,96\nxdl 161,28,s86,0\nxam 65,0x18800\n"
"xm16 8,120,96\nxdl 160,28,s87,0\nxam 65,0x18c00\nxm16 12,124,96\nxdl 161,28,s88,0\nxam 65,0x19000\nxm16 16,128,96\nxdl 160,28,s89,0\nxm16 20,132,96\n"
"xam 65,0x19400\nxm16 24,136,96\nxdl 161,28,s90,0\nxam 65,0x19800\nxm16 28,140,96\nxdl 160,28,s91,0\nxam 65,0x19c00\nxm16 32,144,96\nxdl 161,28,s92,0\n"
"s_add_u32 s28,s28,0x80\ns_addc_u32 s29,s29,0\ns_sub_u32 s30,s30,0x80\nxam 66,0\nxm16 36,148,96\nxdl 162,32,0,0\nxam 66,0x400\nxm16 40,152,96\n"
"xdl 162,32,s93,0\nxm16 44,156,96\nxam 66,0x800\nxm16 48,112,100\nxdl 163,32,s94,0\nxam 66,0xc00\nxm16 52,116,100\nxdl 163,32,s95,0\nxam 66,0x1000\n"
"xm16 56,120,100\nxdl 162,32,s96,0\nxam 66,0x1400\nxm16 60,124,100\nxdl 162,32,s97,0\nxam 66,0x1800\nxm16 64,128,100\nxdl 163,32,s98,0\n"
"xm16 68,132,100\nxam 66,0x1c00\nxm16 72,136,100\nxdl 163,32,s99,0\ns_add_u32 s32,s32,0x80000\ns_addc_u32 s33,s33,0\ns_sub_u32 s34,s34,0x80000\n"
"xam 65,0x20000\nxm16 76,140,100\nxdl 160,28,0,0\nxam 65,0x20400\nxm16 80,144,100\nxdl 161,28,s86,0\nxam 65,0x20800\nxm16 84,148,100\n"
"xdl 160,28,s87,0\nxam 65,0x20c00\nxm16 88,152,100\nxdl 161,28,s88,0\nxm16 92,156,100\nxam 65,0x21000\nxm16 96,112,104\nxdl 160,28,s89,0\n"
"xam 65,0x21400\nxm16 100,116,104\nxdl 161,28,s90,0\nxam 65,0x21800\nxm16 104,120,104\nxdl 160,28,s91,0\nxam 65,0x21c00\nxm16 108,124,104\n"
"xdl 161,28,s92,0\ns_add_u32 s28,s28,0x80\ns_addc_u32 s29,s29,0\ns_sub_u32 s30,s30,0x80\nxam 66,0x8000\nxm16 112,128,104\nxdl 162,32,0,0\n"
"xm16 116,132,104\nxam 66,0x8400\nxm16 120,136,104\nxdl 162,32,s93,0\nxam 66,0x8800\nxm16 124,140,104\nxdl 163,32,s94,0\nxam 66,0x8c00\n"
"xm16 128,144,104\nxdl 163,32,s95,0\nxam 66,0x9000\nxm16 132,148,104\nxdl 162,32,s96,0\nxam 66,0x9400\nxm16 136,152,104\nxdl 162,32,s97,0\n"
"xm16 140,156,104\nxam 66,0x9800\nxm16 144,112,108\nxdl 163,32,s98,0\nxam 66,0x9c00\nxm16 148,116,108\nxdl 163,32,s99,0\ns_add_u32 s32,s32,0x80000\n"
"s_addc_u32 s33,s33,0\ns_sub_u32 s34,s34,0x80000\nxam 66,0x10000\nxm16 152,120,108\nxdl 162,32,0,0\nxam 66,0x10400\nxm16 156,124,108\n"
"xdl 162,32,s93,0\nxam 66,0x10800\nxm16 160,128,108\nxdl 163,32,s94,0\nxm16 164,132,108\nxam 66,0x10c00\nxm16 168,136,108\nxdl 163,32,s95,0\n"
"xam 66,0x11000\nxm16 172,140,108\nxdl 162,32,s96,0\nxam 66,0x11400\nxm16 176,144,108\nxdl 162,32,s97,0\nxam 66,0x11800\nxm16 180,148,108\n"
"xdl 163,32,s98,0\nxam 66,0x11c00\nxm16 184,152,108\nxdl 163,32,s99,0\ns_add_u32 s32,s32,0x80000\ns_addc_u32 s33,s33,0\ns_sub_u32 s34,s34,0x80000\n"
"xm16 188,156,108\nv_readfirstlane_b32 s6,v172\nv_readfirstlane_b32 s7,v173\ns_nop 3\ns_add_u32 s6,s6,1\ns_addc_u32 s7,s7,0\ns_nop 3\n"
"s_mov_b32 s72,0x3db8aa3b\nxar 32,0\nxar 33,1\nxar 34,2\nxar 35,3\nxar 36,4\nxar 37,5\nxar 38,6\nxar 39,7\nxmu 32,s72,32\nxmu 33,s72,33\n"
"xmu 34,s72,34\nxmu 35,s72,35\nxmu 36,s72,36\nxmu 37,s72,37\nxmu 38,s72,38\nxmu 39,s72,39\nxcv 48,32,33\nxcv 49,34,35\nxcv 50,36,37\nxcv 51,38,39\n"
"xst1 48,30,40,0,0\nxar 40,8\nxar 41,9\nxar 42,10\nxar 43,11\nxar 44,12\nxar 45,13\nxar 46,14\nxar 47,15\nxmu 40,s72,40\nxmu 41,s72,41\nxmu 42,s72,42\n"
"xmu 43,s72,43\nxmu 44,s72,44\nxmu 45,s72,45\nxmu 46,s72,46\nxmu 47,s72,47\nxcv 52,40,41\nxcv 53,42,43\nxcv 54,44,45\nxcv 55,46,47\n"
"xst1 52,30,40,0,2048\nxar 32,16\nxar 33,17\nxar 34,18\nxar 35,19\nxar 36,20\nxar 37,21\nxar 38,22\nxar 39,23\nxmu 32,s72,32\nxmu 33,s72,33\n"
"xmu 34,s72,34\nxmu 35,s72,35\nxmu 36,s72,36\nxmu 37,s72,37\nxmu 38,s72,38\nxmu 39,s72,39\nxcv 56,32,33\nxcv 57,34,35\nxcv 58,36,37\nxcv 59,38,39\n"
"s_mov_b32 s68,0x1000\nxst1 56,30,40,s68,0\nxar 40,24\nxar 41,25\nxar 42,26\nxar 43,27\nxar 44,28\nxar 45,29\nxar 46,30\nxar 47,31\nxmu 40,s72,40\n"
"xmu 41,s72,41\nxmu 42,s72,42\nxmu 43,s72,43\nxmu 44,s72,44\nxmu 45,s72,45\nxmu 46,s72,46\nxmu 47,s72,47\nxcv 60,40,41\nxcv 61,42,43\nxcv 62,44,45\n"
"xcv 63,46,47\nxst1 60,30,40,s68,2048\nxar 32,48\nxar 33,49\nxar 34,50\nxar 35,51\nxar 36,52\nxar 37,53\nxar 38,54\nxar 39,55\nxmu 32,s72,32\n"
"xmu 33,s72,33\nxmu 34,s72,34\nxmu 35,s72,35\nxmu 36,s72,36\nxmu 37,s72,37\nxmu 38,s72,38\nxmu 39,s72,39\nxcv 48,32,33\nxcv 49,34,35\nxcv 50,36,37\n"
"xcv 51,38,39\nxst1 48,30,40,0,256\nxar 40,56\nxar 41,57\nxar 42,58\nxar 43,59\nxar 44,60\nxar 45,61\nxar 46,62\nxar 47,63\nxmu 40,s72,40\n"
"xmu 41,s72,41\nxmu 42,s72,42\nxmu 43,s72,43\nxmu 44,s72,44\nxmu 45,s72,45\nxmu 46,s72,46\nxmu 47,s72,47\nxcv 52,40,41\nxcv 53,42,43\nxcv 54,44,45\n"
"xcv 55,46,47\nxst1 52,30,40,0,2304\nxar 32,64\nxar 33,65\nxar 34,66\nxar 35,67\nxar 36,68\nxar 37,69\nxar 38,70\nxar 39,71\nxmu 32,s72,32\n"
"xmu 33,s72,33\nxmu 34,s72,34\nxmu 35,s72,35\nxmu 36,s72,36\nxmu 37,s72,37\nxmu 38,s72,38\nxmu 39,s72,39\nxcv 56,32,33\nxcv 57,34,35\nxcv 58,36,37\n"
"xcv 59,38,39\nxst1 56,30,40,s68,256\nxar 40,72\nxar 41,73\nxar 42,74\nxar 43,75\nxar 44,76\nxar 45,77\nxar 46,78\nxar 47,79\nxmu 40,s72,40\n"
"xmu 41,s72,41\nxmu 42,s72,42\nxmu 43,s72,43\nxmu 44,s72,44\nxmu 45,s72,45\nxmu 46,s72,46\nxmu 47,s72,47\nxcv 60,40,41\nxcv 61,42,43\nxcv 62,44,45\n"
"xcv 63,46,47\nxst1 60,30,40,s68,2304\nxar 32,96\nxar 33,97\nxar 34,98\nxar 35,99\nxar 36,100\nxar 37,101\nxar 38,102\nxar 39,103\nxmu 32,s72,32\n"
"xmu 33,s72,33\nxmu 34,s72,34\nxmu 35,s72,35\nxmu 36,s72,36\nxmu 37,s72,37\nxmu 38,s72,38\nxmu 39,s72,39\nxcv 48,32,33\nxcv 49,34,35\nxcv 50,36,37\n"
"xcv 51,38,39\ns_mov_b32 s68,0x4000\nxst1 48,30,40,s68,0\nxar 40,104\nxar 41,105\nxar 42,106\nxar 43,107\nxar 44,108\nxar 45,109\nxar 46,110\n"
"xar 47,111\nxmu 40,s72,40\nxmu 41,s72,41\nxmu 42,s72,42\nxmu 43,s72,43\nxmu 44,s72,44\nxmu 45,s72,45\nxmu 46,s72,46\nxmu 47,s72,47\nxcv 52,40,41\n"
"xcv 53,42,43\nxcv 54,44,45\nxcv 55,46,47\nxst1 52,30,40,s68,2048\nxar 32,112\nxar 33,113\nxar 34,114\nxar 35,115\nxar 36,116\nxar 37,117\nxar 38,118\n"
"xar 39,119\nxmu 32,s72,32\nxmu 33,s72,33\nxmu 34,s72,34\nxmu 35,s72,35\nxmu 36,s72,36\nxmu 37,s72,37\nxmu 38,s72,38\nxmu 39,s72,39\nxcv 56,32,33\n"
"xcv 57,34,35\nxcv 58,36,37\nxcv 59,38,39\ns_mov_b32 s68,0x5000\nxst1 56,30,40,s68,0\nxar 40,120\nxar 41,121\nxar 42,122\nxar 43,123\nxar 44,124\n"
"xar 45,125\nxar 46,126\nxar 47,127\nxmu 40,s72,40\nxmu 41,s72,41\nxmu 42,s72,42\nxmu 43,s72,43\nxmu 44,s72,44\nxmu 45,s72,45\nxmu 46,s72,46\n"
"xmu 47,s72,47\nxcv 60,40,41\nxcv 61,42,43\nxcv 62,44,45\nxcv 63,46,47\nxst1 60,30,40,s68,2048\nxar 32,144\nxar 33,145\nxar 34,146\nxar 35,147\n"
"xar 36,148\nxar 37,149\nxar 38,150\nxar 39,151\nxmu 32,s72,32\nxmu 33,s72,33\nxmu 34,s72,34\nxmu 35,s72,35\nxmu 36,s72,36\nxmu 37,s72,37\n"
"xmu 38,s72,38\nxmu 39,s72,39\nxcv 48,32,33\nxcv 49,34,35\nxcv 50,36,37\nxcv 51,38,39\ns_mov_b32 s68,0x4000\nxst1 48,30,40,s68,256\nxar 40,152\n"
"xar 41,153\nxar 42,154\nxar 43,155\nxar 44,156\nxar 45,157\nxar 46,158\nxar 47,159\nxmu 40,s72,40\nxmu 41,s72,41\nxmu 42,s72,42\nxmu 43,s72,43\n"
"xmu 44,s72,44\nxmu 45,s72,45\nxmu 46,s72,46\nxmu 47,s72,47\nxcv 52,40,41\nxcv 53,42,43\nxcv 54,44,45\nxcv 55,46,47\nxst1 52,30,40,s68,2304\n"
"xar 32,160\nxar 33,161\nxar 34,162\nxar 35,163\nxar 36,164\nxar 37,165\nxar 38,166\nxar 39,167\nxmu 32,s72,32\nxmu 33,s72,33\nxmu 34,s72,34\n"
"xmu 35,s72,35\nxmu 36,s72,36\nxmu 37,s72,37\nxmu 38,s72,38\nxmu 39,s72,39\nxcv 56,32,33\nxcv 57,34,35\nxcv 58,36,37\nxcv 59,38,39\n"
"s_mov_b32 s68,0x5000\nxst1 56,30,40,s68,256\nxar 40,168\nxar 41,169\nxar 42,170\nxar 43,171\nxar 44,172\nxar 45,173\nxar 46,174\nxar 47,175\n"
"xmu 40,s72,40\nxmu 41,s72,41\nxmu 42,s72,42\nxmu 43,s72,43\nxmu 44,s72,44\nxmu 45,s72,45\nxmu 46,s72,46\nxmu 47,s72,47\nxcv 60,40,41\nxcv 61,42,43\n"
"xcv 62,44,45\nxcv 63,46,47\nxst1 60,30,40,s68,2304\nv_mov_b32 v64,0xc0135761\ns_mov_b32 s72,0xbdd2d3e8\nxar 32,32\nxar 33,33\nxar 34,34\nxar 35,35\n"
"xar 36,36\nxar 37,37\nxar 38,38\nxar 39,39\nxmu 48,v32,32\nxmu 49,v33,33\nxmu 50,v34,34\nxmu 51,v35,35\nxmu 52,v36,36\nxmu 53,v37,37\nxmu 54,v38,38\n"
"xmu 55,v39,39\nxfm 48,v48,s72,v64\nxfm 49,v49,s72,v64\nxfm 50,v50,s72,v64\nxfm 51,v51,s72,v64\nxfm 52,v52,s72,v64\nxfm 53,v53,s72,v64\n"
"xfm 54,v54,s72,v64\nxfm 55,v55,s72,v64\nxmu 48,v48,32\nxmu 49,v49,33\nxmu 50,v50,34\nxmu 51,v51,35\nxmu 52,v52,36\nxmu 53,v53,37\nxmu 54,v54,38\n"
"xmu 55,v55,39\nxex 48,48\nxex 49,49\nxex 50,50\nxex 51,51\nxex 52,52\nxex 53,53\nxex 54,54\nxex 55,55\nxad 48,1.0,48\nxad 49,1.0,49\nxad 50,1.0,50\n"
"xad 51,1.0,51\nxad 52,1.0,52\nxad 53,1.0,53\nxad 54,1.0,54\nxad 55,1.0,55\nxrc 48,48\nxrc 49,49\nxrc 50,50\nxrc 51,51\nxrc 52,52\nxrc 53,53\n"
"xrc 54,54\nxrc 55,55\nxmu 32,v32,48\nxmu 33,v33,49\nxmu 34,v34,50\nxmu 35,v35,51\nxmu 36,v36,52\nxmu 37,v37,53\nxmu 38,v38,54\nxmu 39,v39,55\n"
"xar 40,40\nxar 41,41\nxar 42,42\nxar 43,43\nxar 44,44\nxar 45,45\nxar 46,46\nxar 47,47\nxmu 56,v40,40\nxmu 57,v41,41\nxmu 58,v42,42\nxmu 59,v43,43\n"
"xmu 60,v44,44\nxmu 61,v45,45\nxmu 62,v46,46\nxmu 63,v47,47\nxfm 56,v56,s72,v64\nxfm 57,v57,s72,v64\nxfm 58,v58,s72,v64\nxfm 59,v59,s72,v64\n"
"xfm 60,v60,s72,v64\nxfm 61,v61,s72,v64\nxfm 62,v62,s72,v64\nxfm 63,v63,s72,v64\nxmu 56,v56,40\nxmu 57,v57,41\nxmu 58,v58,42\nxmu 59,v59,43\n"
"xmu 60,v60,44\nxmu 61,v61,45\nxmu 62,v62,46\nxmu 63,v63,47\nxex 56,56\nxex 57,57\nxex 58,58\nxex 59,59\nxex 60,60\nxex 61,61\nxex 62,62\nxex 63,63\n"
"xad 56,1.0,56\nxad 57,1.0,57\nxad 58,1.0,58\nxad 59,1.0,59\nxad 60,1.0,60\nxad 61,1.0,61\nxad 62,1.0,62\nxad 63,1.0,63\nxrc 56,56\nxrc 57,57\n"
"xrc 58,58\nxrc 59,59\nxrc 60,60\nxrc 61,61\nxrc 62,62\nxrc 63,63\nxmu 40,v40,56\nxmu 41,v41,57\nxmu 42,v42,58\nxmu 43,v43,59\nxmu 44,v44,60\n"
"xmu 45,v45,61\nxmu 46,v46,62\nxmu 47,v47,63\nxcv 68,32,33\nxcv 69,34,35\nxcv 70,36,37\nxcv 71,38,39\nxst1 68,31,48,0,0\nxcv 72,40,41\nxcv 73,42,43\n"
"xcv 74,44,45\nxcv 75,46,47\nxst1 72,31,48,0,1024\nxar 32,80\nxar 33,81\nxar 34,82\nxar 35,83\nxar 36,84\nxar 37,85\nxar 38,86\nxar 39,87\n"
"xmu 48,v32,32\nxmu 49,v33,33\nxmu 50,v34,34\nxmu 51,v35,35\nxmu 52,v36,36\nxmu 53,v37,37\nxmu 54,v38,38\nxmu 55,v39,39\nxfm 48,v48,s72,v64\n"
"xfm 49,v49,s72,v64\nxfm 50,v50,s72,v64\nxfm 51,v51,s72,v64\nxfm 52,v52,s72,v64\nxfm 53,v53,s72,v64\nxfm 54,v54,s72,v64\nxfm 55,v55,s72,v64\n"
"xmu 48,v48,32\nxmu 49,v49,33\nxmu 50,v50,34\nxmu 51,v51,35\nxmu 52,v52,36\nxmu 53,v53,37\nxmu 54,v54,38\nxmu 55,v55,39\nxex 48,48\nxex 49,49\n"
"xex 50,50\nxex 51,51\nxex 52,52\nxex 53,53\nxex 54,54\nxex 55,55\nxad 48,1.0,48\nxad 49,1.0,49\nxad 50,1.0,50\nxad 51,1.0,51\nxad 52,1.0,52\n"
"xad 53,1.0,53\nxad 54,1.0,54\nxad 55,1.0,55\nxrc 48,48\nxrc 49,49\nxrc 50,50\nxrc 51,51\nxrc 52,52\nxrc 53,53\nxrc 54,54\nxrc 55,55\nxmu 32,v32,48\n"
"xmu 33,v33,49\nxmu 34,v34,50\nxmu 35,v35,51\nxmu 36,v36,52\nxmu 37,v37,53\nxmu 38,v38,54\nxmu 39,v39,55\nxar 40,88\nxar 41,89\nxar 42,90\nxar 43,91\n"
"xar 44,92\nxar 45,93\nxar 46,94\nxar 47,95\nxmu 56,v40,40\nxmu 57,v41,41\nxmu 58,v42,42\nxmu 59,v43,43\nxmu 60,v44,44\nxmu 61,v45,45\nxmu 62,v46,46\n"
"xmu 63,v47,47\nxfm 56,v56,s72,v64\nxfm 57,v57,s72,v64\nxfm 58,v58,s72,v64\nxfm 59,v59,s72,v64\nxfm 60,v60,s72,v64\nxfm 61,v61,s72,v64\n"
"xfm 62,v62,s72,v64\nxfm 63,v63,s72,v64\nxmu 56,v56,40\nxmu 57,v57,41\nxmu 58,v58,42\nxmu 59,v59,43\nxmu 60,v60,44\nxmu 61,v61,45\nxmu 62,v62,46\n"
"xmu 63,v63,47\nxex 56,56\nxex 57,57\nxex 58,58\nxex 59,59\nxex 60,60\nxex 61,61\nxex 62,62\nxex 63,63\nxad 56,1.0,56\nxad 57,1.0,57\nxad 58,1.0,58\n"
"xad 59,1.0,59\nxad 60,1.0,60\nxad 61,1.0,61\nxad 62,1.0,62\nxad 63,1.0,63\nxrc 56,56\nxrc 57,57\nxrc 58,58\nxrc 59,59\nxrc 60,60\nxrc 61,61\n"
"xrc 62,62\nxrc 63,63\nxmu 40,v40,56\nxmu 41,v41,57\nxmu 42,v42,58\nxmu 43,v43,59\nxmu 44,v44,60\nxmu 45,v45,61\nxmu 46,v46,62\nxmu 47,v47,63\n"
"xcv 76,32,33\nxcv 77,34,35\nxcv 78,36,37\nxcv 79,38,39\ns_mov_b32 s68,0x7000\nxst1 76,31,48,s68,0\nxcv 80,40,41\nxcv 81,42,43\nxcv 82,44,45\n"
"xcv 83,46,47\nxst1 80,31,48,s68,1024\nxar 32,128\nxar 33,129\nxar 34,130\nxar 35,131\nxar 36,132\nxar 37,133\nxar 38,134\nxar 39,135\nxmu 48,v32,32\n"
"xmu 49,v33,33\nxmu 50,v34,34\nxmu 51,v35,35\nxmu 52,v36,36\nxmu 53,v37,37\nxmu 54,v38,38\nxmu 55,v39,39\nxfm 48,v48,s72,v64\nxfm 49,v49,s72,v64\n"
"xfm 50,v50,s72,v64\nxfm 51,v51,s72,v64\nxfm 52,v52,s72,v64\nxfm 53,v53,s72,v64\nxfm 54,v54,s72,v64\nxfm 55,v55,s72,v64\nxmu 48,v48,32\nxmu 49,v49,33\n"
"xmu 50,v50,34\nxmu 51,v51,35\nxmu 52,v52,36\nxmu 53,v53,37\nxmu 54,v54,38\nxmu 55,v55,39\nxex 48,48\nxex 49,49\nxex 50,50\nxex 51,51\nxex 52,52\n"
"xex 53,53\nxex 54,54\nxex 55,55\nxad 48,1.0,48\nxad 49,1.0,49\nxad 50,1.0,50\nxad 51,1.0,51\nxad 52,1.0,52\nxad 53,1.0,53\nxad 54,1.0,54\n"
"xad 55,1.0,55\nxrc 48,48\nxrc 49,49\nxrc 50,50\nxrc 51,51\nxrc 52,52\nxrc 53,53\nxrc 54,54\nxrc 55,55\nxmu 32,v32,48\nxmu 33,v33,49\nxmu 34,v34,50\n"
"xmu 35,v35,51\nxmu 36,v36,52\nxmu 37,v37,53\nxmu 38,v38,54\nxmu 39,v39,55\nxar 40,136\nxar 41,137\nxar 42,138\nxar 43,139\nxar 44,140\nxar 45,141\n"
"xar 46,142\nxar 47,143\nxmu 56,v40,40\nxmu 57,v41,41\nxmu 58,v42,42\nxmu 59,v43,43\nxmu 60,v44,44\nxmu 61,v45,45\nxmu 62,v46,46\nxmu 63,v47,47\n"
"xfm 56,v56,s72,v64\nxfm 57,v57,s72,v64\nxfm 58,v58,s72,v64\nxfm 59,v59,s72,v64\nxfm 60,v60,s72,v64\nxfm 61,v61,s72,v64\nxfm 62,v62,s72,v64\n"
"xfm 63,v63,s72,v64\nxmu 56,v56,40\nxmu 57,v57,41\nxmu 58,v58,42\nxmu 59,v59,43\nxmu 60,v60,44\nxmu 61,v61,45\nxmu 62,v62,46\nxmu 63,v63,47\n"
"xex 56,56\nxex 57,57\nxex 58,58\nxex 59,59\nxex 60,60\nxex 61,61\nxex 62,62\nxex 63,63\nxad 56,1.0,56\nxad 57,1.0,57\nxad 58,1.0,58\nxad 59,1.0,59\n"
"xad 60,1.0,60\nxad 61,1.0,61\nxad 62,1.0,62\nxad 63,1.0,63\nxrc 56,56\nxrc 57,57\nxrc 58,58\nxrc 59,59\nxrc 60,60\nxrc 61,61\nxrc 62,62\nxrc 63,63\n"
"xmu 40,v40,56\nxmu 41,v41,57\nxmu 42,v42,58\nxmu 43,v43,59\nxmu 44,v44,60\nxmu 45,v45,61\nxmu 46,v46,62\nxmu 47,v47,63\nxcv 68,32,33\nxcv 69,34,35\n"
"xcv 70,36,37\nxcv 71,38,39\ns_mov_b32 s68,0xe000\nxst1 68,31,48,s68,0\nxcv 72,40,41\nxcv 73,42,43\nxcv 74,44,45\nxcv 75,46,47\n"
"xst1 72,31,48,s68,1024\nxar 32,176\nxar 33,177\nxar 34,178\nxar 35,179\nxar 36,180\nxar 37,181\nxar 38,182\nxar 39,183\nxmu 48,v32,32\nxmu 49,v33,33\n"
"xmu 50,v34,34\nxmu 51,v35,35\nxmu 52,v36,36\nxmu 53,v37,37\nxmu 54,v38,38\nxmu 55,v39,39\nxfm 48,v48,s72,v64\nxfm 49,v49,s72,v64\nxfm 50,v50,s72,v64\n"
"xfm 51,v51,s72,v64\nxfm 52,v52,s72,v64\nxfm 53,v53,s72,v64\nxfm 54,v54,s72,v64\nxfm 55,v55,s72,v64\nxmu 48,v48,32\nxmu 49,v49,33\nxmu 50,v50,34\n"
"xmu 51,v51,35\nxmu 52,v52,36\nxmu 53,v53,37\nxmu 54,v54,38\nxmu 55,v55,39\nxex 48,48\nxex 49,49\nxex 50,50\nxex 51,51\nxex 52,52\nxex 53,53\n"
"xex 54,54\nxex 55,55\nxad 48,1.0,48\nxad 49,1.0,49\nxad 50,1.0,50\nxad 51,1.0,51\nxad 52,1.0,52\nxad 53,1.0,53\nxad 54,1.0,54\nxad 55,1.0,55\n"
"xrc 48,48\nxrc 49,49\nxrc 50,50\nxrc 51,51\nxrc 52,52\nxrc 53,53\nxrc 54,54\nxrc 55,55\nxmu 32,v32,48\nxmu 33,v33,49\nxmu 34,v34,50\nxmu 35,v35,51\n"
"xmu 36,v36,52\nxmu 37,v37,53\nxmu 38,v38,54\nxmu 39,v39,55\nxar 40,184\nxar 41,185\nxar 42,186\nxar 43,187\nxar 44,188\nxar 45,189\nxar 46,190\n"
"xar 47,191\nxmu 56,v40,40\nxmu 57,v41,41\nxmu 58,v42,42\nxmu 59,v43,43\nxmu 60,v44,44\nxmu 61,v45,45\nxmu 62,v46,46\nxmu 63,v47,47\n"
"xfm 56,v56,s72,v64\nxfm 57,v57,s72,v64\nxfm 58,v58,s72,v64\nxfm 59,v59,s72,v64\nxfm 60,v60,s72,v64\nxfm 61,v61,s72,v64\nxfm 62,v62,s72,v64\n"
"xfm 63,v63,s72,v64\nxmu 56,v56,40\nxmu 57,v57,41\nxmu 58,v58,42\nxmu 59,v59,43\nxmu 60,v60,44\nxmu 61,v61,45\nxmu 62,v62,46\nxmu 63,v63,47\n"
"xex 56,56\nxex 57,57\nxex 58,58\nxex 59,59\nxex 60,60\nxex 61,61\nxex 62,62\nxex 63,63\nxad 56,1.0,56\nxad 57,1.0,57\nxad 58,1.0,58\nxad 59,1.0,59\n"
"xad 60,1.0,60\nxad 61,1.0,61\nxad 62,1.0,62\nxad 63,1.0,63\nxrc 56,56\nxrc 57,57\nxrc 58,58\nxrc 59,59\nxrc 60,60\nxrc 61,61\nxrc 62,62\nxrc 63,63\n"
"xmu 40,v40,56\nxmu 41,v41,57\nxmu 42,v42,58\nxmu 43,v43,59\nxmu 44,v44,60\nxmu 45,v45,61\nxmu 46,v46,62\nxmu 47,v47,63\nxcv 76,32,33\nxcv 77,34,35\n"
"xcv 78,36,37\nxcv 79,38,39\ns_mov_b32 s68,0x15000\nxst1 76,31,48,s68,0\nxcv 80,40,41\nxcv 81,42,43\nxcv 82,44,45\nxcv 83,46,47\n"
"xst1 80,31,48,s68,1024\ns_branch Lkv_3_%=\nLqz2_1_%=x:\ns_and_b32 s98,s100,15\ns_add_u32 s98,s98,2\ns_lshr_b32 s98,s98,4\ns_cmp_lg_u32 s98,0\n"
"s_cbranch_scc1 Lqg_2_%=\ns_nop 4\nv_mbcnt_lo_u32_b32 v252,-1,0\nv_mbcnt_hi_u32_b32 v252,-1,v252\ns_load_dwordx2 s[76:77],s[0:1],0\n"
"s_load_dwordx2 s[78:79],s[0:1],16\ns_load_dwordx2 s[80:81],s[0:1],48\nxwl 0\ns_lshr_b32 s99,s100,4\ns_and_b32 s99,s99,15\ns_mul_i32 s99,s99,0x1c000\n"
"s_add_u32 s16,s76,s99\ns_addc_u32 s17,s77,0\ns_and_b32 s17,s17,0xffff\ns_lshr_b32 s18,s100,4\ns_and_b32 s18,s18,15\ns_mul_i32 s18,s18,0x1c000\n"
"s_sub_u32 s18,0x1c0000,s18\ns_mov_b32 s19,0x20000\ns_and_b32 s99,s100,15\ns_lshl_b32 s99,s99,9\ns_add_u32 s20,s78,s99\ns_addc_u32 s21,s79,0\n"
"s_and_b32 s21,s21,0xffff\ns_and_b32 s22,s100,15\ns_lshl_b32 s22,s22,9\ns_sub_u32 s22,0x380000,s22\ns_mov_b32 s23,0x20000\ns_and_b32 s99,s100,15\n"
"s_lshl_b32 s99,s99,8\ns_add_u32 s24,s80,s99\ns_addc_u32 s25,s81,0\ns_and_b32 s25,s25,0xffff\ns_and_b32 s26,s100,15\ns_lshl_b32 s26,s26,8\n"
"s_sub_u32 s26,0x188000,s26\ns_mov_b32 s27,0x20000\nv_lshrrev_b32 v0,3,v252\ns_lshl_b32 s99,s3,5\nv_add_u32 v0,s99,v0\nv_mul_u32_u24 v0,0x380,v0\n"
"v_and_b32 v247,7,v252\nv_lshrrev_b32 v246,3,v252\nv_lshrrev_b32 v246,1,v246\nv_xor_b32 v247,v247,v246\nv_lshlrev_b32 v247,4,v247\n"
"v_add_u32 v0,v0,v247\nv_xor_b32 v1,64,v0\nv_lshrrev_b32 v2,4,v252\ns_and_b32 s99,s3,1\ns_lshl_b32 s99,s99,5\nv_add_u32 v2,s99,v2\n"
"v_lshlrev_b32 v2,13,v2\ns_lshr_b32 s99,s3,1\ns_lshl_b32 s99,s99,8\nv_add_u32 v2,s99,v2\nv_and_b32 v247,15,v252\nv_lshrrev_b32 v247,1,v247\n"
"v_lshrrev_b32 v246,4,v252\nv_xor_b32 v247,v247,v246\nv_lshlrev_b32 v247,1,v247\nv_and_b32 v246,15,v252\nv_and_b32 v246,1,v246\n"
"v_or_b32 v247,v247,v246\nv_lshlrev_b32 v247,4,v247\nv_add_u32 v2,v2,v247\nv_xor_b32 v3,0x80,v2\nv_lshrrev_b32 v4,4,v252\ns_lshl_b32 s99,s3,4\n"
"v_add_u32 v4,s99,v4\nv_mul_u32_u24 v4,0xe00,v4\nv_and_b32 v247,15,v252\nv_lshrrev_b32 v247,1,v247\nv_lshrrev_b32 v246,4,v252\n"
"v_xor_b32 v247,v247,v246\nv_lshlrev_b32 v247,1,v247\nv_and_b32 v246,15,v252\nv_and_b32 v246,1,v246\nv_or_b32 v247,v247,v246\n"
"v_lshlrev_b32 v247,4,v247\nv_add_u32 v4,v4,v247\nv_xor_b32 v5,0x80,v4\nv_and_b32 v6,15,v252\ns_lshr_b32 s99,s3,1\ns_lshl_b32 s99,s99,6\n"
"v_add_u32 v6,s99,v6\nv_lshlrev_b32 v6,7,v6\nv_lshrrev_b32 v247,4,v252\nv_and_b32 v246,15,v252\nv_lshrrev_b32 v246,1,v246\nv_xor_b32 v247,v247,v246\n"
"v_lshlrev_b32 v247,4,v247\nv_add_u32 v6,v6,v247\nv_add_u32 v6,0x10000,v6\nv_xor_b32 v7,64,v6\nv_lshrrev_b32 v250,4,v252\nv_lshlrev_b32 v250,3,v250\n"
"v_and_b32 v247,15,v252\nv_lshrrev_b32 v247,2,v247\nv_add_u32 v250,v250,v247\nv_lshlrev_b32 v250,8,v250\ns_and_b32 s99,s3,1\ns_lshl_b32 s99,s99,14\n"
"v_add_u32 v250,s99,v250\nv_and_b32 v247,3,v252\nv_lshlrev_b32 v247,3,v247\nv_add_u32 v250,v250,v247\nv_and_b32 v251,15,v252\n"
"v_lshrrev_b32 v251,2,v251\nv_lshrrev_b32 v247,4,v252\nv_and_b32 v247,1,v247\nv_lshlrev_b32 v247,2,v247\nv_or_b32 v251,v251,v247\n"
"v_lshlrev_b32 v251,5,v251\nv_xor_b32 v8,0,v251\nv_add_u32 v8,v8,v250\nv_xor_b32 v9,32,v251\nv_add_u32 v9,v9,v250\nv_xor_b32 v10,64,v251\n"
"v_add_u32 v10,v10,v250\nv_xor_b32 v11,0x60,v251\nv_add_u32 v11,v11,v250\nv_xor_b32 v12,0x80,v251\nv_add_u32 v12,v12,v250\nv_xor_b32 v13,0xa0,v251\n"
"v_add_u32 v13,v13,v250\nv_xor_b32 v14,0xc0,v251\nv_add_u32 v14,v14,v250\nv_xor_b32 v15,0xe0,v251\nv_add_u32 v15,v15,v250\nv_lshrrev_b32 v250,4,v252\n"
"v_lshlrev_b32 v250,3,v250\nv_and_b32 v247,15,v252\nv_lshrrev_b32 v247,2,v247\nv_add_u32 v250,v250,v247\nv_lshlrev_b32 v250,8,v250\n"
"v_and_b32 v247,3,v252\nv_lshlrev_b32 v247,3,v247\nv_add_u32 v250,v250,v247\nv_add_u32 v250,0x1c000,v250\nv_and_b32 v251,15,v252\n"
"v_lshrrev_b32 v251,2,v251\nv_lshrrev_b32 v247,4,v252\nv_and_b32 v247,1,v247\nv_lshlrev_b32 v247,2,v247\nv_or_b32 v251,v251,v247\ns_and_b32 s99,s3,1\n"
"s_lshl_b32 s99,s99,2\nv_xor_b32 v251,s99,v251\nv_lshlrev_b32 v251,5,v251\nv_xor_b32 v16,0,v251\nv_add_u32 v16,v16,v250\nv_xor_b32 v17,32,v251\n"
"v_add_u32 v17,v17,v250\nv_xor_b32 v18,64,v251\nv_add_u32 v18,v18,v250\nv_xor_b32 v19,0x60,v251\nv_add_u32 v19,v19,v250\n"
"s_load_dwordx2 s[82:83],s[0:1],8\ns_load_dwordx2 s[84:85],s[0:1],24\ns_load_dwordx2 s[86:87],s[0:1],32\nxwl 0\ns_lshr_b32 s99,s100,8\n"
"s_and_b32 s99,s99,15\ns_lshl_b32 s99,s99,19\ns_add_u32 s28,s82,s99\ns_addc_u32 s29,s83,0\ns_and_b32 s29,s29,0xffff\ns_lshr_b32 s30,s100,8\n"
"s_and_b32 s30,s30,15\ns_lshl_b32 s30,s30,19\ns_sub_u32 s30,0x400000,s30\ns_mov_b32 s31,0x20000\ns_lshr_b32 s99,s100,16\ns_and_b32 s99,s99,1\n"
"s_cmp_lg_u32 s99,0\ns_cselect_b32 s74,s86,s84\ns_lshr_b32 s99,s100,16\ns_and_b32 s99,s99,1\ns_cmp_lg_u32 s99,0\ns_cselect_b32 s75,s87,s85\n"
"s_lshr_b32 s99,s100,12\ns_and_b32 s99,s99,15\ns_lshl_b32 s99,s99,9\ns_add_u32 s32,s74,s99\ns_addc_u32 s33,s75,0\ns_and_b32 s33,s33,0xffff\n"
"s_lshr_b32 s34,s100,12\ns_and_b32 s34,s34,15\ns_lshl_b32 s34,s34,9\ns_sub_u32 s34,0x800000,s34\ns_mov_b32 s35,0x20000\nv_lshrrev_b32 v160,3,v252\n"
"s_lshl_b32 s99,s3,6\nv_add_u32 v160,s99,v160\nv_lshlrev_b32 v160,11,v160\nv_and_b32 v247,7,v252\nv_lshrrev_b32 v246,3,v252\n"
"v_lshrrev_b32 v246,1,v246\nv_xor_b32 v247,v247,v246\nv_lshlrev_b32 v247,4,v247\nv_add_u32 v160,v160,v247\nv_xor_b32 v161,64,v160\n"
"v_lshrrev_b32 v162,4,v252\ns_and_b32 s99,s3,1\ns_lshl_b32 s99,s99,5\nv_add_u32 v162,s99,v162\nv_lshlrev_b32 v162,13,v162\ns_lshr_b32 s99,s3,1\n"
"s_lshl_b32 s99,s99,8\nv_add_u32 v162,s99,v162\nv_and_b32 v247,15,v252\nv_lshrrev_b32 v247,1,v247\nv_lshrrev_b32 v246,4,v252\n"
"v_xor_b32 v247,v247,v246\nv_lshlrev_b32 v247,1,v247\nv_and_b32 v246,15,v252\nv_and_b32 v246,1,v246\nv_or_b32 v247,v247,v246\n"
"v_lshlrev_b32 v247,4,v247\nv_add_u32 v162,v162,v247\nv_xor_b32 v163,0x80,v162\ns_lshl_b32 s99,s2,14\ns_add_u32 s53,s82,s99\ns_addc_u32 s54,s83,0\n"
"s_and_b32 s54,s54,0xffff\ns_lshl_b32 s99,s2,15\ns_add_u32 s55,s84,s99\ns_addc_u32 s56,s85,0\ns_and_b32 s56,s56,0xffff\ns_lshl_b32 s99,s2,15\n"
"s_add_u32 s57,s86,s99\ns_addc_u32 s58,s87,0\ns_and_b32 s58,s58,0xffff\ns_lshr_b32 s52,s100,16\ns_and_b32 s52,s52,1\nv_lshlrev_b32 v164,6,v252\n"
"s_lshl_b32 s99,s3,12\nv_add_u32 v164,s99,v164\nv_lshrrev_b32 v30,4,v252\nv_lshrrev_b32 v30,1,v30\nv_lshlrev_b32 v30,10,v30\ns_lshr_b32 s99,s3,1\n"
"s_lshl_b32 s99,s99,15\ns_and_b32 s98,s3,1\ns_lshl_b32 s98,s98,13\ns_add_u32 s99,s99,s98\nv_add_u32 v30,s99,v30\nv_lshrrev_b32 v247,4,v252\n"
"v_and_b32 v247,1,v247\nv_lshlrev_b32 v247,9,v247\nv_add_u32 v30,v30,v247\nv_and_b32 v247,15,v252\nv_lshlrev_b32 v247,4,v247\nv_add_u32 v30,v30,v247\n"
"s_and_b32 s99,s100,15\ns_lshl_b32 s99,s99,20\ns_add_u32 s99,0x2000000,s99\ns_lshr_b32 s98,s100,4\ns_and_b32 s98,s98,15\ns_lshl_b32 s98,s98,16\n"
"s_add_u32 s99,s99,s98\ns_add_u32 s40,s4,s99\ns_addc_u32 s41,s5,0\ns_and_b32 s41,s41,0xffff\ns_mov_b32 s42,0x10000\ns_mov_b32 s43,0x20000\n"
"s_add_u32 s44,s4,0x6c00000\ns_addc_u32 s45,s5,0\ns_and_b32 s45,s45,0xffff\ns_mov_b32 s46,0x8000\ns_mov_b32 s47,0x20000\nv_and_b32 v31,15,v252\n"
"s_lshr_b32 s99,s3,1\ns_lshl_b32 s99,s99,6\nv_add_u32 v31,s99,v31\nv_lshlrev_b32 v31,7,v31\ns_and_b32 s99,s100,15\ns_sub_u32 s99,s99,7\n"
"s_lshl_b32 s99,s99,1\ns_and_b32 s98,s3,1\ns_add_u32 s99,s99,s98\ns_lshl_b32 s99,s99,3\nv_add_u32 v31,s99,v31\ns_lshr_b32 s99,s100,4\n"
"s_and_b32 s99,s99,15\ns_lshl_b32 s99,s99,14\ns_add_u32 s99,0x6980000,s99\ns_add_u32 s48,s4,s99\ns_addc_u32 s49,s5,0\ns_and_b32 s49,s49,0xffff\n"
"s_mov_b32 s50,0x4000\ns_mov_b32 s51,0x20000\ns_mov_b32 s74,0x1c00\ns_mov_b32 s75,0x3800\ns_mov_b32 s76,0x5400\ns_mul_i32 s62,s3,0x1000\n"
"s_mov_b32 s77,0x8000\ns_mov_b32 s78,0x10000\ns_mov_b32 s79,0x18000\ns_mov_b32 s80,0x20000\ns_mov_b32 s81,0x28000\ns_mov_b32 s82,0x30000\n"
"s_mov_b32 s83,0x38000\ns_mul_i32 s63,s3,0x2000\ns_mov_b32 s84,0x7000\ns_mov_b32 s85,0xa800\ns_mul_i32 s64,s3,0x1000\nxam 62,0x10000\ns_nop 0\n"
"xdl 0,16,0,0\nxam 62,0x10400\ns_nop 0\nxdl 1,16,s74,0\nxam 62,0x10800\ns_nop 0\nxdl 0,16,s75,0\nxam 62,0x10c00\ns_nop 0\nxdl 1,16,s76,0\n"
"s_add_u32 s16,s16,0x80\ns_addc_u32 s17,s17,0\ns_sub_u32 s18,s18,0x80\nxam 63,0\ns_nop 0\nxdl 2,20,0,0\nxam 63,0x400\ns_nop 0\nxdl 2,20,s77,0\n"
"xam 63,0x800\ns_nop 0\nxdl 3,20,s78,0\nxam 63,0xc00\ns_nop 0\nxdl 3,20,s79,0\nxam 63,0x1000\ns_nop 0\nxdl 2,20,s80,0\nxam 63,0x1400\ns_nop 0\n"
"xdl 2,20,s81,0\nxam 63,0x1800\ns_nop 0\nxdl 3,20,s82,0\nxam 63,0x1c00\ns_nop 0\nxdl 3,20,s83,0\ns_add_u32 s20,s20,0x80000\ns_addc_u32 s21,s21,0\n"
"s_sub_u32 s22,s22,0x80000\nxam 64,0x1c000\ns_nop 0\nxdl 4,24,0,0\nxam 64,0x1c400\ns_nop 0\nxdl 4,24,s75,0\nxam 64,0x1c800\ns_nop 0\nxdl 5,24,s84,0\n"
"xam 64,0x1cc00\ns_nop 0\nxdl 5,24,s85,0\ns_add_u32 s24,s24,0x38000\ns_addc_u32 s25,s25,0\ns_sub_u32 s26,s26,0x38000\nxam 62,0x14000\ns_nop 0\n"
"xdl 0,16,0,0\nxam 62,0x14400\ns_nop 0\nxdl 1,16,s74,0\nxam 62,0x14800\ns_nop 0\nxdl 0,16,s75,0\nxam 62,0x14c00\ns_nop 0\nxdl 1,16,s76,0\n"
"s_add_u32 s16,s16,0x80\ns_addc_u32 s17,s17,0\ns_sub_u32 s18,s18,0x80\nxam 63,0x8000\ns_nop 0\nxdl 2,20,0,0\nxam 63,0x8400\ns_nop 0\nxdl 2,20,s77,0\n"
"xam 63,0x8800\ns_nop 0\nxdl 3,20,s78,0\nxam 63,0x8c00\ns_nop 0\nxdl 3,20,s79,0\nxam 63,0x9000\ns_nop 0\nxdl 2,20,s80,0\nxam 63,0x9400\ns_nop 0\n"
"xdl 2,20,s81,0\nxam 63,0x9800\ns_nop 0\nxdl 3,20,s82,0\nxam 63,0x9c00\ns_nop 0\nxdl 3,20,s83,0\ns_add_u32 s20,s20,0x80000\ns_addc_u32 s21,s21,0\n"
"s_sub_u32 s22,s22,0x80000\nxam 64,0x20000\ns_nop 0\nxdl 4,24,0,0\nxam 64,0x20400\ns_nop 0\nxdl 4,24,s75,0\nxam 64,0x20800\ns_nop 0\nxdl 5,24,s84,0\n"
"xam 64,0x20c00\ns_nop 0\nxdl 5,24,s85,0\ns_add_u32 s24,s24,0x38000\ns_addc_u32 s25,s25,0\ns_sub_u32 s26,s26,0x38000\nxam 62,0x18000\ns_nop 0\n"
"xdl 0,16,0,0\nxam 62,0x18400\ns_nop 0\nxdl 1,16,s74,0\nxam 62,0x18800\ns_nop 0\nxdl 0,16,s75,0\nxam 62,0x18c00\ns_nop 0\nxdl 1,16,s76,0\n"
"s_add_u32 s16,s16,0x80\ns_addc_u32 s17,s17,0\ns_sub_u32 s18,s18,0x80\nxam 64,0x24000\ns_nop 0\nxdl 4,24,0,0\nxam 64,0x24400\ns_nop 0\nxdl 4,24,s75,0\n"
"xam 64,0x24800\ns_nop 0\nxdl 5,24,s84,0\nxam 64,0x24c00\ns_nop 0\nxdl 5,24,s85,0\ns_add_u32 s24,s24,0x38000\ns_addc_u32 s25,s25,0\n"
"s_sub_u32 s26,s26,0x38000\nv_mov_b32 v174,0\nbuffer_load_dwordx2 v[172:173],v174,s[44:47],0 offen offset:0 sc1\ns_mov_b32 s39,0x20000\n"
"s_mov_b32 s36,s53\ns_mov_b32 s37,s54\ns_mov_b32 s38,0x4000\nbuffer_load_dword v171,v164,s[36:39],0 offen offset:0\ns_mov_b32 s36,s55\n"
"s_mov_b32 s37,s56\ns_mov_b32 s38,0x8000\nbuffer_load_dword v171,v164,s[36:39],0 offen offset:0\ns_mov_b32 s68,0x4000\n"
"buffer_load_dword v171,v164,s[36:39],s68 offen offset:0\ns_mov_b32 s36,s57\ns_mov_b32 s37,s58\ns_mov_b32 s38,0x8000\n"
"buffer_load_dword v171,v164,s[36:39],0 offen offset:0\ns_mov_b32 s68,0x4000\nbuffer_load_dword v171,v164,s[36:39],s68 offen offset:0\n"
"s_mov_b32 s86,0x4000\ns_cmp_eq_u32 s52,0\ns_cselect_b32 s86,s86,0x8000\ns_mov_b32 s87,0x8000\ns_cmp_eq_u32 s52,0\ns_cselect_b32 s87,s87,0x4000\n"
"s_mov_b32 s88,0xc000\ns_mov_b32 s89,0x10000\ns_mov_b32 s90,0x14000\ns_cmp_eq_u32 s52,0\ns_cselect_b32 s90,s90,0x18000\ns_mov_b32 s91,0x18000\n"
"s_cmp_eq_u32 s52,0\ns_cselect_b32 s91,s91,0x14000\ns_mov_b32 s92,0x1c000\ns_mul_i32 s65,s3,0x2000\ns_mov_b32 s93,0x8000\ns_mov_b32 s94,0x10000\n"
"s_mov_b32 s95,0x18000\ns_mov_b32 s96,0x20000\ns_mov_b32 s97,0x28000\ns_mov_b32 s98,0x30000\ns_mov_b32 s99,0x38000\ns_mul_i32 s66,s3,0x2000\n"
"s_waitcnt vmcnt(30)\ns_barrier\nxrt 48,8,0\nxrt 50,8,1024\nxrt 52,9,0\nxrt 54,9,1024\nxrt 56,10,0\nxrt 58,10,1024\nxrt 60,11,0\nxrt 62,11,1024\n"
"xrt 64,12,0\nxrt 66,12,1024\nxrt 68,13,0\nxrt 70,13,1024\nxrt 72,14,0\nxrt 74,14,1024\nxrt 76,15,0\nxrt 78,15,1024\nxrt 80,16,0\nxrt 82,16,1024\n"
"xrt 84,17,0\nxrt 86,17,1024\nxrt 88,18,0\nxrt 90,18,1024\nxrt 92,19,0\nxrt 94,19,1024\nxrb 32,6,0\nxrb 36,6,2048\nxrb 40,6,4096\nxrb 44,6,6144\n"
"xwl 3\nxm16z 0,48,32\nxrt 112,8,8192\nxm16z 4,52,32\nxrt 114,8,9216\nxm16z 8,56,32\nxrt 116,9,8192\nxm16z 12,60,32\nxrt 118,9,9216\nxm16z 16,64,32\n"
"xm16z 20,68,32\nxrt 120,10,8192\nxm16z 24,72,32\nxrt 122,10,9216\nxm16z 28,76,32\nxrt 124,11,8192\nxm16z 32,80,32\nxm16z 36,84,32\nxrt 126,11,9216\n"
"xm16z 40,88,32\nxrt 128,12,8192\nxm16z 44,92,32\nxrt 130,12,9216\nxwl 12\nxm16z 48,48,36\nxrt 132,13,8192\nxm16z 52,52,36\nxm16z 56,56,36\n"
"xrt 134,13,9216\nxm16z 60,60,36\nxrt 136,14,8192\nxm16z 64,64,36\nxrt 138,14,9216\nxm16z 68,68,36\nxm16z 72,72,36\nxrt 140,15,8192\nxm16z 76,76,36\n"
"xrt 142,15,9216\nxm16z 80,80,36\nxrt 144,16,8192\nxm16z 84,84,36\nxrt 146,16,9216\nxm16z 88,88,36\nxm16z 92,92,36\nxrt 148,17,8192\nxwl 15\n"
"xm16z 96,48,40\nxrt 150,17,9216\nxm16z 100,52,40\nxrt 152,18,8192\nxm16z 104,56,40\nxm16z 108,60,40\nxrt 154,18,9216\nxm16z 112,64,40\n"
"xrt 156,19,8192\nxm16z 116,68,40\nxrt 158,19,9216\nxm16z 120,72,40\nxrb 96,7,0\nxm16z 124,76,40\nxm16z 128,80,40\nxrb 100,7,2048\nxm16z 132,84,40\n"
"xrb 104,7,4096\nxm16z 136,88,40\nxrb 108,7,6144\nxm16z 140,92,40\nxm16z 144,48,44\nxm16z 148,52,44\nxm16z 152,56,44\nxm16z 156,60,44\n"
"xm16z 160,64,44\nxm16z 164,68,44\nxm16z 168,72,44\nxm16z 172,76,44\nxm16z 176,80,44\nxm16z 180,84,44\nxm16z 184,88,44\nxm16z 188,92,44\n"
"s_waitcnt vmcnt(14) lgkmcnt(0)\ns_barrier\nxam 63,0\nxm16 0,112,96\nxdl 2,20,0,0\nxrt 48,8,32768\nxm16 4,116,96\nxrt 50,8,33792\nxm16 8,120,96\n"
"xrt 52,9,32768\nxm16 12,124,96\nxrt 54,9,33792\nxm16 16,128,96\nxam 63,0x400\nxm16 20,132,96\nxdl 2,20,s77,0\nxrt 56,10,32768\nxm16 24,136,96\n"
"xrt 58,10,33792\nxm16 28,140,96\nxrt 60,11,32768\nxm16 32,144,96\nxm16 36,148,96\nxrt 62,11,33792\nxam 63,0x800\nxm16 40,152,96\nxdl 3,20,s78,0\n"
"xrt 64,12,32768\nxm16 44,156,96\nxrt 66,12,33792\nxm16 48,112,100\nxrt 68,13,32768\nxm16 52,116,100\nxm16 56,120,100\nxrt 70,13,33792\n"
"xm16 60,124,100\nxrt 72,14,32768\nxam 63,0xc00\nxm16 64,128,100\nxdl 3,20,s79,0\nxrt 74,14,33792\nxm16 68,132,100\nxm16 72,136,100\nxrt 76,15,32768\n"
"xm16 76,140,100\nxrt 78,15,33792\nxm16 80,144,100\nxrt 80,16,16384\nxam 63,0x1000\nxm16 84,148,100\nxdl 2,20,s80,0\nxrt 82,16,17408\nxm16 88,152,100\n"
"xm16 92,156,100\nxrt 84,17,16384\nxm16 96,112,104\nxrt 86,17,17408\nxm16 100,116,104\nxrt 88,18,16384\nxam 63,0x1400\nxm16 104,120,104\n"
"xdl 2,20,s81,0\nxm16 108,124,104\nxrt 90,18,17408\nxm16 112,128,104\nxrt 92,19,16384\nxm16 116,132,104\nxrt 94,19,17408\nxm16 120,136,104\n"
"xrb 32,6,16384\nxm16 124,140,104\nxam 63,0x1800\nxm16 128,144,104\nxdl 3,20,s82,0\nxrb 36,6,18432\nxm16 132,148,104\nxrb 40,6,20480\n"
"xm16 136,152,104\nxrb 44,6,22528\nxm16 140,156,104\nxm16 144,112,108\nxam 63,0x1c00\nxm16 148,116,108\nxdl 3,20,s83,0\ns_add_u32 s20,s20,0x80000\n"
"s_addc_u32 s21,s21,0\ns_sub_u32 s22,s22,0x80000\nxm16 152,120,108\nxm16 156,124,108\nxm16 160,128,108\nxm16 164,132,108\nxm16 168,136,108\n"
"xm16 172,140,108\nxm16 176,144,108\nxm16 180,148,108\nxm16 184,152,108\nxm16 188,156,108\nxam 62,0x10000\nxwl 3\nxm16 0,48,32\nxdl 0,16,0,0\n"
"xrt 112,8,40960\nxm16 4,52,32\nxrt 114,8,41984\nxm16 8,56,32\nxrt 116,9,40960\nxm16 12,60,32\nxrt 118,9,41984\nxm16 16,64,32\nxam 62,0x10400\n"
"xm16 20,68,32\nxdl 1,16,s74,0\nxrt 120,10,40960\nxm16 24,72,32\nxrt 122,10,41984\nxm16 28,76,32\nxrt 124,11,40960\nxm16 32,80,32\nxm16 36,84,32\n"
"xrt 126,11,41984\nxam 62,0x10800\nxm16 40,88,32\nxdl 0,16,s75,0\nxrt 128,12,40960\nxm16 44,92,32\nxrt 130,12,41984\nxwl 12\nxm16 48,48,36\n"
"xrt 132,13,40960\nxm16 52,52,36\nxm16 56,56,36\nxrt 134,13,41984\nxam 62,0x10c00\nxm16 60,60,36\nxdl 1,16,s76,0\ns_add_u32 s16,s16,0x80\n"
"s_addc_u32 s17,s17,0\ns_sub_u32 s18,s18,0x80\nxrt 136,14,40960\nxm16 64,64,36\nxrt 138,14,41984\nxm16 68,68,36\nxm16 72,72,36\nxrt 140,15,40960\n"
"xm16 76,76,36\nxrt 142,15,41984\nxm16 80,80,36\nxrt 144,16,24576\nxam 64,0x1c000\nxm16 84,84,36\nxdl 4,24,0,0\nxrt 146,16,25600\nxm16 88,88,36\n"
"xm16 92,92,36\nxrt 148,17,24576\nxwl 15\nxm16 96,48,40\nxrt 150,17,25600\nxm16 100,52,40\nxrt 152,18,24576\nxam 64,0x1c400\nxm16 104,56,40\n"
"xdl 4,24,s75,0\nxm16 108,60,40\nxrt 154,18,25600\nxm16 112,64,40\nxrt 156,19,24576\nxm16 116,68,40\nxrt 158,19,25600\nxm16 120,72,40\nxrb 96,7,16384\n"
"xam 64,0x1c800\nxm16 124,76,40\nxdl 5,24,s84,0\nxm16 128,80,40\nxrb 100,7,18432\nxm16 132,84,40\nxrb 104,7,20480\nxm16 136,88,40\nxrb 108,7,22528\n"
"xm16 140,92,40\nxam 64,0x1cc00\nxm16 144,48,44\nxdl 5,24,s85,0\ns_add_u32 s24,s24,0x38000\ns_addc_u32 s25,s25,0\ns_sub_u32 s26,s26,0x38000\n"
"xm16 148,52,44\nxm16 152,56,44\nxm16 156,60,44\nxm16 160,64,44\nxm16 164,68,44\nxm16 168,72,44\nxm16 172,76,44\nxm16 176,80,44\nxm16 180,84,44\n"
"xm16 184,88,44\nxm16 188,92,44\ns_waitcnt vmcnt(8) lgkmcnt(0)\ns_barrier\nxam 63,0x8000\nxm16 0,112,96\nxdl 2,20,0,0\nxrt 48,8,0\nxm16 4,116,96\n"
"xrt 50,8,1024\nxm16 8,120,96\nxrt 52,9,0\nxm16 12,124,96\nxrt 54,9,1024\nxm16 16,128,96\nxam 63,0x8400\nxm16 20,132,96\nxdl 2,20,s77,0\nxrt 56,10,0\n"
"xm16 24,136,96\nxrt 58,10,1024\nxm16 28,140,96\nxrt 60,11,0\nxm16 32,144,96\nxm16 36,148,96\nxrt 62,11,1024\nxam 63,0x8800\nxm16 40,152,96\n"
"xdl 3,20,s78,0\nxrt 64,12,0\nxm16 44,156,96\nxrt 66,12,1024\nxm16 48,112,100\nxrt 68,13,0\nxm16 52,116,100\nxm16 56,120,100\nxrt 70,13,1024\n"
"xm16 60,124,100\nxrt 72,14,0\nxam 63,0x8c00\nxm16 64,128,100\nxdl 3,20,s79,0\nxrt 74,14,1024\nxm16 68,132,100\nxm16 72,136,100\nxrt 76,15,0\n"
"xm16 76,140,100\nxrt 78,15,1024\nxm16 80,144,100\nxrt 80,16,32768\nxam 63,0x9000\nxm16 84,148,100\nxdl 2,20,s80,0\nxrt 82,16,33792\nxm16 88,152,100\n"
"xm16 92,156,100\nxrt 84,17,32768\nxm16 96,112,104\nxrt 86,17,33792\nxm16 100,116,104\nxrt 88,18,32768\nxam 63,0x9400\nxm16 104,120,104\n"
"xdl 2,20,s81,0\nxm16 108,124,104\nxrt 90,18,33792\nxm16 112,128,104\nxrt 92,19,32768\nxm16 116,132,104\nxrt 94,19,33792\nxm16 120,136,104\n"
"xrb 32,6,32768\nxm16 124,140,104\nxam 63,0x9800\nxm16 128,144,104\nxdl 3,20,s82,0\nxrb 36,6,34816\nxm16 132,148,104\nxrb 40,6,36864\n"
"xm16 136,152,104\nxrb 44,6,38912\nxm16 140,156,104\nxm16 144,112,108\nxam 63,0x9c00\nxm16 148,116,108\nxdl 3,20,s83,0\ns_add_u32 s20,s20,0x80000\n"
"s_addc_u32 s21,s21,0\ns_sub_u32 s22,s22,0x80000\nxm16 152,120,108\nxm16 156,124,108\nxm16 160,128,108\nxm16 164,132,108\nxm16 168,136,108\n"
"xm16 172,140,108\nxm16 176,144,108\nxm16 180,148,108\nxm16 184,152,108\nxm16 188,156,108\nxam 62,0x14000\nxwl 3\nxm16 0,48,32\nxdl 0,16,0,0\n"
"xrt 112,8,8192\nxm16 4,52,32\nxrt 114,8,9216\nxm16 8,56,32\nxrt 116,9,8192\nxm16 12,60,32\nxrt 118,9,9216\nxm16 16,64,32\nxam 62,0x14400\n"
"xm16 20,68,32\nxdl 1,16,s74,0\nxrt 120,10,8192\nxm16 24,72,32\nxrt 122,10,9216\nxm16 28,76,32\nxrt 124,11,8192\nxm16 32,80,32\nxm16 36,84,32\n"
"xrt 126,11,9216\nxam 62,0x14800\nxm16 40,88,32\nxdl 0,16,s75,0\nxrt 128,12,8192\nxm16 44,92,32\nxrt 130,12,9216\nxwl 12\nxm16 48,48,36\n"
"xrt 132,13,8192\nxm16 52,52,36\nxm16 56,56,36\nxrt 134,13,9216\nxam 62,0x14c00\nxm16 60,60,36\nxdl 1,16,s76,0\ns_add_u32 s16,s16,0x80\n"
"s_addc_u32 s17,s17,0\ns_sub_u32 s18,s18,0x80\nxrt 136,14,8192\nxm16 64,64,36\nxrt 138,14,9216\nxm16 68,68,36\nxm16 72,72,36\nxrt 140,15,8192\n"
"xm16 76,76,36\nxrt 142,15,9216\nxm16 80,80,36\nxrt 144,16,40960\nxam 64,0x20000\nxm16 84,84,36\nxdl 4,24,0,0\nxrt 146,16,41984\nxm16 88,88,36\n"
"xm16 92,92,36\nxrt 148,17,40960\nxwl 15\nxm16 96,48,40\nxrt 150,17,41984\nxm16 100,52,40\nxrt 152,18,40960\nxam 64,0x20400\nxm16 104,56,40\n"
"xdl 4,24,s75,0\nxm16 108,60,40\nxrt 154,18,41984\nxm16 112,64,40\nxrt 156,19,40960\nxm16 116,68,40\nxrt 158,19,41984\nxm16 120,72,40\nxrb 96,7,32768\n"
"xam 64,0x20800\nxm16 124,76,40\nxdl 5,24,s84,0\nxm16 128,80,40\nxrb 100,7,34816\nxm16 132,84,40\nxrb 104,7,36864\nxm16 136,88,40\nxrb 108,7,38912\n"
"xm16 140,92,40\nxam 64,0x20c00\nxm16 144,48,44\nxdl 5,24,s85,0\ns_add_u32 s24,s24,0x38000\ns_addc_u32 s25,s25,0\ns_sub_u32 s26,s26,0x38000\n"
"xm16 148,52,44\nxm16 152,56,44\nxm16 156,60,44\nxm16 160,64,44\nxm16 164,68,44\nxm16 168,72,44\nxm16 172,76,44\nxm16 176,80,44\nxm16 180,84,44\n"
"xm16 184,88,44\nxm16 188,92,44\ns_waitcnt vmcnt(8) lgkmcnt(0)\ns_barrier\nxam 63,0\nxm16 0,112,96\nxdl 2,20,0,0\nxrt 48,8,32768\nxm16 4,116,96\n"
"xrt 50,8,33792\nxm16 8,120,96\nxrt 52,9,32768\nxm16 12,124,96\nxrt 54,9,33792\nxm16 16,128,96\nxam 63,0x400\nxm16 20,132,96\nxdl 2,20,s77,0\n"
"xrt 56,10,32768\nxm16 24,136,96\nxrt 58,10,33792\nxm16 28,140,96\nxrt 60,11,32768\nxm16 32,144,96\nxm16 36,148,96\nxrt 62,11,33792\nxam 63,0x800\n"
"xm16 40,152,96\nxdl 3,20,s78,0\nxrt 64,12,32768\nxm16 44,156,96\nxrt 66,12,33792\nxm16 48,112,100\nxrt 68,13,32768\nxm16 52,116,100\nxm16 56,120,100\n"
"xrt 70,13,33792\nxm16 60,124,100\nxrt 72,14,32768\nxam 63,0xc00\nxm16 64,128,100\nxdl 3,20,s79,0\nxrt 74,14,33792\nxm16 68,132,100\nxm16 72,136,100\n"
"xrt 76,15,32768\nxm16 76,140,100\nxrt 78,15,33792\nxm16 80,144,100\nxrt 80,16,0\nxam 63,0x1000\nxm16 84,148,100\nxdl 2,20,s80,0\nxrt 82,16,1024\n"
"xm16 88,152,100\nxm16 92,156,100\nxrt 84,17,0\nxm16 96,112,104\nxrt 86,17,1024\nxm16 100,116,104\nxrt 88,18,0\nxam 63,0x1400\nxm16 104,120,104\n"
"xdl 2,20,s81,0\nxm16 108,124,104\nxrt 90,18,1024\nxm16 112,128,104\nxrt 92,19,0\nxm16 116,132,104\nxrt 94,19,1024\nxm16 120,136,104\nxrb 32,6,0\n"
"xm16 124,140,104\nxam 63,0x1800\nxm16 128,144,104\nxdl 3,20,s82,0\nxrb 36,6,2048\nxm16 132,148,104\nxrb 40,6,4096\nxm16 136,152,104\nxrb 44,6,6144\n"
"xm16 140,156,104\nxm16 144,112,108\nxam 63,0x1c00\nxm16 148,116,108\nxdl 3,20,s83,0\ns_add_u32 s20,s20,0x80000\ns_addc_u32 s21,s21,0\n"
"s_sub_u32 s22,s22,0x80000\nxm16 152,120,108\nxm16 156,124,108\nxm16 160,128,108\nxm16 164,132,108\nxm16 168,136,108\nxm16 172,140,108\n"
"xm16 176,144,108\nxm16 180,148,108\nxm16 184,152,108\nxm16 188,156,108\nxam 62,0x18000\nxwl 3\nxm16 0,48,32\nxdl 0,16,0,0\nxrt 112,8,40960\n"
"xm16 4,52,32\nxrt 114,8,41984\nxm16 8,56,32\nxrt 116,9,40960\nxm16 12,60,32\nxrt 118,9,41984\nxm16 16,64,32\nxam 62,0x18400\nxm16 20,68,32\n"
"xdl 1,16,s74,0\nxrt 120,10,40960\nxm16 24,72,32\nxrt 122,10,41984\nxm16 28,76,32\nxrt 124,11,40960\nxm16 32,80,32\nxm16 36,84,32\nxrt 126,11,41984\n"
"xam 62,0x18800\nxm16 40,88,32\nxdl 0,16,s75,0\nxrt 128,12,40960\nxm16 44,92,32\nxrt 130,12,41984\nxwl 12\nxm16 48,48,36\nxrt 132,13,40960\n"
"xm16 52,52,36\nxm16 56,56,36\nxrt 134,13,41984\nxam 62,0x18c00\nxm16 60,60,36\nxdl 1,16,s76,0\ns_add_u32 s16,s16,0x80\ns_addc_u32 s17,s17,0\n"
"s_sub_u32 s18,s18,0x80\nxrt 136,14,40960\nxm16 64,64,36\nxrt 138,14,41984\nxm16 68,68,36\nxm16 72,72,36\nxrt 140,15,40960\nxm16 76,76,36\n"
"xrt 142,15,41984\nxm16 80,80,36\nxrt 144,16,8192\nxam 64,0x24000\nxm16 84,84,36\nxdl 4,24,0,0\nxrt 146,16,9216\nxm16 88,88,36\nxm16 92,92,36\n"
"xrt 148,17,8192\nxwl 15\nxm16 96,48,40\nxrt 150,17,9216\nxm16 100,52,40\nxrt 152,18,8192\nxam 64,0x24400\nxm16 104,56,40\nxdl 4,24,s75,0\n"
"xm16 108,60,40\nxrt 154,18,9216\nxm16 112,64,40\nxrt 156,19,8192\nxm16 116,68,40\nxrt 158,19,9216\nxm16 120,72,40\nxrb 96,7,0\nxam 64,0x24800\n"
"xm16 124,76,40\nxdl 5,24,s84,0\nxm16 128,80,40\nxrb 100,7,2048\nxm16 132,84,40\nxrb 104,7,4096\nxm16 136,88,40\nxrb 108,7,6144\nxm16 140,92,40\n"
"xam 64,0x24c00\nxm16 144,48,44\nxdl 5,24,s85,0\ns_add_u32 s24,s24,0x38000\ns_addc_u32 s25,s25,0\ns_sub_u32 s26,s26,0x38000\nxm16 148,52,44\n"
"xm16 152,56,44\nxm16 156,60,44\nxm16 160,64,44\nxm16 164,68,44\nxm16 168,72,44\nxm16 172,76,44\nxm16 176,80,44\nxm16 180,84,44\nxm16 184,88,44\n"
"xm16 188,92,44\ns_waitcnt vmcnt(8) lgkmcnt(0)\ns_barrier\nxam 63,0x8000\nxm16 0,112,96\nxdl 2,20,0,0\nxrt 48,8,0\nxm16 4,116,96\nxrt 50,8,1024\n"
"xm16 8,120,96\nxrt 52,9,0\nxm16 12,124,96\nxrt 54,9,1024\nxm16 16,128,96\nxam 63,0x8400\nxm16 20,132,96\nxdl 2,20,s77,0\nxrt 56,10,0\nxm16 24,136,96\n"
"xrt 58,10,1024\nxm16 28,140,96\nxrt 60,11,0\nxm16 32,144,96\nxm16 36,148,96\nxrt 62,11,1024\nxam 63,0x8800\nxm16 40,152,96\nxdl 3,20,s78,0\n"
"xrt 64,12,0\nxm16 44,156,96\nxrt 66,12,1024\nxm16 48,112,100\nxrt 68,13,0\nxm16 52,116,100\nxm16 56,120,100\nxrt 70,13,1024\nxm16 60,124,100\n"
"xrt 72,14,0\nxam 63,0x8c00\nxm16 64,128,100\nxdl 3,20,s79,0\nxrt 74,14,1024\nxm16 68,132,100\nxm16 72,136,100\nxrt 76,15,0\nxm16 76,140,100\n"
"xrt 78,15,1024\nxm16 80,144,100\nxrt 80,16,16384\nxam 63,0x9000\nxm16 84,148,100\nxdl 2,20,s80,0\nxrt 82,16,17408\nxm16 88,152,100\nxm16 92,156,100\n"
"xrt 84,17,16384\nxm16 96,112,104\nxrt 86,17,17408\nxm16 100,116,104\nxrt 88,18,16384\nxam 63,0x9400\nxm16 104,120,104\nxdl 2,20,s81,0\n"
"xm16 108,124,104\nxrt 90,18,17408\nxm16 112,128,104\nxrt 92,19,16384\nxm16 116,132,104\nxrt 94,19,17408\nxm16 120,136,104\nxrb 32,6,16384\n"
"xm16 124,140,104\nxam 63,0x9800\nxm16 128,144,104\nxdl 3,20,s82,0\nxrb 36,6,18432\nxm16 132,148,104\nxrb 40,6,20480\nxm16 136,152,104\n"
"xrb 44,6,22528\nxm16 140,156,104\nxm16 144,112,108\nxam 63,0x9c00\nxm16 148,116,108\nxdl 3,20,s83,0\ns_add_u32 s20,s20,0x80000\ns_addc_u32 s21,s21,0\n"
"s_sub_u32 s22,s22,0x80000\nxm16 152,120,108\nxm16 156,124,108\nxm16 160,128,108\nxm16 164,132,108\nxm16 168,136,108\nxm16 172,140,108\n"
"xm16 176,144,108\nxm16 180,148,108\nxm16 184,152,108\nxm16 188,156,108\nxam 62,0x10000\nxwl 3\nxm16 0,48,32\nxdl 0,16,0,0\nxrt 112,8,8192\n"
"xm16 4,52,32\nxrt 114,8,9216\nxm16 8,56,32\nxrt 116,9,8192\nxm16 12,60,32\nxrt 118,9,9216\nxm16 16,64,32\nxam 62,0x10400\nxm16 20,68,32\n"
"xdl 1,16,s74,0\nxrt 120,10,8192\nxm16 24,72,32\nxrt 122,10,9216\nxm16 28,76,32\nxrt 124,11,8192\nxm16 32,80,32\nxm16 36,84,32\nxrt 126,11,9216\n"
"xam 62,0x10800\nxm16 40,88,32\nxdl 0,16,s75,0\nxrt 128,12,8192\nxm16 44,92,32\nxrt 130,12,9216\nxwl 12\nxm16 48,48,36\nxrt 132,13,8192\n"
"xm16 52,52,36\nxm16 56,56,36\nxrt 134,13,9216\nxam 62,0x10c00\nxm16 60,60,36\nxdl 1,16,s76,0\ns_add_u32 s16,s16,0x80\ns_addc_u32 s17,s17,0\n"
"s_sub_u32 s18,s18,0x80\nxrt 136,14,8192\nxm16 64,64,36\nxrt 138,14,9216\nxm16 68,68,36\nxm16 72,72,36\nxrt 140,15,8192\nxm16 76,76,36\n"
"xrt 142,15,9216\nxm16 80,80,36\nxrt 144,16,24576\nxam 64,0x1c000\nxm16 84,84,36\nxdl 4,24,0,0\nxrt 146,16,25600\nxm16 88,88,36\nxm16 92,92,36\n"
"xrt 148,17,24576\nxwl 15\nxm16 96,48,40\nxrt 150,17,25600\nxm16 100,52,40\nxrt 152,18,24576\nxam 64,0x1c400\nxm16 104,56,40\nxdl 4,24,s75,0\n"
"xm16 108,60,40\nxrt 154,18,25600\nxm16 112,64,40\nxrt 156,19,24576\nxm16 116,68,40\nxrt 158,19,25600\nxm16 120,72,40\nxrb 96,7,16384\nxam 64,0x1c800\n"
"xm16 124,76,40\nxdl 5,24,s84,0\nxm16 128,80,40\nxrb 100,7,18432\nxm16 132,84,40\nxrb 104,7,20480\nxm16 136,88,40\nxrb 108,7,22528\nxm16 140,92,40\n"
"xam 64,0x1cc00\nxm16 144,48,44\nxdl 5,24,s85,0\ns_add_u32 s24,s24,0x38000\ns_addc_u32 s25,s25,0\ns_sub_u32 s26,s26,0x38000\nxm16 148,52,44\n"
"xm16 152,56,44\nxm16 156,60,44\nxm16 160,64,44\nxm16 164,68,44\nxm16 168,72,44\nxm16 172,76,44\nxm16 176,80,44\nxm16 180,84,44\nxm16 184,88,44\n"
"xm16 188,92,44\ns_waitcnt vmcnt(8) lgkmcnt(0)\ns_barrier\nxam 63,0\nxm16 0,112,96\nxdl 2,20,0,0\nxrt 48,8,32768\nxm16 4,116,96\nxrt 50,8,33792\n"
"xm16 8,120,96\nxrt 52,9,32768\nxm16 12,124,96\nxrt 54,9,33792\nxm16 16,128,96\nxam 63,0x400\nxm16 20,132,96\nxdl 2,20,s77,0\nxrt 56,10,32768\n"
"xm16 24,136,96\nxrt 58,10,33792\nxm16 28,140,96\nxrt 60,11,32768\nxm16 32,144,96\nxm16 36,148,96\nxrt 62,11,33792\nxam 63,0x800\nxm16 40,152,96\n"
"xdl 3,20,s78,0\nxrt 64,12,32768\nxm16 44,156,96\nxrt 66,12,33792\nxm16 48,112,100\nxrt 68,13,32768\nxm16 52,116,100\nxm16 56,120,100\n"
"xrt 70,13,33792\nxm16 60,124,100\nxrt 72,14,32768\nxam 63,0xc00\nxm16 64,128,100\nxdl 3,20,s79,0\nxrt 74,14,33792\nxm16 68,132,100\nxm16 72,136,100\n"
"xrt 76,15,32768\nxm16 76,140,100\nxrt 78,15,33792\nxm16 80,144,100\nxrt 80,16,32768\nxam 63,0x1000\nxm16 84,148,100\nxdl 2,20,s80,0\nxrt 82,16,33792\n"
"xm16 88,152,100\nxm16 92,156,100\nxrt 84,17,32768\nxm16 96,112,104\nxrt 86,17,33792\nxm16 100,116,104\nxrt 88,18,32768\nxam 63,0x1400\n"
"xm16 104,120,104\nxdl 2,20,s81,0\nxm16 108,124,104\nxrt 90,18,33792\nxm16 112,128,104\nxrt 92,19,32768\nxm16 116,132,104\nxrt 94,19,33792\n"
"xm16 120,136,104\nxrb 32,6,32768\nxm16 124,140,104\nxam 63,0x1800\nxm16 128,144,104\nxdl 3,20,s82,0\nxrb 36,6,34816\nxm16 132,148,104\n"
"xrb 40,6,36864\nxm16 136,152,104\nxrb 44,6,38912\nxm16 140,156,104\nxm16 144,112,108\nxam 63,0x1c00\nxm16 148,116,108\nxdl 3,20,s83,0\n"
"s_add_u32 s20,s20,0x80000\ns_addc_u32 s21,s21,0\ns_sub_u32 s22,s22,0x80000\nxm16 152,120,108\nxm16 156,124,108\nxm16 160,128,108\nxm16 164,132,108\n"
"xm16 168,136,108\nxm16 172,140,108\nxm16 176,144,108\nxm16 180,148,108\nxm16 184,152,108\nxm16 188,156,108\nxwl 3\nxm16 0,48,32\nxrt 112,8,40960\n"
"xm16 4,52,32\nxrt 114,8,41984\nxm16 8,56,32\nxrt 116,9,40960\nxm16 12,60,32\nxrt 118,9,41984\nxm16 16,64,32\nxm16 20,68,32\nxrt 120,10,40960\n"
"xm16 24,72,32\nxrt 122,10,41984\nxm16 28,76,32\nxrt 124,11,40960\nxm16 32,80,32\nxm16 36,84,32\nxrt 126,11,41984\nxm16 40,88,32\nxrt 128,12,40960\n"
"xm16 44,92,32\nxrt 130,12,41984\nxwl 12\nxm16 48,48,36\nxrt 132,13,40960\nxm16 52,52,36\nxm16 56,56,36\nxrt 134,13,41984\nxm16 60,60,36\n"
"xrt 136,14,40960\nxm16 64,64,36\nxrt 138,14,41984\nxm16 68,68,36\nxm16 72,72,36\nxrt 140,15,40960\nxm16 76,76,36\nxrt 142,15,41984\nxm16 80,80,36\n"
"xrt 144,16,40960\nxm16 84,84,36\nxrt 146,16,41984\nxm16 88,88,36\nxm16 92,92,36\nxrt 148,17,40960\nxwl 15\nxm16 96,48,40\nxrt 150,17,41984\n"
"xm16 100,52,40\nxrt 152,18,40960\nxm16 104,56,40\nxm16 108,60,40\nxrt 154,18,41984\nxm16 112,64,40\nxrt 156,19,40960\nxm16 116,68,40\n"
"xrt 158,19,41984\nxm16 120,72,40\nxrb 96,7,32768\nxm16 124,76,40\nxm16 128,80,40\nxrb 100,7,34816\nxm16 132,84,40\nxrb 104,7,36864\nxm16 136,88,40\n"
"xrb 108,7,38912\nxm16 140,92,40\nxm16 144,48,44\nxm16 148,52,44\nxm16 152,56,44\nxm16 156,60,44\nxm16 160,64,44\nxm16 164,68,44\nxm16 168,72,44\n"
"xm16 172,76,44\nxm16 176,80,44\nxm16 180,84,44\nxm16 184,88,44\nxm16 188,92,44\ns_waitcnt vmcnt(0) lgkmcnt(0)\ns_barrier\nxm16 0,112,96\nxrt 48,8,0\n"
"xm16 4,116,96\nxrt 50,8,1024\nxm16 8,120,96\nxrt 52,9,0\nxm16 12,124,96\nxrt 54,9,1024\nxm16 16,128,96\nxm16 20,132,96\nxrt 56,10,0\nxm16 24,136,96\n"
"xrt 58,10,1024\nxm16 28,140,96\nxrt 60,11,0\nxm16 32,144,96\nxm16 36,148,96\nxrt 62,11,1024\nxm16 40,152,96\nxrt 64,12,0\nxm16 44,156,96\n"
"xrt 66,12,1024\nxm16 48,112,100\nxrt 68,13,0\nxm16 52,116,100\nxm16 56,120,100\nxrt 70,13,1024\nxm16 60,124,100\nxrt 72,14,0\nxm16 64,128,100\n"
"xrt 74,14,1024\nxm16 68,132,100\nxm16 72,136,100\nxrt 76,15,0\nxm16 76,140,100\nxrt 78,15,1024\nxm16 80,144,100\nxrt 80,16,0\nxm16 84,148,100\n"
"xrt 82,16,1024\nxm16 88,152,100\nxm16 92,156,100\nxrt 84,17,0\nxm16 96,112,104\nxrt 86,17,1024\nxm16 100,116,104\nxrt 88,18,0\nxm16 104,120,104\n"
"xm16 108,124,104\nxrt 90,18,1024\nxm16 112,128,104\nxrt 92,19,0\nxm16 116,132,104\nxrt 94,19,1024\nxm16 120,136,104\nxrb 32,6,0\nxm16 124,140,104\n"
"xm16 128,144,104\nxrb 36,6,2048\nxm16 132,148,104\nxrb 40,6,4096\nxm16 136,152,104\nxrb 44,6,6144\nxm16 140,156,104\nxm16 144,112,108\n"
"xm16 148,116,108\nxm16 152,120,108\nxm16 156,124,108\nxm16 160,128,108\nxm16 164,132,108\nxm16 168,136,108\nxm16 172,140,108\nxm16 176,144,108\n"
"xm16 180,148,108\nxm16 184,152,108\nxm16 188,156,108\nxwl 3\nxm16 0,48,32\nxrt 112,8,8192\nxm16 4,52,32\nxrt 114,8,9216\nxm16 8,56,32\n"
"xrt 116,9,8192\nxm16 12,60,32\nxrt 118,9,9216\nxm16 16,64,32\nxm16 20,68,32\nxrt 120,10,8192\nxm16 24,72,32\nxrt 122,10,9216\nxm16 28,76,32\n"
"xrt 124,11,8192\nxm16 32,80,32\nxm16 36,84,32\nxrt 126,11,9216\nxm16 40,88,32\nxrt 128,12,8192\nxm16 44,92,32\nxrt 130,12,9216\nxwl 12\n"
"xm16 48,48,36\nxrt 132,13,8192\nxm16 52,52,36\nxm16 56,56,36\nxrt 134,13,9216\nxm16 60,60,36\nxrt 136,14,8192\nxm16 64,64,36\nxrt 138,14,9216\n"
"xm16 68,68,36\nxm16 72,72,36\nxrt 140,15,8192\nxm16 76,76,36\nxrt 142,15,9216\nxm16 80,80,36\nxrt 144,16,8192\nxm16 84,84,36\nxrt 146,16,9216\n"
"xm16 88,88,36\nxm16 92,92,36\nxrt 148,17,8192\nxwl 15\nxm16 96,48,40\nxrt 150,17,9216\nxm16 100,52,40\nxrt 152,18,8192\nxm16 104,56,40\n"
"xm16 108,60,40\nxrt 154,18,9216\nxm16 112,64,40\nxrt 156,19,8192\nxm16 116,68,40\nxrt 158,19,9216\nxm16 120,72,40\nxrb 96,7,0\nxm16 124,76,40\n"
"xm16 128,80,40\nxrb 100,7,2048\nxm16 132,84,40\nxrb 104,7,4096\nxm16 136,88,40\nxrb 108,7,6144\nxm16 140,92,40\nxm16 144,48,44\nxm16 148,52,44\n"
"xm16 152,56,44\nxm16 156,60,44\nxm16 160,64,44\nxm16 164,68,44\nxm16 168,72,44\nxm16 172,76,44\nxm16 176,80,44\nxm16 180,84,44\nxm16 184,88,44\n"
"xm16 188,92,44\nxwl 0\ns_barrier\nxam 65,0x18000\nxm16 0,112,96\nxdl 160,28,0,0\nxam 65,0x18400\nxm16 4,116,96\nxdl 161,28,s86,0\nxam 65,0x18800\n"
"xm16 8,120,96\nxdl 160,28,s87,0\nxam 65,0x18c00\nxm16 12,124,96\nxdl 161,28,s88,0\nxam 65,0x19000\nxm16 16,128,96\nxdl 160,28,s89,0\nxm16 20,132,96\n"
"xam 65,0x19400\nxm16 24,136,96\nxdl 161,28,s90,0\nxam 65,0x19800\nxm16 28,140,96\nxdl 160,28,s91,0\nxam 65,0x19c00\nxm16 32,144,96\nxdl 161,28,s92,0\n"
"s_add_u32 s28,s28,0x80\ns_addc_u32 s29,s29,0\ns_sub_u32 s30,s30,0x80\nxam 66,0\nxm16 36,148,96\nxdl 162,32,0,0\nxam 66,0x400\nxm16 40,152,96\n"
"xdl 162,32,s93,0\nxm16 44,156,96\nxam 66,0x800\nxm16 48,112,100\nxdl 163,32,s94,0\nxam 66,0xc00\nxm16 52,116,100\nxdl 163,32,s95,0\nxam 66,0x1000\n"
"xm16 56,120,100\nxdl 162,32,s96,0\nxam 66,0x1400\nxm16 60,124,100\nxdl 162,32,s97,0\nxam 66,0x1800\nxm16 64,128,100\nxdl 163,32,s98,0\n"
"xm16 68,132,100\nxam 66,0x1c00\nxm16 72,136,100\nxdl 163,32,s99,0\ns_add_u32 s32,s32,0x80000\ns_addc_u32 s33,s33,0\ns_sub_u32 s34,s34,0x80000\n"
"xam 65,0x20000\nxm16 76,140,100\nxdl 160,28,0,0\nxam 65,0x20400\nxm16 80,144,100\nxdl 161,28,s86,0\nxam 65,0x20800\nxm16 84,148,100\n"
"xdl 160,28,s87,0\nxam 65,0x20c00\nxm16 88,152,100\nxdl 161,28,s88,0\nxm16 92,156,100\nxam 65,0x21000\nxm16 96,112,104\nxdl 160,28,s89,0\n"
"xam 65,0x21400\nxm16 100,116,104\nxdl 161,28,s90,0\nxam 65,0x21800\nxm16 104,120,104\nxdl 160,28,s91,0\nxam 65,0x21c00\nxm16 108,124,104\n"
"xdl 161,28,s92,0\ns_add_u32 s28,s28,0x80\ns_addc_u32 s29,s29,0\ns_sub_u32 s30,s30,0x80\nxam 66,0x8000\nxm16 112,128,104\nxdl 162,32,0,0\n"
"xm16 116,132,104\nxam 66,0x8400\nxm16 120,136,104\nxdl 162,32,s93,0\nxam 66,0x8800\nxm16 124,140,104\nxdl 163,32,s94,0\nxam 66,0x8c00\n"
"xm16 128,144,104\nxdl 163,32,s95,0\nxam 66,0x9000\nxm16 132,148,104\nxdl 162,32,s96,0\nxam 66,0x9400\nxm16 136,152,104\nxdl 162,32,s97,0\n"
"xm16 140,156,104\nxam 66,0x9800\nxm16 144,112,108\nxdl 163,32,s98,0\nxam 66,0x9c00\nxm16 148,116,108\nxdl 163,32,s99,0\ns_add_u32 s32,s32,0x80000\n"
"s_addc_u32 s33,s33,0\ns_sub_u32 s34,s34,0x80000\nxam 66,0x10000\nxm16 152,120,108\nxdl 162,32,0,0\nxam 66,0x10400\nxm16 156,124,108\n"
"xdl 162,32,s93,0\nxam 66,0x10800\nxm16 160,128,108\nxdl 163,32,s94,0\nxm16 164,132,108\nxam 66,0x10c00\nxm16 168,136,108\nxdl 163,32,s95,0\n"
"xam 66,0x11000\nxm16 172,140,108\nxdl 162,32,s96,0\nxam 66,0x11400\nxm16 176,144,108\nxdl 162,32,s97,0\nxam 66,0x11800\nxm16 180,148,108\n"
"xdl 163,32,s98,0\nxam 66,0x11c00\nxm16 184,152,108\nxdl 163,32,s99,0\ns_add_u32 s32,s32,0x80000\ns_addc_u32 s33,s33,0\ns_sub_u32 s34,s34,0x80000\n"
"xm16 188,156,108\nv_readfirstlane_b32 s6,v172\nv_readfirstlane_b32 s7,v173\ns_nop 3\ns_add_u32 s6,s6,1\ns_addc_u32 s7,s7,0\ns_nop 3\n"
"s_mov_b32 s72,0x3db8aa3b\nxar 32,0\nxar 33,1\nxar 34,2\nxar 35,3\nxar 36,4\nxar 37,5\nxar 38,6\nxar 39,7\nxmu 32,s72,32\nxmu 33,s72,33\n"
"xmu 34,s72,34\nxmu 35,s72,35\nxmu 36,s72,36\nxmu 37,s72,37\nxmu 38,s72,38\nxmu 39,s72,39\nxcv 48,32,33\nxcv 49,34,35\nxcv 50,36,37\nxcv 51,38,39\n"
"xst1 48,30,40,0,0\nxar 40,8\nxar 41,9\nxar 42,10\nxar 43,11\nxar 44,12\nxar 45,13\nxar 46,14\nxar 47,15\nxmu 40,s72,40\nxmu 41,s72,41\nxmu 42,s72,42\n"
"xmu 43,s72,43\nxmu 44,s72,44\nxmu 45,s72,45\nxmu 46,s72,46\nxmu 47,s72,47\nxcv 52,40,41\nxcv 53,42,43\nxcv 54,44,45\nxcv 55,46,47\n"
"xst1 52,30,40,0,2048\nxar 32,16\nxar 33,17\nxar 34,18\nxar 35,19\nxar 36,20\nxar 37,21\nxar 38,22\nxar 39,23\nxmu 32,s72,32\nxmu 33,s72,33\n"
"xmu 34,s72,34\nxmu 35,s72,35\nxmu 36,s72,36\nxmu 37,s72,37\nxmu 38,s72,38\nxmu 39,s72,39\nxcv 56,32,33\nxcv 57,34,35\nxcv 58,36,37\nxcv 59,38,39\n"
"s_mov_b32 s68,0x1000\nxst1 56,30,40,s68,0\nxar 40,24\nxar 41,25\nxar 42,26\nxar 43,27\nxar 44,28\nxar 45,29\nxar 46,30\nxar 47,31\nxmu 40,s72,40\n"
"xmu 41,s72,41\nxmu 42,s72,42\nxmu 43,s72,43\nxmu 44,s72,44\nxmu 45,s72,45\nxmu 46,s72,46\nxmu 47,s72,47\nxcv 60,40,41\nxcv 61,42,43\nxcv 62,44,45\n"
"xcv 63,46,47\nxst1 60,30,40,s68,2048\nxar 32,48\nxar 33,49\nxar 34,50\nxar 35,51\nxar 36,52\nxar 37,53\nxar 38,54\nxar 39,55\nxmu 32,s72,32\n"
"xmu 33,s72,33\nxmu 34,s72,34\nxmu 35,s72,35\nxmu 36,s72,36\nxmu 37,s72,37\nxmu 38,s72,38\nxmu 39,s72,39\nxcv 48,32,33\nxcv 49,34,35\nxcv 50,36,37\n"
"xcv 51,38,39\nxst1 48,30,40,0,256\nxar 40,56\nxar 41,57\nxar 42,58\nxar 43,59\nxar 44,60\nxar 45,61\nxar 46,62\nxar 47,63\nxmu 40,s72,40\n"
"xmu 41,s72,41\nxmu 42,s72,42\nxmu 43,s72,43\nxmu 44,s72,44\nxmu 45,s72,45\nxmu 46,s72,46\nxmu 47,s72,47\nxcv 52,40,41\nxcv 53,42,43\nxcv 54,44,45\n"
"xcv 55,46,47\nxst1 52,30,40,0,2304\nxar 32,64\nxar 33,65\nxar 34,66\nxar 35,67\nxar 36,68\nxar 37,69\nxar 38,70\nxar 39,71\nxmu 32,s72,32\n"
"xmu 33,s72,33\nxmu 34,s72,34\nxmu 35,s72,35\nxmu 36,s72,36\nxmu 37,s72,37\nxmu 38,s72,38\nxmu 39,s72,39\nxcv 56,32,33\nxcv 57,34,35\nxcv 58,36,37\n"
"xcv 59,38,39\nxst1 56,30,40,s68,256\nxar 40,72\nxar 41,73\nxar 42,74\nxar 43,75\nxar 44,76\nxar 45,77\nxar 46,78\nxar 47,79\nxmu 40,s72,40\n"
"xmu 41,s72,41\nxmu 42,s72,42\nxmu 43,s72,43\nxmu 44,s72,44\nxmu 45,s72,45\nxmu 46,s72,46\nxmu 47,s72,47\nxcv 60,40,41\nxcv 61,42,43\nxcv 62,44,45\n"
"xcv 63,46,47\nxst1 60,30,40,s68,2304\nxar 32,96\nxar 33,97\nxar 34,98\nxar 35,99\nxar 36,100\nxar 37,101\nxar 38,102\nxar 39,103\nxmu 32,s72,32\n"
"xmu 33,s72,33\nxmu 34,s72,34\nxmu 35,s72,35\nxmu 36,s72,36\nxmu 37,s72,37\nxmu 38,s72,38\nxmu 39,s72,39\nxcv 48,32,33\nxcv 49,34,35\nxcv 50,36,37\n"
"xcv 51,38,39\ns_mov_b32 s68,0x4000\nxst1 48,30,40,s68,0\nxar 40,104\nxar 41,105\nxar 42,106\nxar 43,107\nxar 44,108\nxar 45,109\nxar 46,110\n"
"xar 47,111\nxmu 40,s72,40\nxmu 41,s72,41\nxmu 42,s72,42\nxmu 43,s72,43\nxmu 44,s72,44\nxmu 45,s72,45\nxmu 46,s72,46\nxmu 47,s72,47\nxcv 52,40,41\n"
"xcv 53,42,43\nxcv 54,44,45\nxcv 55,46,47\nxst1 52,30,40,s68,2048\nxar 32,112\nxar 33,113\nxar 34,114\nxar 35,115\nxar 36,116\nxar 37,117\nxar 38,118\n"
"xar 39,119\nxmu 32,s72,32\nxmu 33,s72,33\nxmu 34,s72,34\nxmu 35,s72,35\nxmu 36,s72,36\nxmu 37,s72,37\nxmu 38,s72,38\nxmu 39,s72,39\nxcv 56,32,33\n"
"xcv 57,34,35\nxcv 58,36,37\nxcv 59,38,39\ns_mov_b32 s68,0x5000\nxst1 56,30,40,s68,0\nxar 40,120\nxar 41,121\nxar 42,122\nxar 43,123\nxar 44,124\n"
"xar 45,125\nxar 46,126\nxar 47,127\nxmu 40,s72,40\nxmu 41,s72,41\nxmu 42,s72,42\nxmu 43,s72,43\nxmu 44,s72,44\nxmu 45,s72,45\nxmu 46,s72,46\n"
"xmu 47,s72,47\nxcv 60,40,41\nxcv 61,42,43\nxcv 62,44,45\nxcv 63,46,47\nxst1 60,30,40,s68,2048\nxar 32,144\nxar 33,145\nxar 34,146\nxar 35,147\n"
"xar 36,148\nxar 37,149\nxar 38,150\nxar 39,151\nxmu 32,s72,32\nxmu 33,s72,33\nxmu 34,s72,34\nxmu 35,s72,35\nxmu 36,s72,36\nxmu 37,s72,37\n"
"xmu 38,s72,38\nxmu 39,s72,39\nxcv 48,32,33\nxcv 49,34,35\nxcv 50,36,37\nxcv 51,38,39\ns_mov_b32 s68,0x4000\nxst1 48,30,40,s68,256\nxar 40,152\n"
"xar 41,153\nxar 42,154\nxar 43,155\nxar 44,156\nxar 45,157\nxar 46,158\nxar 47,159\nxmu 40,s72,40\nxmu 41,s72,41\nxmu 42,s72,42\nxmu 43,s72,43\n"
"xmu 44,s72,44\nxmu 45,s72,45\nxmu 46,s72,46\nxmu 47,s72,47\nxcv 52,40,41\nxcv 53,42,43\nxcv 54,44,45\nxcv 55,46,47\nxst1 52,30,40,s68,2304\n"
"xar 32,160\nxar 33,161\nxar 34,162\nxar 35,163\nxar 36,164\nxar 37,165\nxar 38,166\nxar 39,167\nxmu 32,s72,32\nxmu 33,s72,33\nxmu 34,s72,34\n"
"xmu 35,s72,35\nxmu 36,s72,36\nxmu 37,s72,37\nxmu 38,s72,38\nxmu 39,s72,39\nxcv 56,32,33\nxcv 57,34,35\nxcv 58,36,37\nxcv 59,38,39\n"
"s_mov_b32 s68,0x5000\nxst1 56,30,40,s68,256\nxar 40,168\nxar 41,169\nxar 42,170\nxar 43,171\nxar 44,172\nxar 45,173\nxar 46,174\nxar 47,175\n"
"xmu 40,s72,40\nxmu 41,s72,41\nxmu 42,s72,42\nxmu 43,s72,43\nxmu 44,s72,44\nxmu 45,s72,45\nxmu 46,s72,46\nxmu 47,s72,47\nxcv 60,40,41\nxcv 61,42,43\n"
"xcv 62,44,45\nxcv 63,46,47\nxst1 60,30,40,s68,2304\nv_mov_b32 v64,0xc0135761\ns_mov_b32 s72,0xbdd2d3e8\nxar 32,32\nxar 33,33\nxar 34,34\nxar 35,35\n"
"xar 36,36\nxar 37,37\nxar 38,38\nxar 39,39\nxmu 48,v32,32\nxmu 49,v33,33\nxmu 50,v34,34\nxmu 51,v35,35\nxmu 52,v36,36\nxmu 53,v37,37\nxmu 54,v38,38\n"
"xmu 55,v39,39\nxfm 48,v48,s72,v64\nxfm 49,v49,s72,v64\nxfm 50,v50,s72,v64\nxfm 51,v51,s72,v64\nxfm 52,v52,s72,v64\nxfm 53,v53,s72,v64\n"
"xfm 54,v54,s72,v64\nxfm 55,v55,s72,v64\nxmu 48,v48,32\nxmu 49,v49,33\nxmu 50,v50,34\nxmu 51,v51,35\nxmu 52,v52,36\nxmu 53,v53,37\nxmu 54,v54,38\n"
"xmu 55,v55,39\nxex 48,48\nxex 49,49\nxex 50,50\nxex 51,51\nxex 52,52\nxex 53,53\nxex 54,54\nxex 55,55\nxad 48,1.0,48\nxad 49,1.0,49\nxad 50,1.0,50\n"
"xad 51,1.0,51\nxad 52,1.0,52\nxad 53,1.0,53\nxad 54,1.0,54\nxad 55,1.0,55\nxrc 48,48\nxrc 49,49\nxrc 50,50\nxrc 51,51\nxrc 52,52\nxrc 53,53\n"
"xrc 54,54\nxrc 55,55\nxmu 32,v32,48\nxmu 33,v33,49\nxmu 34,v34,50\nxmu 35,v35,51\nxmu 36,v36,52\nxmu 37,v37,53\nxmu 38,v38,54\nxmu 39,v39,55\n"
"xar 40,40\nxar 41,41\nxar 42,42\nxar 43,43\nxar 44,44\nxar 45,45\nxar 46,46\nxar 47,47\nxmu 56,v40,40\nxmu 57,v41,41\nxmu 58,v42,42\nxmu 59,v43,43\n"
"xmu 60,v44,44\nxmu 61,v45,45\nxmu 62,v46,46\nxmu 63,v47,47\nxfm 56,v56,s72,v64\nxfm 57,v57,s72,v64\nxfm 58,v58,s72,v64\nxfm 59,v59,s72,v64\n"
"xfm 60,v60,s72,v64\nxfm 61,v61,s72,v64\nxfm 62,v62,s72,v64\nxfm 63,v63,s72,v64\nxmu 56,v56,40\nxmu 57,v57,41\nxmu 58,v58,42\nxmu 59,v59,43\n"
"xmu 60,v60,44\nxmu 61,v61,45\nxmu 62,v62,46\nxmu 63,v63,47\nxex 56,56\nxex 57,57\nxex 58,58\nxex 59,59\nxex 60,60\nxex 61,61\nxex 62,62\nxex 63,63\n"
"xad 56,1.0,56\nxad 57,1.0,57\nxad 58,1.0,58\nxad 59,1.0,59\nxad 60,1.0,60\nxad 61,1.0,61\nxad 62,1.0,62\nxad 63,1.0,63\nxrc 56,56\nxrc 57,57\n"
"xrc 58,58\nxrc 59,59\nxrc 60,60\nxrc 61,61\nxrc 62,62\nxrc 63,63\nxmu 40,v40,56\nxmu 41,v41,57\nxmu 42,v42,58\nxmu 43,v43,59\nxmu 44,v44,60\n"
"xmu 45,v45,61\nxmu 46,v46,62\nxmu 47,v47,63\nxcv 192,32,33\nxcv 193,34,35\nxcv 194,36,37\nxcv 195,38,39\nxcv 196,40,41\nxcv 197,42,43\nxcv 198,44,45\n"
"xcv 199,46,47\nxad 84,v32,33\nxad 84,v84,34\nxad 84,v84,35\nxad 84,v84,36\nxad 84,v84,37\nxad 84,v84,38\nxad 84,v84,39\nxad 84,v84,40\nxad 84,v84,41\n"
"xad 84,v84,42\nxad 84,v84,43\nxad 84,v84,44\nxad 84,v84,45\nxad 84,v84,46\nxad 84,v84,47\nv_mov_b32 v85,v84\ns_nop 1\nv_permlane16_swap_b32 v84,v85\n"
"xad 84,v84,85\nv_mov_b32 v85,v84\ns_nop 1\nv_permlane32_swap_b32 v84,v85\nxad 84,v84,85\nxmu 84,0x3c800000,84\nv_sub_f32 v48,v32,v84\n"
"v_sub_f32 v49,v33,v84\nv_sub_f32 v50,v34,v84\nv_sub_f32 v51,v35,v84\nv_sub_f32 v52,v36,v84\nv_sub_f32 v53,v37,v84\nv_sub_f32 v54,v38,v84\n"
"v_sub_f32 v55,v39,v84\nv_sub_f32 v56,v40,v84\nv_sub_f32 v57,v41,v84\nv_sub_f32 v58,v42,v84\nv_sub_f32 v59,v43,v84\nv_sub_f32 v60,v44,v84\n"
"v_sub_f32 v61,v45,v84\nv_sub_f32 v62,v46,v84\nv_sub_f32 v63,v47,v84\nxmu 86,v48,48\nxfm 86,v49,v49,v86\nxfm 86,v50,v50,v86\nxfm 86,v51,v51,v86\n"
"xfm 86,v52,v52,v86\nxfm 86,v53,v53,v86\nxfm 86,v54,v54,v86\nxfm 86,v55,v55,v86\nxfm 86,v56,v56,v86\nxfm 86,v57,v57,v86\nxfm 86,v58,v58,v86\n"
"xfm 86,v59,v59,v86\nxfm 86,v60,v60,v86\nxfm 86,v61,v61,v86\nxfm 86,v62,v62,v86\nxfm 86,v63,v63,v86\nv_mov_b32 v85,v86\ns_nop 1\n"
"v_permlane16_swap_b32 v86,v85\nxad 86,v86,85\nv_mov_b32 v85,v86\ns_nop 1\nv_permlane32_swap_b32 v86,v85\nxad 86,v86,85\nv_mov_b32 v88,v84\n"
"v_mov_b32 v89,v86\nbuffer_store_dwordx2 v[88:89],v31,s[48:51],0 offen offset:0 sc1\nxar 32,80\nxar 33,81\nxar 34,82\nxar 35,83\nxar 36,84\nxar 37,85\n"
"xar 38,86\nxar 39,87\nxmu 48,v32,32\nxmu 49,v33,33\nxmu 50,v34,34\nxmu 51,v35,35\nxmu 52,v36,36\nxmu 53,v37,37\nxmu 54,v38,38\nxmu 55,v39,39\n"
"xfm 48,v48,s72,v64\nxfm 49,v49,s72,v64\nxfm 50,v50,s72,v64\nxfm 51,v51,s72,v64\nxfm 52,v52,s72,v64\nxfm 53,v53,s72,v64\nxfm 54,v54,s72,v64\n"
"xfm 55,v55,s72,v64\nxmu 48,v48,32\nxmu 49,v49,33\nxmu 50,v50,34\nxmu 51,v51,35\nxmu 52,v52,36\nxmu 53,v53,37\nxmu 54,v54,38\nxmu 55,v55,39\n"
"xex 48,48\nxex 49,49\nxex 50,50\nxex 51,51\nxex 52,52\nxex 53,53\nxex 54,54\nxex 55,55\nxad 48,1.0,48\nxad 49,1.0,49\nxad 50,1.0,50\nxad 51,1.0,51\n"
"xad 52,1.0,52\nxad 53,1.0,53\nxad 54,1.0,54\nxad 55,1.0,55\nxrc 48,48\nxrc 49,49\nxrc 50,50\nxrc 51,51\nxrc 52,52\nxrc 53,53\nxrc 54,54\nxrc 55,55\n"
"xmu 32,v32,48\nxmu 33,v33,49\nxmu 34,v34,50\nxmu 35,v35,51\nxmu 36,v36,52\nxmu 37,v37,53\nxmu 38,v38,54\nxmu 39,v39,55\nxar 40,88\nxar 41,89\n"
"xar 42,90\nxar 43,91\nxar 44,92\nxar 45,93\nxar 46,94\nxar 47,95\nxmu 56,v40,40\nxmu 57,v41,41\nxmu 58,v42,42\nxmu 59,v43,43\nxmu 60,v44,44\n"
"xmu 61,v45,45\nxmu 62,v46,46\nxmu 63,v47,47\nxfm 56,v56,s72,v64\nxfm 57,v57,s72,v64\nxfm 58,v58,s72,v64\nxfm 59,v59,s72,v64\nxfm 60,v60,s72,v64\n"
"xfm 61,v61,s72,v64\nxfm 62,v62,s72,v64\nxfm 63,v63,s72,v64\nxmu 56,v56,40\nxmu 57,v57,41\nxmu 58,v58,42\nxmu 59,v59,43\nxmu 60,v60,44\nxmu 61,v61,45\n"
"xmu 62,v62,46\nxmu 63,v63,47\nxex 56,56\nxex 57,57\nxex 58,58\nxex 59,59\nxex 60,60\nxex 61,61\nxex 62,62\nxex 63,63\nxad 56,1.0,56\nxad 57,1.0,57\n"
"xad 58,1.0,58\nxad 59,1.0,59\nxad 60,1.0,60\nxad 61,1.0,61\nxad 62,1.0,62\nxad 63,1.0,63\nxrc 56,56\nxrc 57,57\nxrc 58,58\nxrc 59,59\nxrc 60,60\n"
"xrc 61,61\nxrc 62,62\nxrc 63,63\nxmu 40,v40,56\nxmu 41,v41,57\nxmu 42,v42,58\nxmu 43,v43,59\nxmu 44,v44,60\nxmu 45,v45,61\nxmu 46,v46,62\n"
"xmu 47,v47,63\nxcv 200,32,33\nxcv 201,34,35\nxcv 202,36,37\nxcv 203,38,39\nxcv 204,40,41\nxcv 205,42,43\nxcv 206,44,45\nxcv 207,46,47\nxad 84,v32,33\n"
"xad 84,v84,34\nxad 84,v84,35\nxad 84,v84,36\nxad 84,v84,37\nxad 84,v84,38\nxad 84,v84,39\nxad 84,v84,40\nxad 84,v84,41\nxad 84,v84,42\nxad 84,v84,43\n"
"xad 84,v84,44\nxad 84,v84,45\nxad 84,v84,46\nxad 84,v84,47\nv_mov_b32 v85,v84\ns_nop 1\nv_permlane16_swap_b32 v84,v85\nxad 84,v84,85\n"
"v_mov_b32 v85,v84\ns_nop 1\nv_permlane32_swap_b32 v84,v85\nxad 84,v84,85\nxmu 84,0x3c800000,84\nv_sub_f32 v48,v32,v84\nv_sub_f32 v49,v33,v84\n"
"v_sub_f32 v50,v34,v84\nv_sub_f32 v51,v35,v84\nv_sub_f32 v52,v36,v84\nv_sub_f32 v53,v37,v84\nv_sub_f32 v54,v38,v84\nv_sub_f32 v55,v39,v84\n"
"v_sub_f32 v56,v40,v84\nv_sub_f32 v57,v41,v84\nv_sub_f32 v58,v42,v84\nv_sub_f32 v59,v43,v84\nv_sub_f32 v60,v44,v84\nv_sub_f32 v61,v45,v84\n"
"v_sub_f32 v62,v46,v84\nv_sub_f32 v63,v47,v84\nxmu 86,v48,48\nxfm 86,v49,v49,v86\nxfm 86,v50,v50,v86\nxfm 86,v51,v51,v86\nxfm 86,v52,v52,v86\n"
"xfm 86,v53,v53,v86\nxfm 86,v54,v54,v86\nxfm 86,v55,v55,v86\nxfm 86,v56,v56,v86\nxfm 86,v57,v57,v86\nxfm 86,v58,v58,v86\nxfm 86,v59,v59,v86\n"
"xfm 86,v60,v60,v86\nxfm 86,v61,v61,v86\nxfm 86,v62,v62,v86\nxfm 86,v63,v63,v86\nv_mov_b32 v85,v86\ns_nop 1\nv_permlane16_swap_b32 v86,v85\n"
"xad 86,v86,85\nv_mov_b32 v85,v86\ns_nop 1\nv_permlane32_swap_b32 v86,v85\nxad 86,v86,85\nv_mov_b32 v90,v84\nv_mov_b32 v91,v86\n"
"buffer_store_dwordx2 v[90:91],v31,s[48:51],0 offen offset:2048 sc1\nxar 32,128\nxar 33,129\nxar 34,130\nxar 35,131\nxar 36,132\nxar 37,133\n"
"xar 38,134\nxar 39,135\nxmu 48,v32,32\nxmu 49,v33,33\nxmu 50,v34,34\nxmu 51,v35,35\nxmu 52,v36,36\nxmu 53,v37,37\nxmu 54,v38,38\nxmu 55,v39,39\n"
"xfm 48,v48,s72,v64\nxfm 49,v49,s72,v64\nxfm 50,v50,s72,v64\nxfm 51,v51,s72,v64\nxfm 52,v52,s72,v64\nxfm 53,v53,s72,v64\nxfm 54,v54,s72,v64\n"
"xfm 55,v55,s72,v64\nxmu 48,v48,32\nxmu 49,v49,33\nxmu 50,v50,34\nxmu 51,v51,35\nxmu 52,v52,36\nxmu 53,v53,37\nxmu 54,v54,38\nxmu 55,v55,39\n"
"xex 48,48\nxex 49,49\nxex 50,50\nxex 51,51\nxex 52,52\nxex 53,53\nxex 54,54\nxex 55,55\nxad 48,1.0,48\nxad 49,1.0,49\nxad 50,1.0,50\nxad 51,1.0,51\n"
"xad 52,1.0,52\nxad 53,1.0,53\nxad 54,1.0,54\nxad 55,1.0,55\nxrc 48,48\nxrc 49,49\nxrc 50,50\nxrc 51,51\nxrc 52,52\nxrc 53,53\nxrc 54,54\nxrc 55,55\n"
"xmu 32,v32,48\nxmu 33,v33,49\nxmu 34,v34,50\nxmu 35,v35,51\nxmu 36,v36,52\nxmu 37,v37,53\nxmu 38,v38,54\nxmu 39,v39,55\nxar 40,136\nxar 41,137\n"
"xar 42,138\nxar 43,139\nxar 44,140\nxar 45,141\nxar 46,142\nxar 47,143\nxmu 56,v40,40\nxmu 57,v41,41\nxmu 58,v42,42\nxmu 59,v43,43\nxmu 60,v44,44\n"
"xmu 61,v45,45\nxmu 62,v46,46\nxmu 63,v47,47\nxfm 56,v56,s72,v64\nxfm 57,v57,s72,v64\nxfm 58,v58,s72,v64\nxfm 59,v59,s72,v64\nxfm 60,v60,s72,v64\n"
"xfm 61,v61,s72,v64\nxfm 62,v62,s72,v64\nxfm 63,v63,s72,v64\nxmu 56,v56,40\nxmu 57,v57,41\nxmu 58,v58,42\nxmu 59,v59,43\nxmu 60,v60,44\nxmu 61,v61,45\n"
"xmu 62,v62,46\nxmu 63,v63,47\nxex 56,56\nxex 57,57\nxex 58,58\nxex 59,59\nxex 60,60\nxex 61,61\nxex 62,62\nxex 63,63\nxad 56,1.0,56\nxad 57,1.0,57\n"
"xad 58,1.0,58\nxad 59,1.0,59\nxad 60,1.0,60\nxad 61,1.0,61\nxad 62,1.0,62\nxad 63,1.0,63\nxrc 56,56\nxrc 57,57\nxrc 58,58\nxrc 59,59\nxrc 60,60\n"
"xrc 61,61\nxrc 62,62\nxrc 63,63\nxmu 40,v40,56\nxmu 41,v41,57\nxmu 42,v42,58\nxmu 43,v43,59\nxmu 44,v44,60\nxmu 45,v45,61\nxmu 46,v46,62\n"
"xmu 47,v47,63\nxcv 208,32,33\nxcv 209,34,35\nxcv 210,36,37\nxcv 211,38,39\nxcv 212,40,41\nxcv 213,42,43\nxcv 214,44,45\nxcv 215,46,47\nxad 84,v32,33\n"
"xad 84,v84,34\nxad 84,v84,35\nxad 84,v84,36\nxad 84,v84,37\nxad 84,v84,38\nxad 84,v84,39\nxad 84,v84,40\nxad 84,v84,41\nxad 84,v84,42\nxad 84,v84,43\n"
"xad 84,v84,44\nxad 84,v84,45\nxad 84,v84,46\nxad 84,v84,47\nv_mov_b32 v85,v84\ns_nop 1\nv_permlane16_swap_b32 v84,v85\nxad 84,v84,85\n"
"v_mov_b32 v85,v84\ns_nop 1\nv_permlane32_swap_b32 v84,v85\nxad 84,v84,85\nxmu 84,0x3c800000,84\nv_sub_f32 v48,v32,v84\nv_sub_f32 v49,v33,v84\n"
"v_sub_f32 v50,v34,v84\nv_sub_f32 v51,v35,v84\nv_sub_f32 v52,v36,v84\nv_sub_f32 v53,v37,v84\nv_sub_f32 v54,v38,v84\nv_sub_f32 v55,v39,v84\n"
"v_sub_f32 v56,v40,v84\nv_sub_f32 v57,v41,v84\nv_sub_f32 v58,v42,v84\nv_sub_f32 v59,v43,v84\nv_sub_f32 v60,v44,v84\nv_sub_f32 v61,v45,v84\n"
"v_sub_f32 v62,v46,v84\nv_sub_f32 v63,v47,v84\nxmu 86,v48,48\nxfm 86,v49,v49,v86\nxfm 86,v50,v50,v86\nxfm 86,v51,v51,v86\nxfm 86,v52,v52,v86\n"
"xfm 86,v53,v53,v86\nxfm 86,v54,v54,v86\nxfm 86,v55,v55,v86\nxfm 86,v56,v56,v86\nxfm 86,v57,v57,v86\nxfm 86,v58,v58,v86\nxfm 86,v59,v59,v86\n"
"xfm 86,v60,v60,v86\nxfm 86,v61,v61,v86\nxfm 86,v62,v62,v86\nxfm 86,v63,v63,v86\nv_mov_b32 v85,v86\ns_nop 1\nv_permlane16_swap_b32 v86,v85\n"
"xad 86,v86,85\nv_mov_b32 v85,v86\ns_nop 1\nv_permlane32_swap_b32 v86,v85\nxad 86,v86,85\nv_mov_b32 v88,v84\nv_mov_b32 v89,v86\ns_mov_b32 s68,0x1000\n"
"buffer_store_dwordx2 v[88:89],v31,s[48:51],s68 offen offset:0 sc1\nxar 32,176\nxar 33,177\nxar 34,178\nxar 35,179\nxar 36,180\nxar 37,181\n"
"xar 38,182\nxar 39,183\nxmu 48,v32,32\nxmu 49,v33,33\nxmu 50,v34,34\nxmu 51,v35,35\nxmu 52,v36,36\nxmu 53,v37,37\nxmu 54,v38,38\nxmu 55,v39,39\n"
"xfm 48,v48,s72,v64\nxfm 49,v49,s72,v64\nxfm 50,v50,s72,v64\nxfm 51,v51,s72,v64\nxfm 52,v52,s72,v64\nxfm 53,v53,s72,v64\nxfm 54,v54,s72,v64\n"
"xfm 55,v55,s72,v64\nxmu 48,v48,32\nxmu 49,v49,33\nxmu 50,v50,34\nxmu 51,v51,35\nxmu 52,v52,36\nxmu 53,v53,37\nxmu 54,v54,38\nxmu 55,v55,39\n"
"xex 48,48\nxex 49,49\nxex 50,50\nxex 51,51\nxex 52,52\nxex 53,53\nxex 54,54\nxex 55,55\nxad 48,1.0,48\nxad 49,1.0,49\nxad 50,1.0,50\nxad 51,1.0,51\n"
"xad 52,1.0,52\nxad 53,1.0,53\nxad 54,1.0,54\nxad 55,1.0,55\nxrc 48,48\nxrc 49,49\nxrc 50,50\nxrc 51,51\nxrc 52,52\nxrc 53,53\nxrc 54,54\nxrc 55,55\n"
"xmu 32,v32,48\nxmu 33,v33,49\nxmu 34,v34,50\nxmu 35,v35,51\nxmu 36,v36,52\nxmu 37,v37,53\nxmu 38,v38,54\nxmu 39,v39,55\nxar 40,184\nxar 41,185\n"
"xar 42,186\nxar 43,187\nxar 44,188\nxar 45,189\nxar 46,190\nxar 47,191\nxmu 56,v40,40\nxmu 57,v41,41\nxmu 58,v42,42\nxmu 59,v43,43\nxmu 60,v44,44\n"
"xmu 61,v45,45\nxmu 62,v46,46\nxmu 63,v47,47\nxfm 56,v56,s72,v64\nxfm 57,v57,s72,v64\nxfm 58,v58,s72,v64\nxfm 59,v59,s72,v64\nxfm 60,v60,s72,v64\n"
"xfm 61,v61,s72,v64\nxfm 62,v62,s72,v64\nxfm 63,v63,s72,v64\nxmu 56,v56,40\nxmu 57,v57,41\nxmu 58,v58,42\nxmu 59,v59,43\nxmu 60,v60,44\nxmu 61,v61,45\n"
"xmu 62,v62,46\nxmu 63,v63,47\nxex 56,56\nxex 57,57\nxex 58,58\nxex 59,59\nxex 60,60\nxex 61,61\nxex 62,62\nxex 63,63\nxad 56,1.0,56\nxad 57,1.0,57\n"
"xad 58,1.0,58\nxad 59,1.0,59\nxad 60,1.0,60\nxad 61,1.0,61\nxad 62,1.0,62\nxad 63,1.0,63\nxrc 56,56\nxrc 57,57\nxrc 58,58\nxrc 59,59\nxrc 60,60\n"
"xrc 61,61\nxrc 62,62\nxrc 63,63\nxmu 40,v40,56\nxmu 41,v41,57\nxmu 42,v42,58\nxmu 43,v43,59\nxmu 44,v44,60\nxmu 45,v45,61\nxmu 46,v46,62\n"
"xmu 47,v47,63\nxcv 216,32,33\nxcv 217,34,35\nxcv 218,36,37\nxcv 219,38,39\nxcv 220,40,41\nxcv 221,42,43\nxcv 222,44,45\nxcv 223,46,47\nxad 84,v32,33\n"
"xad 84,v84,34\nxad 84,v84,35\nxad 84,v84,36\nxad 84,v84,37\nxad 84,v84,38\nxad 84,v84,39\nxad 84,v84,40\nxad 84,v84,41\nxad 84,v84,42\nxad 84,v84,43\n"
"xad 84,v84,44\nxad 84,v84,45\nxad 84,v84,46\nxad 84,v84,47\nv_mov_b32 v85,v84\ns_nop 1\nv_permlane16_swap_b32 v84,v85\nxad 84,v84,85\n"
"v_mov_b32 v85,v84\ns_nop 1\nv_permlane32_swap_b32 v84,v85\nxad 84,v84,85\nxmu 84,0x3c800000,84\nv_sub_f32 v48,v32,v84\nv_sub_f32 v49,v33,v84\n"
"v_sub_f32 v50,v34,v84\nv_sub_f32 v51,v35,v84\nv_sub_f32 v52,v36,v84\nv_sub_f32 v53,v37,v84\nv_sub_f32 v54,v38,v84\nv_sub_f32 v55,v39,v84\n"
"v_sub_f32 v56,v40,v84\nv_sub_f32 v57,v41,v84\nv_sub_f32 v58,v42,v84\nv_sub_f32 v59,v43,v84\nv_sub_f32 v60,v44,v84\nv_sub_f32 v61,v45,v84\n"
"v_sub_f32 v62,v46,v84\nv_sub_f32 v63,v47,v84\nxmu 86,v48,48\nxfm 86,v49,v49,v86\nxfm 86,v50,v50,v86\nxfm 86,v51,v51,v86\nxfm 86,v52,v52,v86\n"
"xfm 86,v53,v53,v86\nxfm 86,v54,v54,v86\nxfm 86,v55,v55,v86\nxfm 86,v56,v56,v86\nxfm 86,v57,v57,v86\nxfm 86,v58,v58,v86\nxfm 86,v59,v59,v86\n"
"xfm 86,v60,v60,v86\nxfm 86,v61,v61,v86\nxfm 86,v62,v62,v86\nxfm 86,v63,v63,v86\nv_mov_b32 v85,v86\ns_nop 1\nv_permlane16_swap_b32 v86,v85\n"
"xad 86,v86,85\nv_mov_b32 v85,v86\ns_nop 1\nv_permlane32_swap_b32 v86,v85\nxad 86,v86,85\nv_mov_b32 v90,v84\nv_mov_b32 v91,v86\n"
"buffer_store_dwordx2 v[90:91],v31,s[48:51],s68 offen offset:2048 sc1\ns_branch Lkv_3_%=\nLqg_2_%=:\ns_nop 4\nv_mbcnt_lo_u32_b32 v252,-1,0\n"
"v_mbcnt_hi_u32_b32 v252,-1,v252\ns_load_dwordx2 s[76:77],s[0:1],0\ns_load_dwordx2 s[78:79],s[0:1],16\ns_load_dwordx2 s[80:81],s[0:1],80\nxwl 0\n"
"s_lshr_b32 s99,s100,4\ns_and_b32 s99,s99,15\ns_mul_i32 s99,s99,0x1c000\ns_add_u32 s16,s76,s99\ns_addc_u32 s17,s77,0\ns_and_b32 s17,s17,0xffff\n"
"s_lshr_b32 s18,s100,4\ns_and_b32 s18,s18,15\ns_mul_i32 s18,s18,0x1c000\ns_sub_u32 s18,0x1c0000,s18\ns_mov_b32 s19,0x20000\ns_and_b32 s99,s100,15\n"
"s_lshl_b32 s99,s99,9\ns_add_u32 s20,s78,s99\ns_addc_u32 s21,s79,0\ns_and_b32 s21,s21,0xffff\ns_and_b32 s22,s100,15\ns_lshl_b32 s22,s22,9\n"
"s_sub_u32 s22,0x380000,s22\ns_mov_b32 s23,0x20000\ns_mov_b32 s24,s80\ns_and_b32 s25,s81,0xffff\ns_mov_b32 s26,0x380\ns_mov_b32 s27,0x20000\n"
"v_lshrrev_b32 v0,3,v252\ns_lshl_b32 s99,s3,5\nv_add_u32 v0,s99,v0\nv_mul_u32_u24 v0,0x380,v0\nv_and_b32 v247,7,v252\nv_lshrrev_b32 v246,3,v252\n"
"v_lshrrev_b32 v246,1,v246\nv_xor_b32 v247,v247,v246\nv_lshlrev_b32 v247,4,v247\nv_add_u32 v0,v0,v247\nv_xor_b32 v1,64,v0\nv_lshrrev_b32 v2,4,v252\n"
"s_and_b32 s99,s3,1\ns_lshl_b32 s99,s99,5\nv_add_u32 v2,s99,v2\nv_lshlrev_b32 v2,13,v2\ns_lshr_b32 s99,s3,1\ns_lshl_b32 s99,s99,8\n"
"v_add_u32 v2,s99,v2\nv_and_b32 v247,15,v252\nv_lshrrev_b32 v247,1,v247\nv_lshrrev_b32 v246,4,v252\nv_xor_b32 v247,v247,v246\n"
"v_lshlrev_b32 v247,1,v247\nv_and_b32 v246,15,v252\nv_and_b32 v246,1,v246\nv_or_b32 v247,v247,v246\nv_lshlrev_b32 v247,4,v247\nv_add_u32 v2,v2,v247\n"
"v_xor_b32 v3,0x80,v2\nv_and_b32 v6,15,v252\ns_lshr_b32 s99,s3,1\ns_lshl_b32 s99,s99,6\nv_add_u32 v6,s99,v6\nv_lshlrev_b32 v6,7,v6\n"
"v_lshrrev_b32 v247,4,v252\nv_and_b32 v246,15,v252\nv_lshrrev_b32 v246,1,v246\nv_xor_b32 v247,v247,v246\nv_lshlrev_b32 v247,4,v247\n"
"v_add_u32 v6,v6,v247\nv_add_u32 v6,0x18000,v6\nv_xor_b32 v7,64,v6\nv_lshrrev_b32 v250,4,v252\nv_lshlrev_b32 v250,3,v250\nv_and_b32 v247,15,v252\n"
"v_lshrrev_b32 v247,2,v247\nv_add_u32 v250,v250,v247\nv_lshlrev_b32 v250,8,v250\ns_and_b32 s99,s3,1\ns_lshl_b32 s99,s99,14\nv_add_u32 v250,s99,v250\n"
"v_and_b32 v247,3,v252\nv_lshlrev_b32 v247,3,v247\nv_add_u32 v250,v250,v247\nv_and_b32 v251,15,v252\nv_lshrrev_b32 v251,2,v251\n"
"v_lshrrev_b32 v247,4,v252\nv_and_b32 v247,1,v247\nv_lshlrev_b32 v247,2,v247\nv_or_b32 v251,v251,v247\nv_lshlrev_b32 v251,5,v251\nv_xor_b32 v8,0,v251\n"
"v_add_u32 v8,v8,v250\nv_xor_b32 v9,32,v251\nv_add_u32 v9,v9,v250\nv_xor_b32 v10,64,v251\nv_add_u32 v10,v10,v250\nv_xor_b32 v11,0x60,v251\n"
"v_add_u32 v11,v11,v250\nv_xor_b32 v12,0x80,v251\nv_add_u32 v12,v12,v250\nv_xor_b32 v13,0xa0,v251\nv_add_u32 v13,v13,v250\nv_xor_b32 v14,0xc0,v251\n"
"v_add_u32 v14,v14,v250\nv_xor_b32 v15,0xe0,v251\nv_add_u32 v15,v15,v250\ns_load_dwordx2 s[82:83],s[0:1],8\ns_load_dwordx2 s[84:85],s[0:1],24\n"
"s_load_dwordx2 s[86:87],s[0:1],32\nxwl 0\ns_lshr_b32 s99,s100,8\ns_and_b32 s99,s99,15\ns_lshl_b32 s99,s99,19\ns_add_u32 s28,s82,s99\n"
"s_addc_u32 s29,s83,0\ns_and_b32 s29,s29,0xffff\ns_lshr_b32 s30,s100,8\ns_and_b32 s30,s30,15\ns_lshl_b32 s30,s30,19\ns_sub_u32 s30,0x400000,s30\n"
"s_mov_b32 s31,0x20000\ns_lshr_b32 s99,s100,16\ns_and_b32 s99,s99,1\ns_cmp_lg_u32 s99,0\ns_cselect_b32 s74,s86,s84\ns_lshr_b32 s99,s100,16\n"
"s_and_b32 s99,s99,1\ns_cmp_lg_u32 s99,0\ns_cselect_b32 s75,s87,s85\ns_lshr_b32 s99,s100,12\ns_and_b32 s99,s99,15\ns_lshl_b32 s99,s99,9\n"
"s_add_u32 s32,s74,s99\ns_addc_u32 s33,s75,0\ns_and_b32 s33,s33,0xffff\ns_lshr_b32 s34,s100,12\ns_and_b32 s34,s34,15\ns_lshl_b32 s34,s34,9\n"
"s_sub_u32 s34,0x800000,s34\ns_mov_b32 s35,0x20000\nv_lshrrev_b32 v160,3,v252\ns_lshl_b32 s99,s3,6\nv_add_u32 v160,s99,v160\n"
"v_lshlrev_b32 v160,11,v160\nv_and_b32 v247,7,v252\nv_lshrrev_b32 v246,3,v252\nv_lshrrev_b32 v246,1,v246\nv_xor_b32 v247,v247,v246\n"
"v_lshlrev_b32 v247,4,v247\nv_add_u32 v160,v160,v247\nv_xor_b32 v161,64,v160\nv_lshrrev_b32 v162,4,v252\ns_and_b32 s99,s3,1\ns_lshl_b32 s99,s99,5\n"
"v_add_u32 v162,s99,v162\nv_lshlrev_b32 v162,13,v162\ns_lshr_b32 s99,s3,1\ns_lshl_b32 s99,s99,8\nv_add_u32 v162,s99,v162\nv_and_b32 v247,15,v252\n"
"v_lshrrev_b32 v247,1,v247\nv_lshrrev_b32 v246,4,v252\nv_xor_b32 v247,v247,v246\nv_lshlrev_b32 v247,1,v247\nv_and_b32 v246,15,v252\n"
"v_and_b32 v246,1,v246\nv_or_b32 v247,v247,v246\nv_lshlrev_b32 v247,4,v247\nv_add_u32 v162,v162,v247\nv_xor_b32 v163,0x80,v162\ns_lshl_b32 s99,s2,14\n"
"s_add_u32 s53,s82,s99\ns_addc_u32 s54,s83,0\ns_and_b32 s54,s54,0xffff\ns_lshl_b32 s99,s2,15\ns_add_u32 s55,s84,s99\ns_addc_u32 s56,s85,0\n"
"s_and_b32 s56,s56,0xffff\ns_lshl_b32 s99,s2,15\ns_add_u32 s57,s86,s99\ns_addc_u32 s58,s87,0\ns_and_b32 s58,s58,0xffff\ns_lshr_b32 s52,s100,16\n"
"s_and_b32 s52,s52,1\nv_lshlrev_b32 v164,6,v252\ns_lshl_b32 s99,s3,12\nv_add_u32 v164,s99,v164\nv_lshrrev_b32 v30,4,v252\nv_lshrrev_b32 v30,1,v30\n"
"v_lshlrev_b32 v30,10,v30\ns_lshr_b32 s99,s3,1\ns_lshl_b32 s99,s99,15\ns_and_b32 s98,s3,1\ns_lshl_b32 s98,s98,13\ns_add_u32 s99,s99,s98\n"
"v_add_u32 v30,s99,v30\nv_lshrrev_b32 v247,4,v252\nv_and_b32 v247,1,v247\nv_lshlrev_b32 v247,9,v247\nv_add_u32 v30,v30,v247\nv_and_b32 v247,15,v252\n"
"v_lshlrev_b32 v247,4,v247\nv_add_u32 v30,v30,v247\ns_and_b32 s99,s100,15\ns_lshl_b32 s99,s99,20\ns_add_u32 s99,0x2000000,s99\ns_lshr_b32 s98,s100,4\n"
"s_and_b32 s98,s98,15\ns_lshl_b32 s98,s98,16\ns_add_u32 s99,s99,s98\ns_add_u32 s40,s4,s99\ns_addc_u32 s41,s5,0\ns_and_b32 s41,s41,0xffff\n"
"s_mov_b32 s42,0x10000\ns_mov_b32 s43,0x20000\ns_add_u32 s44,s4,0x6c00000\ns_addc_u32 s45,s5,0\ns_and_b32 s45,s45,0xffff\ns_mov_b32 s46,0x8000\n"
"s_mov_b32 s47,0x20000\ns_lshr_b32 s99,s3,1\ns_lshl_b32 s99,s99,6\nv_add_u32 v31,s99,v252\nv_lshlrev_b32 v31,2,v31\nv_lshrrev_b32 v247,4,v252\n"
"s_and_b32 s99,s3,1\ns_and_b32 s98,s100,15\ns_and_b32 s98,s98,1\ns_or_b32 s99,s99,s98\nv_or_b32 v247,s99,v247\nv_lshlrev_b32 v247,16,v247\n"
"v_add_u32 v31,v31,v247\ns_lshr_b32 s99,s100,4\ns_and_b32 s99,s99,15\ns_lshl_b32 s99,s99,9\ns_add_u32 s99,0x5400000,s99\ns_add_u32 s48,s4,s99\n"
"s_addc_u32 s49,s5,0\ns_and_b32 s49,s49,0xffff\ns_mov_b32 s50,0x200\ns_mov_b32 s51,0x20000\nv_lshrrev_b32 v175,4,v252\nv_lshlrev_b32 v175,4,v175\n"
"v_and_b32 v247,15,v252\nv_lshlrev_b32 v247,16,v247\nv_or_b32 v175,v175,v247\ns_mov_b32 s74,0x1c00\ns_mov_b32 s75,0x3800\ns_mov_b32 s76,0x5400\n"
"s_mul_i32 s62,s3,0x1000\ns_mov_b32 s77,0x8000\ns_mov_b32 s78,0x10000\ns_mov_b32 s79,0x18000\ns_mov_b32 s80,0x20000\ns_mov_b32 s81,0x28000\n"
"s_mov_b32 s82,0x30000\ns_mov_b32 s83,0x38000\ns_mul_i32 s63,s3,0x2000\nxam 62,0x18000\ns_nop 0\nxdl 0,16,0,0\nxam 62,0x18400\ns_nop 0\n"
"xdl 1,16,s74,0\nxam 62,0x18800\ns_nop 0\nxdl 0,16,s75,0\nxam 62,0x18c00\ns_nop 0\nxdl 1,16,s76,0\ns_add_u32 s16,s16,0x80\ns_addc_u32 s17,s17,0\n"
"s_sub_u32 s18,s18,0x80\nxam 63,0\ns_nop 0\nxdl 2,20,0,0\nxam 63,0x400\ns_nop 0\nxdl 2,20,s77,0\nxam 63,0x800\ns_nop 0\nxdl 3,20,s78,0\nxam 63,0xc00\n"
"s_nop 0\nxdl 3,20,s79,0\nxam 63,0x1000\ns_nop 0\nxdl 2,20,s80,0\nxam 63,0x1400\ns_nop 0\nxdl 2,20,s81,0\nxam 63,0x1800\ns_nop 0\nxdl 3,20,s82,0\n"
"xam 63,0x1c00\ns_nop 0\nxdl 3,20,s83,0\ns_add_u32 s20,s20,0x80000\ns_addc_u32 s21,s21,0\ns_sub_u32 s22,s22,0x80000\nxam 62,0x1c000\ns_nop 0\n"
"xdl 0,16,0,0\nxam 62,0x1c400\ns_nop 0\nxdl 1,16,s74,0\nxam 62,0x1c800\ns_nop 0\nxdl 0,16,s75,0\nxam 62,0x1cc00\ns_nop 0\nxdl 1,16,s76,0\n"
"s_add_u32 s16,s16,0x80\ns_addc_u32 s17,s17,0\ns_sub_u32 s18,s18,0x80\nxam 63,0x8000\ns_nop 0\nxdl 2,20,0,0\nxam 63,0x8400\ns_nop 0\nxdl 2,20,s77,0\n"
"xam 63,0x8800\ns_nop 0\nxdl 3,20,s78,0\nxam 63,0x8c00\ns_nop 0\nxdl 3,20,s79,0\nxam 63,0x9000\ns_nop 0\nxdl 2,20,s80,0\nxam 63,0x9400\ns_nop 0\n"
"xdl 2,20,s81,0\nxam 63,0x9800\ns_nop 0\nxdl 3,20,s82,0\nxam 63,0x9c00\ns_nop 0\nxdl 3,20,s83,0\ns_add_u32 s20,s20,0x80000\ns_addc_u32 s21,s21,0\n"
"s_sub_u32 s22,s22,0x80000\nxam 62,0x20000\ns_nop 0\nxdl 0,16,0,0\nxam 62,0x20400\ns_nop 0\nxdl 1,16,s74,0\nxam 62,0x20800\ns_nop 0\nxdl 0,16,s75,0\n"
"xam 62,0x20c00\ns_nop 0\nxdl 1,16,s76,0\ns_add_u32 s16,s16,0x80\ns_addc_u32 s17,s17,0\ns_sub_u32 s18,s18,0x80\nxam 63,0x10000\ns_nop 0\nxdl 2,20,0,0\n"
"xam 63,0x10400\ns_nop 0\nxdl 2,20,s77,0\nxam 63,0x10800\ns_nop 0\nxdl 3,20,s78,0\nxam 63,0x10c00\ns_nop 0\nxdl 3,20,s79,0\nxam 63,0x11000\ns_nop 0\n"
"xdl 2,20,s80,0\nxam 63,0x11400\ns_nop 0\nxdl 2,20,s81,0\nxam 63,0x11800\ns_nop 0\nxdl 3,20,s82,0\nxam 63,0x11c00\ns_nop 0\nxdl 3,20,s83,0\n"
"s_add_u32 s20,s20,0x80000\ns_addc_u32 s21,s21,0\ns_sub_u32 s22,s22,0x80000\nv_mov_b32 v174,0\n"
"buffer_load_dwordx2 v[172:173],v174,s[44:47],0 offen offset:0 sc1\nbuffer_load_dwordx4 v[176:179],v175,s[24:27],0 offen offset:0\n"
"buffer_load_dwordx4 v[180:183],v175,s[24:27],0 offen offset:64\nbuffer_load_dwordx4 v[184:187],v175,s[24:27],0 offen offset:128\n"
"buffer_load_dwordx4 v[188:191],v175,s[24:27],0 offen offset:192\nbuffer_load_dwordx4 v[192:195],v175,s[24:27],0 offen offset:256\n"
"buffer_load_dwordx4 v[196:199],v175,s[24:27],0 offen offset:320\nbuffer_load_dwordx4 v[200:203],v175,s[24:27],0 offen offset:384\n"
"buffer_load_dwordx4 v[204:207],v175,s[24:27],0 offen offset:448\nbuffer_load_dwordx4 v[208:211],v175,s[24:27],0 offen offset:512\n"
"buffer_load_dwordx4 v[212:215],v175,s[24:27],0 offen offset:576\nbuffer_load_dwordx4 v[216:219],v175,s[24:27],0 offen offset:640\n"
"buffer_load_dwordx4 v[220:223],v175,s[24:27],0 offen offset:704\nbuffer_load_dwordx4 v[224:227],v175,s[24:27],0 offen offset:768\n"
"buffer_load_dwordx4 v[228:231],v175,s[24:27],0 offen offset:832\ns_mov_b32 s39,0x20000\ns_mov_b32 s36,s53\ns_mov_b32 s37,s54\ns_mov_b32 s38,0x4000\n"
"buffer_load_dword v171,v164,s[36:39],0 offen offset:0\ns_mov_b32 s36,s55\ns_mov_b32 s37,s56\ns_mov_b32 s38,0x8000\n"
"buffer_load_dword v171,v164,s[36:39],0 offen offset:0\ns_mov_b32 s68,0x4000\nbuffer_load_dword v171,v164,s[36:39],s68 offen offset:0\n"
"s_mov_b32 s36,s57\ns_mov_b32 s37,s58\ns_mov_b32 s38,0x8000\nbuffer_load_dword v171,v164,s[36:39],0 offen offset:0\ns_mov_b32 s68,0x4000\n"
"buffer_load_dword v171,v164,s[36:39],s68 offen offset:0\ns_mov_b32 s84,0x4000\ns_cmp_eq_u32 s52,0\ns_cselect_b32 s84,s84,0x8000\n"
"s_mov_b32 s85,0x8000\ns_cmp_eq_u32 s52,0\ns_cselect_b32 s85,s85,0x4000\ns_mov_b32 s86,0xc000\ns_mov_b32 s87,0x10000\ns_mov_b32 s88,0x14000\n"
"s_cmp_eq_u32 s52,0\ns_cselect_b32 s88,s88,0x18000\ns_mov_b32 s89,0x18000\ns_cmp_eq_u32 s52,0\ns_cselect_b32 s89,s89,0x14000\ns_mov_b32 s90,0x1c000\n"
"s_mul_i32 s65,s3,0x2000\ns_mov_b32 s91,0x8000\ns_mov_b32 s92,0x10000\ns_mov_b32 s93,0x18000\ns_mov_b32 s94,0x20000\ns_mov_b32 s95,0x28000\n"
"s_mov_b32 s96,0x30000\ns_mov_b32 s97,0x38000\ns_mul_i32 s66,s3,0x2000\nv_add_u32 v20,0x10000,v8\nv_add_u32 v21,0x10000,v9\nv_add_u32 v22,0x10000,v10\n"
"v_add_u32 v23,0x10000,v11\nv_add_u32 v24,0x10000,v12\nv_add_u32 v25,0x10000,v13\nv_add_u32 v26,0x10000,v14\nv_add_u32 v27,0x10000,v15\n"
"s_waitcnt vmcnt(44)\ns_barrier\ns_waitcnt vmcnt(0)\nxrt 48,8,0\nxrt 50,8,1024\nxrt 52,9,0\nxrt 54,9,1024\nxrt 56,10,0\nxrt 58,10,1024\nxrt 60,11,0\n"
"xrt 62,11,1024\nxrt 64,12,0\nxrt 66,12,1024\nxrt 68,13,0\nxrt 70,13,1024\nxrt 72,14,0\nxrt 74,14,1024\nxrt 76,15,0\nxrt 78,15,1024\nxrb 32,6,0\n"
"xrb 36,6,2048\nxrb 40,6,4096\nxrb 44,6,6144\nxwl 3\nxm16z 0,48,32\nxrt 96,8,8192\nxm16z 4,52,32\nxrt 98,8,9216\nxm16z 8,56,32\nxrt 100,9,8192\n"
"xm16z 12,60,32\nxrt 102,9,9216\nxm16z 16,64,32\nxrt 104,10,8192\nxm16z 20,68,32\nxm16z 24,72,32\nxrt 106,10,9216\nxm16z 28,76,32\nxrt 108,11,8192\n"
"xm16z 128,176,32\nxwl 9\nxm16z 32,48,36\nxrt 110,11,9216\nxm16z 36,52,36\nxrt 112,12,8192\nxm16z 40,56,36\nxrt 114,12,9216\nxm16z 44,60,36\n"
"xm16z 48,64,36\nxrt 116,13,8192\nxm16z 52,68,36\nxrt 118,13,9216\nxm16z 56,72,36\nxrt 120,14,8192\nxm16z 60,76,36\nxrt 122,14,9216\nxm16z 132,176,36\n"
"xwl 15\nxm16z 64,48,40\nxrt 124,15,8192\nxm16z 68,52,40\nxm16z 72,56,40\nxrt 126,15,9216\nxm16z 76,60,40\nxrb 80,7,0\nxm16z 80,64,40\nxrb 84,7,2048\n"
"xm16z 84,68,40\nxrb 88,7,4096\nxm16z 88,72,40\nxrb 92,7,6144\nxm16z 92,76,40\nxm16z 136,176,40\nxwl 15\nxm16z 96,48,44\nxm16z 100,52,44\n"
"xm16z 104,56,44\nxm16z 108,60,44\nxm16z 112,64,44\nxm16z 116,68,44\nxm16z 120,72,44\nxm16z 124,76,44\nxm16z 140,176,44\n"
"s_waitcnt vmcnt(32) lgkmcnt(0)\ns_barrier\nxam 62,0x18000\nxm16 0,96,80\nxdl 0,16,0,0\nxrt 48,8,32768\nxm16 4,100,80\nxrt 50,8,33792\nxm16 8,104,80\n"
"xrt 52,9,32768\nxm16 12,108,80\nxrt 54,9,33792\nxm16 16,112,80\nxrt 56,10,32768\nxam 62,0x18400\nxm16 20,116,80\nxdl 1,16,s74,0\nxm16 24,120,80\n"
"xrt 58,10,33792\nxm16 28,124,80\nxrt 60,11,32768\nxm16 128,180,80\nxm16 32,96,84\nxrt 62,11,33792\nxm16 36,100,84\nxrt 64,12,32768\nxam 62,0x18800\n"
"xm16 40,104,84\nxdl 0,16,s75,0\nxrt 66,12,33792\nxm16 44,108,84\nxm16 48,112,84\nxrt 68,13,32768\nxm16 52,116,84\nxrt 70,13,33792\nxm16 56,120,84\n"
"xrt 72,14,32768\nxam 62,0x18c00\nxm16 60,124,84\nxdl 1,16,s76,0\ns_add_u32 s16,s16,0x80\ns_addc_u32 s17,s17,0\ns_sub_u32 s18,s18,0x80\n"
"xrt 74,14,33792\nxm16 132,180,84\nxm16 64,96,88\nxrt 76,15,32768\nxm16 68,100,88\nxm16 72,104,88\nxrt 78,15,33792\nxm16 76,108,88\nxrb 32,6,16384\n"
"xam 63,0\nxm16 80,112,88\nxdl 2,20,0,0\nxrb 36,6,18432\nxm16 84,116,88\nxrb 40,6,20480\nxm16 88,120,88\nxrb 44,6,22528\nxm16 92,124,88\n"
"xm16 136,180,88\nxm16 96,96,92\nxam 63,0x400\nxm16 100,100,92\nxdl 2,20,s77,0\nxm16 104,104,92\nxm16 108,108,92\nxm16 112,112,92\nxm16 116,116,92\n"
"xam 63,0x800\nxm16 120,120,92\nxdl 3,20,s78,0\nxm16 124,124,92\nxm16 140,180,92\nxwl 3\nxm16 0,48,32\nxrt 96,8,40960\nxm16 4,52,32\nxrt 98,8,41984\n"
"xm16 8,56,32\nxrt 100,9,40960\nxam 63,0xc00\nxm16 12,60,32\nxdl 3,20,s79,0\nxrt 102,9,41984\nxm16 16,64,32\nxrt 104,10,40960\nxm16 20,68,32\n"
"xm16 24,72,32\nxrt 106,10,41984\nxm16 28,76,32\nxrt 108,11,40960\nxm16 128,184,32\nxam 63,0x1000\nxwl 9\nxm16 32,48,36\nxdl 2,20,s80,0\n"
"xrt 110,11,41984\nxm16 36,52,36\nxrt 112,12,40960\nxm16 40,56,36\nxrt 114,12,41984\nxm16 44,60,36\nxm16 48,64,36\nxrt 116,13,40960\nxam 63,0x1400\n"
"xm16 52,68,36\nxdl 2,20,s81,0\nxrt 118,13,41984\nxm16 56,72,36\nxrt 120,14,40960\nxm16 60,76,36\nxrt 122,14,41984\nxm16 132,184,36\nxwl 15\n"
"xm16 64,48,40\nxrt 124,15,40960\nxm16 68,52,40\nxam 63,0x1800\nxm16 72,56,40\nxdl 3,20,s82,0\nxrt 126,15,41984\nxm16 76,60,40\nxrb 80,7,16384\n"
"xm16 80,64,40\nxrb 84,7,18432\nxm16 84,68,40\nxrb 88,7,20480\nxm16 88,72,40\nxrb 92,7,22528\nxam 63,0x1c00\nxm16 92,76,40\nxdl 3,20,s83,0\n"
"s_add_u32 s20,s20,0x80000\ns_addc_u32 s21,s21,0\ns_sub_u32 s22,s22,0x80000\nxm16 136,184,40\nxwl 15\nxm16 96,48,44\nxm16 100,52,44\nxm16 104,56,44\n"
"xm16 108,60,44\nxm16 112,64,44\nxm16 116,68,44\nxm16 120,72,44\nxm16 124,76,44\nxm16 140,184,44\ns_waitcnt vmcnt(32) lgkmcnt(0)\ns_barrier\n"
"xam 62,0x1c000\nxm16 0,96,80\nxdl 0,16,0,0\nxrt 48,20,0\nxm16 4,100,80\nxrt 50,20,1024\nxm16 8,104,80\nxrt 52,21,0\nxm16 12,108,80\nxrt 54,21,1024\n"
"xm16 16,112,80\nxrt 56,22,0\nxam 62,0x1c400\nxm16 20,116,80\nxdl 1,16,s74,0\nxm16 24,120,80\nxrt 58,22,1024\nxm16 28,124,80\nxrt 60,23,0\n"
"xm16 128,188,80\nxm16 32,96,84\nxrt 62,23,1024\nxm16 36,100,84\nxrt 64,24,0\nxam 62,0x1c800\nxm16 40,104,84\nxdl 0,16,s75,0\nxrt 66,24,1024\n"
"xm16 44,108,84\nxm16 48,112,84\nxrt 68,25,0\nxm16 52,116,84\nxrt 70,25,1024\nxm16 56,120,84\nxrt 72,26,0\nxam 62,0x1cc00\nxm16 60,124,84\n"
"xdl 1,16,s76,0\ns_add_u32 s16,s16,0x80\ns_addc_u32 s17,s17,0\ns_sub_u32 s18,s18,0x80\nxrt 74,26,1024\nxm16 132,188,84\nxm16 64,96,88\nxrt 76,27,0\n"
"xm16 68,100,88\nxm16 72,104,88\nxrt 78,27,1024\nxm16 76,108,88\nxrb 32,6,32768\nxam 63,0x8000\nxm16 80,112,88\nxdl 2,20,0,0\nxrb 36,6,34816\n"
"xm16 84,116,88\nxrb 40,6,36864\nxm16 88,120,88\nxrb 44,6,38912\nxm16 92,124,88\nxm16 136,188,88\nxm16 96,96,92\nxam 63,0x8400\nxm16 100,100,92\n"
"xdl 2,20,s77,0\nxm16 104,104,92\nxm16 108,108,92\nxm16 112,112,92\nxm16 116,116,92\nxam 63,0x8800\nxm16 120,120,92\nxdl 3,20,s78,0\nxm16 124,124,92\n"
"xm16 140,188,92\nxwl 3\nxm16 0,48,32\nxrt 96,20,8192\nxm16 4,52,32\nxrt 98,20,9216\nxm16 8,56,32\nxrt 100,21,8192\nxam 63,0x8c00\nxm16 12,60,32\n"
"xdl 3,20,s79,0\nxrt 102,21,9216\nxm16 16,64,32\nxrt 104,22,8192\nxm16 20,68,32\nxm16 24,72,32\nxrt 106,22,9216\nxm16 28,76,32\nxrt 108,23,8192\n"
"xm16 128,192,32\nxam 63,0x9000\nxwl 9\nxm16 32,48,36\nxdl 2,20,s80,0\nxrt 110,23,9216\nxm16 36,52,36\nxrt 112,24,8192\nxm16 40,56,36\n"
"xrt 114,24,9216\nxm16 44,60,36\nxm16 48,64,36\nxrt 116,25,8192\nxam 63,0x9400\nxm16 52,68,36\nxdl 2,20,s81,0\nxrt 118,25,9216\nxm16 56,72,36\n"
"xrt 120,26,8192\nxm16 60,76,36\nxrt 122,26,9216\nxm16 132,192,36\nxwl 15\nxm16 64,48,40\nxrt 124,27,8192\nxm16 68,52,40\nxam 63,0x9800\n"
"xm16 72,56,40\nxdl 3,20,s82,0\nxrt 126,27,9216\nxm16 76,60,40\nxrb 80,7,32768\nxm16 80,64,40\nxrb 84,7,34816\nxm16 84,68,40\nxrb 88,7,36864\n"
"xm16 88,72,40\nxrb 92,7,38912\nxam 63,0x9c00\nxm16 92,76,40\nxdl 3,20,s83,0\ns_add_u32 s20,s20,0x80000\ns_addc_u32 s21,s21,0\n"
"s_sub_u32 s22,s22,0x80000\nxm16 136,192,40\nxwl 15\nxm16 96,48,44\nxm16 100,52,44\nxm16 104,56,44\nxm16 108,60,44\nxm16 112,64,44\nxm16 116,68,44\n"
"xm16 120,72,44\nxm16 124,76,44\nxm16 140,192,44\ns_waitcnt vmcnt(12) lgkmcnt(0)\ns_barrier\nxam 62,0x20000\nxm16 0,96,80\nxdl 0,16,0,0\nxrt 48,8,0\n"
"xm16 4,100,80\nxrt 50,8,1024\nxm16 8,104,80\nxrt 52,9,0\nxm16 12,108,80\nxrt 54,9,1024\nxm16 16,112,80\nxrt 56,10,0\nxam 62,0x20400\nxm16 20,116,80\n"
"xdl 1,16,s74,0\nxm16 24,120,80\nxrt 58,10,1024\nxm16 28,124,80\nxrt 60,11,0\nxm16 128,196,80\nxm16 32,96,84\nxrt 62,11,1024\nxm16 36,100,84\n"
"xrt 64,12,0\nxam 62,0x20800\nxm16 40,104,84\nxdl 0,16,s75,0\nxrt 66,12,1024\nxm16 44,108,84\nxm16 48,112,84\nxrt 68,13,0\nxm16 52,116,84\n"
"xrt 70,13,1024\nxm16 56,120,84\nxrt 72,14,0\nxam 62,0x20c00\nxm16 60,124,84\nxdl 1,16,s76,0\ns_add_u32 s16,s16,0x80\ns_addc_u32 s17,s17,0\n"
"s_sub_u32 s18,s18,0x80\nxrt 74,14,1024\nxm16 132,196,84\nxm16 64,96,88\nxrt 76,15,0\nxm16 68,100,88\nxm16 72,104,88\nxrt 78,15,1024\nxm16 76,108,88\n"
"xrb 32,6,0\nxam 63,0x10000\nxm16 80,112,88\nxdl 2,20,0,0\nxrb 36,6,2048\nxm16 84,116,88\nxrb 40,6,4096\nxm16 88,120,88\nxrb 44,6,6144\n"
"xm16 92,124,88\nxm16 136,196,88\nxm16 96,96,92\nxam 63,0x10400\nxm16 100,100,92\nxdl 2,20,s77,0\nxm16 104,104,92\nxm16 108,108,92\nxm16 112,112,92\n"
"xm16 116,116,92\nxam 63,0x10800\nxm16 120,120,92\nxdl 3,20,s78,0\nxm16 124,124,92\nxm16 140,196,92\nxwl 3\nxm16 0,48,32\nxrt 96,8,8192\nxm16 4,52,32\n"
"xrt 98,8,9216\nxm16 8,56,32\nxrt 100,9,8192\nxam 63,0x10c00\nxm16 12,60,32\nxdl 3,20,s79,0\nxrt 102,9,9216\nxm16 16,64,32\nxrt 104,10,8192\n"
"xm16 20,68,32\nxm16 24,72,32\nxrt 106,10,9216\nxm16 28,76,32\nxrt 108,11,8192\nxm16 128,200,32\nxam 63,0x11000\nxwl 9\nxm16 32,48,36\nxdl 2,20,s80,0\n"
"xrt 110,11,9216\nxm16 36,52,36\nxrt 112,12,8192\nxm16 40,56,36\nxrt 114,12,9216\nxm16 44,60,36\nxm16 48,64,36\nxrt 116,13,8192\nxam 63,0x11400\n"
"xm16 52,68,36\nxdl 2,20,s81,0\nxrt 118,13,9216\nxm16 56,72,36\nxrt 120,14,8192\nxm16 60,76,36\nxrt 122,14,9216\nxm16 132,200,36\nxwl 15\n"
"xm16 64,48,40\nxrt 124,15,8192\nxm16 68,52,40\nxam 63,0x11800\nxm16 72,56,40\nxdl 3,20,s82,0\nxrt 126,15,9216\nxm16 76,60,40\nxrb 80,7,0\n"
"xm16 80,64,40\nxrb 84,7,2048\nxm16 84,68,40\nxrb 88,7,4096\nxm16 88,72,40\nxrb 92,7,6144\nxam 63,0x11c00\nxm16 92,76,40\nxdl 3,20,s83,0\n"
"s_add_u32 s20,s20,0x80000\ns_addc_u32 s21,s21,0\ns_sub_u32 s22,s22,0x80000\nxm16 136,200,40\nxwl 15\nxm16 96,48,44\nxm16 100,52,44\nxm16 104,56,44\n"
"xm16 108,60,44\nxm16 112,64,44\nxm16 116,68,44\nxm16 120,72,44\nxm16 124,76,44\nxm16 140,200,44\ns_waitcnt vmcnt(12) lgkmcnt(0)\ns_barrier\n"
"xam 62,0x18000\nxm16 0,96,80\nxdl 0,16,0,0\nxrt 48,8,32768\nxm16 4,100,80\nxrt 50,8,33792\nxm16 8,104,80\nxrt 52,9,32768\nxm16 12,108,80\n"
"xrt 54,9,33792\nxm16 16,112,80\nxrt 56,10,32768\nxam 62,0x18400\nxm16 20,116,80\nxdl 1,16,s74,0\nxm16 24,120,80\nxrt 58,10,33792\nxm16 28,124,80\n"
"xrt 60,11,32768\nxm16 128,204,80\nxm16 32,96,84\nxrt 62,11,33792\nxm16 36,100,84\nxrt 64,12,32768\nxam 62,0x18800\nxm16 40,104,84\nxdl 0,16,s75,0\n"
"xrt 66,12,33792\nxm16 44,108,84\nxm16 48,112,84\nxrt 68,13,32768\nxm16 52,116,84\nxrt 70,13,33792\nxm16 56,120,84\nxrt 72,14,32768\nxam 62,0x18c00\n"
"xm16 60,124,84\nxdl 1,16,s76,0\ns_add_u32 s16,s16,0x80\ns_addc_u32 s17,s17,0\ns_sub_u32 s18,s18,0x80\nxrt 74,14,33792\nxm16 132,204,84\n"
"xm16 64,96,88\nxrt 76,15,32768\nxm16 68,100,88\nxm16 72,104,88\nxrt 78,15,33792\nxm16 76,108,88\nxrb 32,6,16384\nxam 63,0\nxm16 80,112,88\n"
"xdl 2,20,0,0\nxrb 36,6,18432\nxm16 84,116,88\nxrb 40,6,20480\nxm16 88,120,88\nxrb 44,6,22528\nxm16 92,124,88\nxm16 136,204,88\nxm16 96,96,92\n"
"xam 63,0x400\nxm16 100,100,92\nxdl 2,20,s77,0\nxm16 104,104,92\nxm16 108,108,92\nxm16 112,112,92\nxm16 116,116,92\nxam 63,0x800\nxm16 120,120,92\n"
"xdl 3,20,s78,0\nxm16 124,124,92\nxm16 140,204,92\nxwl 3\nxm16 0,48,32\nxrt 96,8,40960\nxm16 4,52,32\nxrt 98,8,41984\nxm16 8,56,32\nxrt 100,9,40960\n"
"xam 63,0xc00\nxm16 12,60,32\nxdl 3,20,s79,0\nxrt 102,9,41984\nxm16 16,64,32\nxrt 104,10,40960\nxm16 20,68,32\nxm16 24,72,32\nxrt 106,10,41984\n"
"xm16 28,76,32\nxrt 108,11,40960\nxm16 128,208,32\nxam 63,0x1000\nxwl 9\nxm16 32,48,36\nxdl 2,20,s80,0\nxrt 110,11,41984\nxm16 36,52,36\n"
"xrt 112,12,40960\nxm16 40,56,36\nxrt 114,12,41984\nxm16 44,60,36\nxm16 48,64,36\nxrt 116,13,40960\nxam 63,0x1400\nxm16 52,68,36\nxdl 2,20,s81,0\n"
"xrt 118,13,41984\nxm16 56,72,36\nxrt 120,14,40960\nxm16 60,76,36\nxrt 122,14,41984\nxm16 132,208,36\nxwl 15\nxm16 64,48,40\nxrt 124,15,40960\n"
"xm16 68,52,40\nxam 63,0x1800\nxm16 72,56,40\nxdl 3,20,s82,0\nxrt 126,15,41984\nxm16 76,60,40\nxrb 80,7,16384\nxm16 80,64,40\nxrb 84,7,18432\n"
"xm16 84,68,40\nxrb 88,7,20480\nxm16 88,72,40\nxrb 92,7,22528\nxam 63,0x1c00\nxm16 92,76,40\nxdl 3,20,s83,0\ns_add_u32 s20,s20,0x80000\n"
"s_addc_u32 s21,s21,0\ns_sub_u32 s22,s22,0x80000\nxm16 136,208,40\nxwl 15\nxm16 96,48,44\nxm16 100,52,44\nxm16 104,56,44\nxm16 108,60,44\n"
"xm16 112,64,44\nxm16 116,68,44\nxm16 120,72,44\nxm16 124,76,44\nxm16 140,208,44\ns_waitcnt vmcnt(12) lgkmcnt(0)\ns_barrier\nxm16 0,96,80\n"
"xrt 48,20,0\nxm16 4,100,80\nxrt 50,20,1024\nxm16 8,104,80\nxrt 52,21,0\nxm16 12,108,80\nxrt 54,21,1024\nxm16 16,112,80\nxrt 56,22,0\nxm16 20,116,80\n"
"xm16 24,120,80\nxrt 58,22,1024\nxm16 28,124,80\nxrt 60,23,0\nxm16 128,212,80\nxm16 32,96,84\nxrt 62,23,1024\nxm16 36,100,84\nxrt 64,24,0\n"
"xm16 40,104,84\nxrt 66,24,1024\nxm16 44,108,84\nxm16 48,112,84\nxrt 68,25,0\nxm16 52,116,84\nxrt 70,25,1024\nxm16 56,120,84\nxrt 72,26,0\n"
"xm16 60,124,84\nxrt 74,26,1024\nxm16 132,212,84\nxm16 64,96,88\nxrt 76,27,0\nxm16 68,100,88\nxm16 72,104,88\nxrt 78,27,1024\nxm16 76,108,88\n"
"xrb 32,6,32768\nxm16 80,112,88\nxrb 36,6,34816\nxm16 84,116,88\nxrb 40,6,36864\nxm16 88,120,88\nxrb 44,6,38912\nxm16 92,124,88\nxm16 136,212,88\n"
"xm16 96,96,92\nxm16 100,100,92\nxm16 104,104,92\nxm16 108,108,92\nxm16 112,112,92\nxm16 116,116,92\nxm16 120,120,92\nxm16 124,124,92\n"
"xm16 140,212,92\nxwl 3\nxm16 0,48,32\nxrt 96,20,8192\nxm16 4,52,32\nxrt 98,20,9216\nxm16 8,56,32\nxrt 100,21,8192\nxm16 12,60,32\nxrt 102,21,9216\n"
"xm16 16,64,32\nxrt 104,22,8192\nxm16 20,68,32\nxm16 24,72,32\nxrt 106,22,9216\nxm16 28,76,32\nxrt 108,23,8192\nxm16 128,216,32\nxwl 9\nxm16 32,48,36\n"
"xrt 110,23,9216\nxm16 36,52,36\nxrt 112,24,8192\nxm16 40,56,36\nxrt 114,24,9216\nxm16 44,60,36\nxm16 48,64,36\nxrt 116,25,8192\nxm16 52,68,36\n"
"xrt 118,25,9216\nxm16 56,72,36\nxrt 120,26,8192\nxm16 60,76,36\nxrt 122,26,9216\nxm16 132,216,36\nxwl 15\nxm16 64,48,40\nxrt 124,27,8192\n"
"xm16 68,52,40\nxm16 72,56,40\nxrt 126,27,9216\nxm16 76,60,40\nxrb 80,7,32768\nxm16 80,64,40\nxrb 84,7,34816\nxm16 84,68,40\nxrb 88,7,36864\n"
"xm16 88,72,40\nxrb 92,7,38912\nxm16 92,76,40\nxm16 136,216,40\nxwl 15\nxm16 96,48,44\nxm16 100,52,44\nxm16 104,56,44\nxm16 108,60,44\nxm16 112,64,44\n"
"xm16 116,68,44\nxm16 120,72,44\nxm16 124,76,44\nxm16 140,216,44\ns_waitcnt vmcnt(0) lgkmcnt(0)\ns_barrier\nxm16 0,96,80\nxrt 48,8,0\nxm16 4,100,80\n"
"xrt 50,8,1024\nxm16 8,104,80\nxrt 52,9,0\nxm16 12,108,80\nxrt 54,9,1024\nxm16 16,112,80\nxrt 56,10,0\nxm16 20,116,80\nxm16 24,120,80\nxrt 58,10,1024\n"
"xm16 28,124,80\nxrt 60,11,0\nxm16 128,220,80\nxm16 32,96,84\nxrt 62,11,1024\nxm16 36,100,84\nxrt 64,12,0\nxm16 40,104,84\nxrt 66,12,1024\n"
"xm16 44,108,84\nxm16 48,112,84\nxrt 68,13,0\nxm16 52,116,84\nxrt 70,13,1024\nxm16 56,120,84\nxrt 72,14,0\nxm16 60,124,84\nxrt 74,14,1024\n"
"xm16 132,220,84\nxm16 64,96,88\nxrt 76,15,0\nxm16 68,100,88\nxm16 72,104,88\nxrt 78,15,1024\nxm16 76,108,88\nxrb 32,6,0\nxm16 80,112,88\n"
"xrb 36,6,2048\nxm16 84,116,88\nxrb 40,6,4096\nxm16 88,120,88\nxrb 44,6,6144\nxm16 92,124,88\nxm16 136,220,88\nxm16 96,96,92\nxm16 100,100,92\n"
"xm16 104,104,92\nxm16 108,108,92\nxm16 112,112,92\nxm16 116,116,92\nxm16 120,120,92\nxm16 124,124,92\nxm16 140,220,92\nxwl 3\nxm16 0,48,32\n"
"xrt 96,8,8192\nxm16 4,52,32\nxrt 98,8,9216\nxm16 8,56,32\nxrt 100,9,8192\nxm16 12,60,32\nxrt 102,9,9216\nxm16 16,64,32\nxrt 104,10,8192\n"
"xm16 20,68,32\nxm16 24,72,32\nxrt 106,10,9216\nxm16 28,76,32\nxrt 108,11,8192\nxm16 128,224,32\nxwl 9\nxm16 32,48,36\nxrt 110,11,9216\nxm16 36,52,36\n"
"xrt 112,12,8192\nxm16 40,56,36\nxrt 114,12,9216\nxm16 44,60,36\nxm16 48,64,36\nxrt 116,13,8192\nxm16 52,68,36\nxrt 118,13,9216\nxm16 56,72,36\n"
"xrt 120,14,8192\nxm16 60,76,36\nxrt 122,14,9216\nxm16 132,224,36\nxwl 15\nxm16 64,48,40\nxrt 124,15,8192\nxm16 68,52,40\nxm16 72,56,40\n"
"xrt 126,15,9216\nxm16 76,60,40\nxrb 80,7,0\nxm16 80,64,40\nxrb 84,7,2048\nxm16 84,68,40\nxrb 88,7,4096\nxm16 88,72,40\nxrb 92,7,6144\nxm16 92,76,40\n"
"xm16 136,224,40\nxwl 15\nxm16 96,48,44\nxm16 100,52,44\nxm16 104,56,44\nxm16 108,60,44\nxm16 112,64,44\nxm16 116,68,44\nxm16 120,72,44\n"
"xm16 124,76,44\nxm16 140,224,44\nxwl 0\ns_barrier\nxam 65,0x18000\nxm16 0,96,80\nxdl 160,28,0,0\nxam 65,0x18400\nxm16 4,100,80\nxdl 161,28,s84,0\n"
"xam 65,0x18800\nxm16 8,104,80\nxdl 160,28,s85,0\nxam 65,0x18c00\nxm16 12,108,80\nxdl 161,28,s86,0\nxam 65,0x19000\nxm16 16,112,80\nxdl 160,28,s87,0\n"
"xam 65,0x19400\nxm16 20,116,80\nxdl 161,28,s88,0\nxam 65,0x19800\nxm16 24,120,80\nxdl 160,28,s89,0\nxam 65,0x19c00\nxm16 28,124,80\nxdl 161,28,s90,0\n"
"s_add_u32 s28,s28,0x80\ns_addc_u32 s29,s29,0\ns_sub_u32 s30,s30,0x80\nxm16 128,228,80\nxam 66,0\nxm16 32,96,84\nxdl 162,32,0,0\nxam 66,0x400\n"
"xm16 36,100,84\nxdl 162,32,s91,0\nxam 66,0x800\nxm16 40,104,84\nxdl 163,32,s92,0\nxam 66,0xc00\nxm16 44,108,84\nxdl 163,32,s93,0\nxam 66,0x1000\n"
"xm16 48,112,84\nxdl 162,32,s94,0\nxam 66,0x1400\nxm16 52,116,84\nxdl 162,32,s95,0\nxam 66,0x1800\nxm16 56,120,84\nxdl 163,32,s96,0\nxam 66,0x1c00\n"
"xm16 60,124,84\nxdl 163,32,s97,0\ns_add_u32 s32,s32,0x80000\ns_addc_u32 s33,s33,0\ns_sub_u32 s34,s34,0x80000\nxm16 132,228,84\nxam 65,0x20000\n"
"xm16 64,96,88\nxdl 160,28,0,0\nxam 65,0x20400\nxm16 68,100,88\nxdl 161,28,s84,0\nxam 65,0x20800\nxm16 72,104,88\nxdl 160,28,s85,0\nxam 65,0x20c00\n"
"xm16 76,108,88\nxdl 161,28,s86,0\nxam 65,0x21000\nxm16 80,112,88\nxdl 160,28,s87,0\nxam 65,0x21400\nxm16 84,116,88\nxdl 161,28,s88,0\nxam 65,0x21800\n"
"xm16 88,120,88\nxdl 160,28,s89,0\nxam 65,0x21c00\nxm16 92,124,88\nxdl 161,28,s90,0\ns_add_u32 s28,s28,0x80\ns_addc_u32 s29,s29,0\n"
"s_sub_u32 s30,s30,0x80\nxm16 136,228,88\nxam 66,0x8000\nxm16 96,96,92\nxdl 162,32,0,0\nxam 66,0x8400\nxm16 100,100,92\nxdl 162,32,s91,0\n"
"xam 66,0x8800\nxm16 104,104,92\nxdl 163,32,s92,0\nxam 66,0x8c00\nxm16 108,108,92\nxdl 163,32,s93,0\nxam 66,0x9000\nxm16 112,112,92\nxdl 162,32,s94,0\n"
"xam 66,0x9400\nxm16 116,116,92\nxdl 162,32,s95,0\nxam 66,0x9800\nxm16 120,120,92\nxdl 163,32,s96,0\nxam 66,0x9c00\nxm16 124,124,92\nxdl 163,32,s97,0\n"
"s_add_u32 s32,s32,0x80000\ns_addc_u32 s33,s33,0\ns_sub_u32 s34,s34,0x80000\nxam 66,0x10000\ns_nop 0\nxdl 162,32,0,0\nxam 66,0x10400\ns_nop 0\n"
"xdl 162,32,s91,0\nxam 66,0x10800\ns_nop 0\nxdl 163,32,s92,0\nxam 66,0x10c00\ns_nop 0\nxdl 163,32,s93,0\nxam 66,0x11000\ns_nop 0\nxdl 162,32,s94,0\n"
"xam 66,0x11400\ns_nop 0\nxdl 162,32,s95,0\nxam 66,0x11800\ns_nop 0\nxdl 163,32,s96,0\nxam 66,0x11c00\ns_nop 0\nxdl 163,32,s97,0\n"
"s_add_u32 s32,s32,0x80000\ns_addc_u32 s33,s33,0\ns_sub_u32 s34,s34,0x80000\nxm16 140,228,92\nv_readfirstlane_b32 s6,v172\n"
"v_readfirstlane_b32 s7,v173\ns_nop 3\ns_add_u32 s6,s6,1\ns_addc_u32 s7,s7,0\ns_nop 3\ns_mov_b32 s72,0x3db8aa3b\nxar 32,0\nxar 33,1\nxar 34,2\n"
"xar 35,3\nxar 36,4\nxar 37,5\nxar 38,6\nxar 39,7\nxmu 32,s72,32\nxmu 33,s72,33\nxmu 34,s72,34\nxmu 35,s72,35\nxmu 36,s72,36\nxmu 37,s72,37\n"
"xmu 38,s72,38\nxmu 39,s72,39\nxcv 48,32,33\nxcv 49,34,35\nxcv 50,36,37\nxcv 51,38,39\nxst1 48,30,40,0,0\nxar 40,8\nxar 41,9\nxar 42,10\nxar 43,11\n"
"xar 44,12\nxar 45,13\nxar 46,14\nxar 47,15\nxmu 40,s72,40\nxmu 41,s72,41\nxmu 42,s72,42\nxmu 43,s72,43\nxmu 44,s72,44\nxmu 45,s72,45\nxmu 46,s72,46\n"
"xmu 47,s72,47\nxcv 52,40,41\nxcv 53,42,43\nxcv 54,44,45\nxcv 55,46,47\nxst1 52,30,40,0,2048\nxar 32,16\nxar 33,17\nxar 34,18\nxar 35,19\nxar 36,20\n"
"xar 37,21\nxar 38,22\nxar 39,23\nxmu 32,s72,32\nxmu 33,s72,33\nxmu 34,s72,34\nxmu 35,s72,35\nxmu 36,s72,36\nxmu 37,s72,37\nxmu 38,s72,38\n"
"xmu 39,s72,39\nxcv 56,32,33\nxcv 57,34,35\nxcv 58,36,37\nxcv 59,38,39\ns_mov_b32 s68,0x1000\nxst1 56,30,40,s68,0\nxar 40,24\nxar 41,25\nxar 42,26\n"
"xar 43,27\nxar 44,28\nxar 45,29\nxar 46,30\nxar 47,31\nxmu 40,s72,40\nxmu 41,s72,41\nxmu 42,s72,42\nxmu 43,s72,43\nxmu 44,s72,44\nxmu 45,s72,45\n"
"xmu 46,s72,46\nxmu 47,s72,47\nxcv 60,40,41\nxcv 61,42,43\nxcv 62,44,45\nxcv 63,46,47\nxst1 60,30,40,s68,2048\nxar 32,32\nxar 33,33\nxar 34,34\n"
"xar 35,35\nxar 36,36\nxar 37,37\nxar 38,38\nxar 39,39\nxmu 32,s72,32\nxmu 33,s72,33\nxmu 34,s72,34\nxmu 35,s72,35\nxmu 36,s72,36\nxmu 37,s72,37\n"
"xmu 38,s72,38\nxmu 39,s72,39\nxcv 48,32,33\nxcv 49,34,35\nxcv 50,36,37\nxcv 51,38,39\nxst1 48,30,40,0,256\nxar 40,40\nxar 41,41\nxar 42,42\n"
"xar 43,43\nxar 44,44\nxar 45,45\nxar 46,46\nxar 47,47\nxmu 40,s72,40\nxmu 41,s72,41\nxmu 42,s72,42\nxmu 43,s72,43\nxmu 44,s72,44\nxmu 45,s72,45\n"
"xmu 46,s72,46\nxmu 47,s72,47\nxcv 52,40,41\nxcv 53,42,43\nxcv 54,44,45\nxcv 55,46,47\nxst1 52,30,40,0,2304\nxar 32,48\nxar 33,49\nxar 34,50\n"
"xar 35,51\nxar 36,52\nxar 37,53\nxar 38,54\nxar 39,55\nxmu 32,s72,32\nxmu 33,s72,33\nxmu 34,s72,34\nxmu 35,s72,35\nxmu 36,s72,36\nxmu 37,s72,37\n"
"xmu 38,s72,38\nxmu 39,s72,39\nxcv 56,32,33\nxcv 57,34,35\nxcv 58,36,37\nxcv 59,38,39\nxst1 56,30,40,s68,256\nxar 40,56\nxar 41,57\nxar 42,58\n"
"xar 43,59\nxar 44,60\nxar 45,61\nxar 46,62\nxar 47,63\nxmu 40,s72,40\nxmu 41,s72,41\nxmu 42,s72,42\nxmu 43,s72,43\nxmu 44,s72,44\nxmu 45,s72,45\n"
"xmu 46,s72,46\nxmu 47,s72,47\nxcv 60,40,41\nxcv 61,42,43\nxcv 62,44,45\nxcv 63,46,47\nxst1 60,30,40,s68,2304\nxar 32,64\nxar 33,65\nxar 34,66\n"
"xar 35,67\nxar 36,68\nxar 37,69\nxar 38,70\nxar 39,71\nxmu 32,s72,32\nxmu 33,s72,33\nxmu 34,s72,34\nxmu 35,s72,35\nxmu 36,s72,36\nxmu 37,s72,37\n"
"xmu 38,s72,38\nxmu 39,s72,39\nxcv 48,32,33\nxcv 49,34,35\nxcv 50,36,37\nxcv 51,38,39\ns_mov_b32 s68,0x4000\nxst1 48,30,40,s68,0\nxar 40,72\n"
"xar 41,73\nxar 42,74\nxar 43,75\nxar 44,76\nxar 45,77\nxar 46,78\nxar 47,79\nxmu 40,s72,40\nxmu 41,s72,41\nxmu 42,s72,42\nxmu 43,s72,43\n"
"xmu 44,s72,44\nxmu 45,s72,45\nxmu 46,s72,46\nxmu 47,s72,47\nxcv 52,40,41\nxcv 53,42,43\nxcv 54,44,45\nxcv 55,46,47\nxst1 52,30,40,s68,2048\n"
"xar 32,80\nxar 33,81\nxar 34,82\nxar 35,83\nxar 36,84\nxar 37,85\nxar 38,86\nxar 39,87\nxmu 32,s72,32\nxmu 33,s72,33\nxmu 34,s72,34\nxmu 35,s72,35\n"
"xmu 36,s72,36\nxmu 37,s72,37\nxmu 38,s72,38\nxmu 39,s72,39\nxcv 56,32,33\nxcv 57,34,35\nxcv 58,36,37\nxcv 59,38,39\ns_mov_b32 s68,0x5000\n"
"xst1 56,30,40,s68,0\nxar 40,88\nxar 41,89\nxar 42,90\nxar 43,91\nxar 44,92\nxar 45,93\nxar 46,94\nxar 47,95\nxmu 40,s72,40\nxmu 41,s72,41\n"
"xmu 42,s72,42\nxmu 43,s72,43\nxmu 44,s72,44\nxmu 45,s72,45\nxmu 46,s72,46\nxmu 47,s72,47\nxcv 60,40,41\nxcv 61,42,43\nxcv 62,44,45\nxcv 63,46,47\n"
"xst1 60,30,40,s68,2048\nxar 32,96\nxar 33,97\nxar 34,98\nxar 35,99\nxar 36,100\nxar 37,101\nxar 38,102\nxar 39,103\nxmu 32,s72,32\nxmu 33,s72,33\n"
"xmu 34,s72,34\nxmu 35,s72,35\nxmu 36,s72,36\nxmu 37,s72,37\nxmu 38,s72,38\nxmu 39,s72,39\nxcv 48,32,33\nxcv 49,34,35\nxcv 50,36,37\nxcv 51,38,39\n"
"s_mov_b32 s68,0x4000\nxst1 48,30,40,s68,256\nxar 40,104\nxar 41,105\nxar 42,106\nxar 43,107\nxar 44,108\nxar 45,109\nxar 46,110\nxar 47,111\n"
"xmu 40,s72,40\nxmu 41,s72,41\nxmu 42,s72,42\nxmu 43,s72,43\nxmu 44,s72,44\nxmu 45,s72,45\nxmu 46,s72,46\nxmu 47,s72,47\nxcv 52,40,41\nxcv 53,42,43\n"
"xcv 54,44,45\nxcv 55,46,47\nxst1 52,30,40,s68,2304\nxar 32,112\nxar 33,113\nxar 34,114\nxar 35,115\nxar 36,116\nxar 37,117\nxar 38,118\nxar 39,119\n"
"xmu 32,s72,32\nxmu 33,s72,33\nxmu 34,s72,34\nxmu 35,s72,35\nxmu 36,s72,36\nxmu 37,s72,37\nxmu 38,s72,38\nxmu 39,s72,39\nxcv 56,32,33\nxcv 57,34,35\n"
"xcv 58,36,37\nxcv 59,38,39\ns_mov_b32 s68,0x5000\nxst1 56,30,40,s68,256\nxar 40,120\nxar 41,121\nxar 42,122\nxar 43,123\nxar 44,124\nxar 45,125\n"
"xar 46,126\nxar 47,127\nxmu 40,s72,40\nxmu 41,s72,41\nxmu 42,s72,42\nxmu 43,s72,43\nxmu 44,s72,44\nxmu 45,s72,45\nxmu 46,s72,46\nxmu 47,s72,47\n"
"xcv 60,40,41\nxcv 61,42,43\nxcv 62,44,45\nxcv 63,46,47\nxst1 60,30,40,s68,2304\nxar 32,128\nxar 33,132\nxar 34,136\nxar 35,140\nxmu 32,0xbfb8aa3b,32\n"
"xmu 33,0xbfb8aa3b,33\nxmu 34,0xbfb8aa3b,34\nxmu 35,0xbfb8aa3b,35\nxex 32,32\nxex 33,33\nxex 34,34\nxex 35,35\nxad 32,1.0,32\nxad 33,1.0,33\n"
"xad 34,1.0,34\nxad 35,1.0,35\nxrc 32,32\nxrc 33,33\nxrc 34,34\nxrc 35,35\ns_nop 1\nbuffer_store_dword v32,v31,s[48:51],0 offen offset:0 sc1\n"
"buffer_store_dword v33,v31,s[48:51],0 offen offset:64 sc1\nbuffer_store_dword v34,v31,s[48:51],0 offen offset:128 sc1\n"
"buffer_store_dword v35,v31,s[48:51],0 offen offset:192 sc1\nLkv_3_%=:\ns_lshr_b32 s98,s100,16\ns_and_b32 s98,s98,1\ns_cmp_lg_u32 s98,0\n"
"s_cbranch_scc1 Lkvv_5_%=\ns_nop 4\nv_mbcnt_lo_u32_b32 v252,-1,0\nv_mbcnt_hi_u32_b32 v252,-1,v252\ns_mov_b32 s16,s28\ns_mov_b32 s20,s32\n"
"s_mov_b32 s17,s29\ns_mov_b32 s21,s33\ns_mov_b32 s18,s30\ns_mov_b32 s22,s34\ns_mov_b32 s19,s31\ns_mov_b32 s23,s35\ns_load_dwordx2 s[76:77],s[0:1],56\n"
"xwl 0\ns_lshr_b32 s99,s100,17\ns_and_b32 s99,s99,15\ns_lshl_b32 s99,s99,19\ns_add_u32 s28,s76,s99\ns_addc_u32 s29,s77,0\ns_and_b32 s29,s29,0xffff\n"
"s_lshr_b32 s99,s100,17\ns_and_b32 s99,s99,15\ns_lshl_b32 s99,s99,19\ns_sub_u32 s99,0x800000,s99\ns_lshr_b32 s98,s100,29\ns_and_b32 s98,s98,1\n"
"s_cmp_lg_u32 s98,0\ns_cselect_b32 s30,s99,0\ns_mov_b32 s31,0x20000\ns_lshr_b32 s98,s100,21\ns_and_b32 s98,s98,15\ns_lshl_b32 s98,s98,7\n"
"s_add_u32 s98,0x5000000,s98\ns_add_u32 s32,s4,s98\ns_addc_u32 s33,s5,0\ns_and_b32 s33,s33,0xffff\ns_lshr_b32 s98,s100,21\ns_and_b32 s98,s98,15\n"
"s_lshl_b32 s98,s98,7\ns_sub_u32 s98,0x380000,s98\ns_lshr_b32 s99,s100,29\ns_and_b32 s99,s99,1\ns_cmp_lg_u32 s99,0\ns_cselect_b32 s34,s98,0\n"
"s_mov_b32 s35,0x20000\ns_lshl_b32 s99,s2,15\ns_add_u32 s36,s76,s99\ns_addc_u32 s37,s77,0\ns_and_b32 s37,s37,0xffff\ns_mov_b32 s38,0x8000\n"
"s_mov_b32 s39,0x20000\nv_lshrrev_b32 v0,3,v252\ns_lshl_b32 s99,s3,6\nv_add_u32 v0,s99,v0\nv_lshlrev_b32 v0,11,v0\nv_and_b32 v191,7,v252\n"
"v_lshrrev_b32 v190,3,v252\nv_lshrrev_b32 v190,1,v190\nv_xor_b32 v191,v191,v190\nv_lshlrev_b32 v191,4,v191\nv_add_u32 v0,v0,v191\nv_xor_b32 v1,64,v0\n"
"v_lshrrev_b32 v2,4,v252\ns_and_b32 s99,s3,1\ns_lshl_b32 s99,s99,5\nv_add_u32 v2,s99,v2\nv_lshlrev_b32 v2,13,v2\ns_lshr_b32 s99,s3,1\n"
"s_lshl_b32 s99,s99,8\nv_add_u32 v2,s99,v2\nv_and_b32 v191,15,v252\nv_lshrrev_b32 v191,1,v191\nv_lshrrev_b32 v190,4,v252\nv_xor_b32 v191,v191,v190\n"
"v_lshlrev_b32 v191,1,v191\nv_and_b32 v190,15,v252\nv_and_b32 v190,1,v190\nv_or_b32 v191,v191,v190\nv_lshlrev_b32 v191,4,v191\nv_add_u32 v2,v2,v191\n"
"v_xor_b32 v3,0x80,v2\nv_and_b32 v6,15,v252\ns_lshr_b32 s99,s3,1\ns_lshl_b32 s99,s99,7\nv_add_u32 v6,s99,v6\nv_lshlrev_b32 v6,7,v6\n"
"v_lshrrev_b32 v191,4,v252\nv_and_b32 v190,15,v252\nv_lshrrev_b32 v190,1,v190\nv_xor_b32 v191,v191,v190\nv_lshlrev_b32 v191,4,v191\n"
"v_add_u32 v6,v6,v191\nv_add_u32 v6,0x18000,v6\nv_xor_b32 v7,64,v6\nv_lshrrev_b32 v250,4,v252\nv_lshlrev_b32 v250,3,v250\nv_and_b32 v191,15,v252\n"
"v_lshrrev_b32 v191,2,v191\nv_add_u32 v250,v250,v191\nv_lshlrev_b32 v250,8,v250\ns_and_b32 s99,s3,1\ns_lshl_b32 s99,s99,14\nv_add_u32 v250,s99,v250\n"
"v_and_b32 v191,3,v252\nv_lshlrev_b32 v191,3,v191\nv_add_u32 v250,v250,v191\nv_and_b32 v251,15,v252\nv_lshrrev_b32 v251,2,v251\n"
"v_lshrrev_b32 v191,4,v252\nv_and_b32 v191,1,v191\nv_lshlrev_b32 v191,2,v191\nv_or_b32 v251,v251,v191\nv_lshlrev_b32 v251,5,v251\nv_xor_b32 v8,0,v251\n"
"v_add_u32 v8,v8,v250\nv_xor_b32 v9,32,v251\nv_add_u32 v9,v9,v250\nv_xor_b32 v10,64,v251\nv_add_u32 v10,v10,v250\nv_xor_b32 v11,0x60,v251\n"
"v_add_u32 v11,v11,v250\nv_xor_b32 v12,0x80,v251\nv_add_u32 v12,v12,v250\nv_xor_b32 v13,0xa0,v251\nv_add_u32 v13,v13,v250\nv_xor_b32 v14,0xc0,v251\n"
"v_add_u32 v14,v14,v250\nv_xor_b32 v15,0xe0,v251\nv_add_u32 v15,v15,v250\nv_lshrrev_b32 v160,3,v252\ns_lshl_b32 s99,s3,5\nv_add_u32 v160,s99,v160\n"
"v_lshlrev_b32 v160,12,v160\nv_and_b32 v191,7,v252\nv_lshrrev_b32 v190,3,v252\nv_lshrrev_b32 v190,1,v190\nv_xor_b32 v191,v191,v190\n"
"v_lshlrev_b32 v191,4,v191\nv_add_u32 v160,v160,v191\nv_xor_b32 v161,64,v160\nv_lshrrev_b32 v162,3,v252\ns_lshl_b32 s99,s3,4\nv_add_u32 v162,s99,v162\n"
"v_mul_u32_u24 v162,0x700,v162\nv_and_b32 v191,7,v252\nv_lshrrev_b32 v191,1,v191\nv_lshrrev_b32 v190,3,v252\nv_lshrrev_b32 v190,1,v190\n"
"v_and_b32 v190,1,v190\nv_xor_b32 v191,v191,v190\nv_lshlrev_b32 v191,1,v191\nv_and_b32 v190,7,v252\nv_and_b32 v190,1,v190\nv_or_b32 v191,v191,v190\n"
"v_lshlrev_b32 v191,4,v191\nv_add_u32 v162,v162,v191\nv_xor_b32 v163,64,v162\nv_lshlrev_b32 v164,6,v252\ns_lshl_b32 s99,s3,12\n"
"v_add_u32 v164,s99,v164\nv_and_b32 v236,15,v252\ns_lshr_b32 s99,s3,1\ns_lshl_b32 s99,s99,6\nv_add_u32 v236,s99,v236\nv_lshlrev_b32 v236,7,v236\n"
"v_lshrrev_b32 v191,4,v252\nv_lshlrev_b32 v191,3,v191\nv_add_u32 v236,v236,v191\nv_and_b32 v237,15,v252\ns_lshr_b32 s99,s3,1\ns_lshl_b32 s99,s99,6\n"
"v_add_u32 v237,s99,v237\nv_mul_u32_u24 v237,0x700,v237\ns_and_b32 s99,s3,1\ns_lshl_b32 s99,s99,7\nv_add_u32 v237,s99,v237\nv_lshrrev_b32 v191,4,v252\n"
"v_lshlrev_b32 v191,3,v191\nv_add_u32 v237,v237,v191\nv_lshrrev_b32 v250,4,v252\nv_lshrrev_b32 v250,1,v250\nv_xor_b32 v250,1,v250\n"
"v_cvt_f32_u32 v238,v250\nv_mov_b32 v239,0x3a924925\ns_and_b32 s74,s100,15\ns_add_u32 s74,s74,9\ns_lshr_b32 s75,s74,4\ns_and_b32 s75,s75,1\n"
"s_and_b32 s99,s100,15\ns_add_u32 s99,s99,2\ns_lshr_b32 s99,s99,4\ns_xor_b32 s99,s99,1\ns_and_b32 s75,s75,s99\ns_mov_b32 s54,s75\n"
"s_and_b32 s99,s100,15\ns_sub_u32 s99,s99,7\ns_cmp_lg_u32 s75,0\ns_cselect_b32 s74,s99,0\ns_lshr_b32 s99,s100,4\ns_and_b32 s99,s99,15\n"
"s_mul_i32 s99,s99,0x38000\ns_add_u32 s99,0x5000000,s99\ns_lshl_b32 s98,s74,8\ns_add_u32 s99,s99,s98\ns_add_u32 s40,s4,s99\ns_addc_u32 s41,s5,0\n"
"s_and_b32 s41,s41,0xffff\ns_lshl_b32 s42,s74,8\ns_sub_u32 s42,0x38000,s42\ns_mov_b32 s43,0x20000\ns_lshr_b32 s99,s100,4\ns_and_b32 s99,s99,15\n"
"s_lshl_b32 s99,s99,14\ns_add_u32 s99,0x6980000,s99\ns_add_u32 s48,s4,s99\ns_addc_u32 s49,s5,0\ns_and_b32 s49,s49,0xffff\ns_mov_b32 s50,0x4000\n"
"s_mov_b32 s51,0x20000\ns_add_u32 s44,s4,0x6c00000\ns_addc_u32 s45,s5,0\ns_and_b32 s45,s45,0xffff\ns_mov_b32 s46,0x8000\ns_mov_b32 s47,0x20000\n"
"s_lshr_b32 s52,s100,4\ns_and_b32 s52,s52,15\ns_lshl_b32 s52,s52,7\ns_add_u32 s52,0x1000,s52\ns_lshr_b32 s53,s100,4\ns_and_b32 s53,s53,15\n"
"s_lshl_b32 s53,s53,4\ns_and_b32 s99,s100,15\ns_add_u32 s53,s53,s99\ns_lshl_b32 s53,s53,3\ns_add_u32 s53,0x1000,s53\ns_lshl_b32 s55,s74,4\n"
"s_lshr_b32 s99,s100,4\ns_and_b32 s99,s99,15\ns_add_u32 s55,s55,s99\ns_lshl_b32 s55,s55,3\ns_add_u32 s55,0x2000,s55\ns_lshr_b32 s56,s100,21\n"
"s_and_b32 s56,s56,15\ns_lshr_b32 s56,s56,1\ns_lshl_b32 s56,s56,7\ns_add_u32 s56,0x2000,s56\ns_lshr_b32 s57,s100,29\ns_and_b32 s57,s57,1\n"
"s_mov_b32 s74,0x4000\ns_mov_b32 s75,0x8000\ns_mov_b32 s76,0xc000\ns_mov_b32 s77,0x10000\ns_mov_b32 s78,0x14000\ns_mov_b32 s79,0x18000\n"
"s_mov_b32 s80,0x1c000\ns_mul_i32 s62,s3,0x2000\ns_mov_b32 s81,0x20000\ns_mov_b32 s82,0x28000\ns_mov_b32 s83,0x30000\ns_mov_b32 s84,0x38000\n"
"s_mul_i32 s63,s3,0x2000\ns_add_u32 s99,s63,0\ns_add_u32 s98,s99,0x18000\ns_mov_b32 s97,0\ns_mov_b32 s85,0x8000\ns_mov_b32 s86,0x10000\n"
"s_mov_b32 s87,0x18000\ns_mov_b32 s88,0x80\ns_mov_b32 s89,0x8080\ns_mov_b32 s90,0x10080\ns_mov_b32 s91,0x18080\ns_mul_i32 s65,s3,0x1000\n"
"s_mov_b32 s92,0x3800\ns_mov_b32 s93,0x1c000\ns_mov_b32 s94,0x1f800\ns_mul_i32 s66,s3,0x800\nv_mov_b32 v170,0\ns_waitcnt vmcnt(44)\ns_barrier\n"
"xrt 64,8,0\nxrt 66,8,1024\nxrt 68,9,0\nxrt 70,9,1024\nxrt 72,10,0\nxrt 74,10,1024\nxrt 76,11,0\nxrt 78,11,1024\nxrt 80,12,0\nxrt 82,12,1024\n"
"xrt 84,13,0\nxrt 86,13,1024\nxrt 88,14,0\nxrt 90,14,1024\nxrt 92,15,0\nxrt 94,15,1024\nxrb 32,6,0\nxrb 36,6,2048\nxrb 40,6,4096\nxrb 44,6,6144\n"
"xrb 48,6,8192\nxrb 52,6,10240\nxrb 56,6,12288\nxrb 60,6,14336\nxwl 7\nxm16z 0,64,32\nxrt 128,8,8192\nxm16z 4,68,32\nxm16z 8,72,32\nxrt 130,8,9216\n"
"xm16z 12,76,32\nxm16z 16,80,32\nxrt 132,9,8192\nxm16z 20,84,32\nxm16z 24,88,32\nxrt 134,9,9216\nxm16z 28,92,32\nxwl 10\nxm16z 32,64,36\n"
"xrt 136,10,8192\nxm16z 36,68,36\nxm16z 40,72,36\nxrt 138,10,9216\nxm16z 44,76,36\nxm16z 48,80,36\nxrt 140,11,8192\nxm16z 52,84,36\nxm16z 56,88,36\n"
"xrt 142,11,9216\nxm16z 60,92,36\nxwl 13\nxm16z 64,64,40\nxrt 144,12,8192\nxm16z 68,68,40\nxm16z 72,72,40\nxrt 146,12,9216\nxm16z 76,76,40\n"
"xm16z 80,80,40\nxrt 148,13,8192\nxm16z 84,84,40\nxm16z 88,88,40\nxrt 150,13,9216\nxm16z 92,92,40\nxwl 15\nxm16z 96,64,44\nxrt 152,14,8192\n"
"xm16z 100,68,44\nxm16z 104,72,44\nxrt 154,14,9216\nxm16z 108,76,44\nxm16z 112,80,44\nxrt 156,15,8192\nxm16z 116,84,44\nxm16z 120,88,44\n"
"xrt 158,15,9216\nxm16z 124,92,44\nxm16z 128,64,48\nxrb 96,7,0\nxm16z 132,68,48\nxm16z 136,72,48\nxrb 100,7,2048\nxm16z 140,76,48\nxm16z 144,80,48\n"
"xrb 104,7,4096\nxm16z 148,84,48\nxm16z 152,88,48\nxrb 108,7,6144\nxm16z 156,92,48\nxwl 15\nxm16z 160,64,52\nxrb 112,7,8192\nxm16z 164,68,52\n"
"xm16z 168,72,52\nxrb 116,7,10240\nxm16z 172,76,52\nxm16z 176,80,52\nxrb 120,7,12288\nxm16z 180,84,52\nxm16z 184,88,52\nxrb 124,7,14336\n"
"xm16z 188,92,52\nxm16z 192,64,56\nxm16z 196,68,56\nxm16z 200,72,56\nxm16z 204,76,56\nxm16z 208,80,56\nxm16z 212,84,56\nxm16z 216,88,56\n"
"xm16z 220,92,56\nxm16z 224,64,60\nxm16z 228,68,60\nxm16z 232,72,60\nxm16z 236,76,60\nxm16z 240,80,60\nxm16z 244,84,60\nxm16z 248,88,60\n"
"xm16z 252,92,60\ns_waitcnt vmcnt(28) lgkmcnt(0)\ns_barrier\ns_add_u32 s97,s97,0x8000\ns_mov_b32 s96,0x8000\ns_cmp_ge_u32 s97,0x18000\n"
"s_cselect_b32 s96,0xffff0000,s96\ns_cselect_b32 s97,0,s97\nxam 62,0x18000\nxm16 0,128,96\nxdl 0,16,0,0\nv_add_u32 v8,s96,v8\nxrt 64,8,0\n"
"xm16 4,132,96\nxm16 8,136,96\nxrt 66,8,1024\nxm16 12,140,96\nxm16 16,144,96\nv_add_u32 v9,s96,v9\nxrt 68,9,0\nxm16 20,148,96\nxm16 24,152,96\n"
"xrt 70,9,1024\nxam 62,0x18400\nxm16 28,156,96\nxdl 1,16,s74,0\nxm16 32,128,100\nv_add_u32 v10,s96,v10\nxrt 72,10,0\nxm16 36,132,100\nxm16 40,136,100\n"
"xrt 74,10,1024\nxm16 44,140,100\nxm16 48,144,100\nv_add_u32 v11,s96,v11\nxrt 76,11,0\nxm16 52,148,100\nxam 62,0x18800\nxm16 56,152,100\n"
"xdl 0,16,s75,0\nxrt 78,11,1024\nxm16 60,156,100\nxm16 64,128,104\nv_add_u32 v12,s96,v12\nxrt 80,12,0\nxm16 68,132,104\nxm16 72,136,104\n"
"xrt 82,12,1024\nxm16 76,140,104\nxm16 80,144,104\nv_add_u32 v13,s96,v13\nxrt 84,13,0\nxam 62,0x18c00\nxm16 84,148,104\nxdl 1,16,s76,0\n"
"xm16 88,152,104\nxrt 86,13,1024\nxm16 92,156,104\nxm16 96,128,108\nv_add_u32 v14,s96,v14\nxrt 88,14,0\nxm16 100,132,108\nxm16 104,136,108\n"
"xrt 90,14,1024\nxm16 108,140,108\nxam 62,0x19000\nxm16 112,144,108\nxdl 0,16,s77,0\nv_add_u32 v15,s96,v15\nxrt 92,15,0\nxm16 116,148,108\n"
"xm16 120,152,108\nxrt 94,15,1024\nxm16 124,156,108\nxm16 128,128,112\nxrb 32,6,32768\nxm16 132,132,112\nxm16 136,136,112\nxrb 36,6,34816\n"
"xam 62,0x19400\nxm16 140,140,112\nxdl 1,16,s78,0\nxm16 144,144,112\nxrb 40,6,36864\nxm16 148,148,112\nxm16 152,152,112\nxrb 44,6,38912\n"
"xm16 156,156,112\nxm16 160,128,116\nxrb 48,6,40960\nxm16 164,132,116\nxam 62,0x19800\nxm16 168,136,116\nxdl 0,16,s79,0\nxrb 52,6,43008\n"
"xm16 172,140,116\nxm16 176,144,116\nxrb 56,6,45056\nxm16 180,148,116\nxm16 184,152,116\nxrb 60,6,47104\nxm16 188,156,116\nxm16 192,128,120\n"
"xam 62,0x19c00\nxm16 196,132,120\nxdl 1,16,s80,0\ns_add_u32 s16,s16,0x80\ns_addc_u32 s17,s17,0\ns_sub_u32 s18,s18,0x80\nxm16 200,136,120\n"
"xm16 204,140,120\nxm16 208,144,120\nxm16 212,148,120\nxm16 216,152,120\nxm16 220,156,120\nxm16 224,128,124\nxm16 228,132,124\nxm16 232,136,124\n"
"xm16 236,140,124\nxm16 240,144,124\nxm16 244,148,124\nxm16 248,152,124\nxm16 252,156,124\nxam 99,0\nxwl 7\nxm16 0,64,32\nxdl 2,20,0,0\n"
"xrt 128,8,8192\nxm16 4,68,32\nxm16 8,72,32\nxrt 130,8,9216\nxm16 12,76,32\nxm16 16,80,32\nxrt 132,9,8192\nxm16 20,84,32\nxm16 24,88,32\n"
"xrt 134,9,9216\nxam 99,0x400\nxm16 28,92,32\nxdl 2,20,s75,0\nxwl 10\nxm16 32,64,36\nxrt 136,10,8192\nxm16 36,68,36\nxm16 40,72,36\nxrt 138,10,9216\n"
"xm16 44,76,36\nxm16 48,80,36\nxrt 140,11,8192\nxm16 52,84,36\nxam 99,0x800\nxm16 56,88,36\nxdl 3,20,s77,0\nxrt 142,11,9216\nxm16 60,92,36\nxwl 13\n"
"xm16 64,64,40\nxrt 144,12,8192\nxm16 68,68,40\nxm16 72,72,40\nxrt 146,12,9216\nxm16 76,76,40\nxm16 80,80,40\nxrt 148,13,8192\nxam 99,0xc00\n"
"xm16 84,84,40\nxdl 3,20,s79,0\nxm16 88,88,40\nxrt 150,13,9216\nxm16 92,92,40\nxwl 15\nxm16 96,64,44\nxrt 152,14,8192\nxm16 100,68,44\nxm16 104,72,44\n"
"xrt 154,14,9216\nxm16 108,76,44\nxam 99,0x1000\nxm16 112,80,44\nxdl 2,20,s81,0\nxrt 156,15,8192\nxm16 116,84,44\nxm16 120,88,44\nxrt 158,15,9216\n"
"xm16 124,92,44\nxm16 128,64,48\nxrb 96,7,32768\nxm16 132,68,48\nxm16 136,72,48\nxrb 100,7,34816\nxam 99,0x1400\nxm16 140,76,48\nxdl 2,20,s82,0\n"
"xm16 144,80,48\nxrb 104,7,36864\nxm16 148,84,48\nxm16 152,88,48\nxrb 108,7,38912\nxm16 156,92,48\nxwl 15\nxm16 160,64,52\nxrb 112,7,40960\n"
"xm16 164,68,52\nxam 99,0x1800\nxm16 168,72,52\nxdl 3,20,s83,0\nxrb 116,7,43008\nxm16 172,76,52\nxm16 176,80,52\nxrb 120,7,45056\nxm16 180,84,52\n"
"xm16 184,88,52\nxrb 124,7,47104\nxm16 188,92,52\nxm16 192,64,56\nxam 99,0x1c00\nxm16 196,68,56\nxdl 3,20,s84,0\ns_add_u32 s99,s99,0x8000\n"
"s_cmp_ge_u32 s99,s98\ns_cselect_b32 s95,0x18000,0\ns_sub_u32 s99,s99,s95\ns_add_u32 s20,s20,0x80000\ns_addc_u32 s21,s21,0\ns_sub_u32 s22,s22,0x80000\n"
"xm16 200,72,56\nxm16 204,76,56\nxm16 208,80,56\nxm16 212,84,56\nxm16 216,88,56\nxm16 220,92,56\nxm16 224,64,60\nxm16 228,68,60\nxm16 232,72,60\n"
"xm16 236,76,60\nxm16 240,80,60\nxm16 244,84,60\nxm16 248,88,60\nxm16 252,92,60\nbuffer_load_dword v171,v164,s[36:39],0 offen offset:0\n"
"s_mov_b32 s68,0x4000\nbuffer_load_dword v171,v164,s[36:39],s68 offen offset:0\ns_waitcnt vmcnt(8) lgkmcnt(0)\ns_barrier\ns_cmp_eq_u32 s3,0\n"
"s_cbranch_scc0 Lnosig_16_%=\nv_mov_b32 v166,s6\nv_mov_b32 v167,s7\nv_mov_b32 v169,0\ns_mov_b64 exec,1\n"
"buffer_store_dwordx2 v[166:167],v169,s[44:47],s53 offen offset:0 sc1\ns_mov_b64 exec,-1\nLnosig_16_%=:\ns_nop 1\ns_add_u32 s97,s97,0x8000\n"
"s_mov_b32 s96,0x8000\ns_cmp_ge_u32 s97,0x18000\ns_cselect_b32 s96,0xffff0000,s96\ns_cselect_b32 s97,0,s97\nxam 62,0x20000\nxm16 0,128,96\n"
"xdl 0,16,0,0\nv_add_u32 v8,s96,v8\nxrt 64,8,0\nxm16 4,132,96\nxm16 8,136,96\nxrt 66,8,1024\nxm16 12,140,96\nxm16 16,144,96\nv_add_u32 v9,s96,v9\n"
"xrt 68,9,0\nxm16 20,148,96\nxm16 24,152,96\nxrt 70,9,1024\nxam 62,0x20400\nxm16 28,156,96\nxdl 1,16,s74,0\nxm16 32,128,100\nv_add_u32 v10,s96,v10\n"
"xrt 72,10,0\nxm16 36,132,100\nxm16 40,136,100\nxrt 74,10,1024\nxm16 44,140,100\nxm16 48,144,100\nv_add_u32 v11,s96,v11\nxrt 76,11,0\nxm16 52,148,100\n"
"xam 62,0x20800\nxm16 56,152,100\nxdl 0,16,s75,0\nxrt 78,11,1024\nxm16 60,156,100\nxm16 64,128,104\nv_add_u32 v12,s96,v12\nxrt 80,12,0\n"
"xm16 68,132,104\nxm16 72,136,104\nxrt 82,12,1024\nxm16 76,140,104\nxm16 80,144,104\nv_add_u32 v13,s96,v13\nxrt 84,13,0\nxam 62,0x20c00\n"
"xm16 84,148,104\nxdl 1,16,s76,0\nxm16 88,152,104\nxrt 86,13,1024\nxm16 92,156,104\nxm16 96,128,108\nv_add_u32 v14,s96,v14\nxrt 88,14,0\n"
"xm16 100,132,108\nxm16 104,136,108\nxrt 90,14,1024\nxm16 108,140,108\nxam 62,0x21000\nxm16 112,144,108\nxdl 0,16,s77,0\nv_add_u32 v15,s96,v15\n"
"xrt 92,15,0\nxm16 116,148,108\nxm16 120,152,108\nxrt 94,15,1024\nxm16 124,156,108\nxm16 128,128,112\nxrb 32,6,0\nxm16 132,132,112\nxm16 136,136,112\n"
"xrb 36,6,2048\nxam 62,0x21400\nxm16 140,140,112\nxdl 1,16,s78,0\nxm16 144,144,112\nxrb 40,6,4096\nxm16 148,148,112\nxm16 152,152,112\nxrb 44,6,6144\n"
"xm16 156,156,112\nxm16 160,128,116\nxrb 48,6,8192\nxm16 164,132,116\nxam 62,0x21800\nxm16 168,136,116\nxdl 0,16,s79,0\nxrb 52,6,10240\n"
"xm16 172,140,116\nxm16 176,144,116\nxrb 56,6,12288\nxm16 180,148,116\nxm16 184,152,116\nxrb 60,6,14336\nxm16 188,156,116\nxm16 192,128,120\n"
"xam 62,0x21c00\nxm16 196,132,120\nxdl 1,16,s80,0\ns_add_u32 s16,s16,0x80\ns_addc_u32 s17,s17,0\ns_sub_u32 s18,s18,0x80\nxm16 200,136,120\n"
"xm16 204,140,120\nxm16 208,144,120\nxm16 212,148,120\nxm16 216,152,120\nxm16 220,156,120\nxm16 224,128,124\nxm16 228,132,124\nxm16 232,136,124\n"
"xm16 236,140,124\nxm16 240,144,124\nxm16 244,148,124\nxm16 248,152,124\nxm16 252,156,124\ns_mov_b32 s71,5\nLloop_17_%=:\nxam 99,0\nxwl 7\n"
"xm16 0,64,32\nxdl 2,20,0,0\nxrt 128,8,8192\nxm16 4,68,32\nxm16 8,72,32\nxrt 130,8,9216\nxm16 12,76,32\nxm16 16,80,32\nxrt 132,9,8192\nxm16 20,84,32\n"
"xm16 24,88,32\nxrt 134,9,9216\nxam 99,0x400\nxm16 28,92,32\nxdl 2,20,s75,0\nxwl 10\nxm16 32,64,36\nxrt 136,10,8192\nxm16 36,68,36\nxm16 40,72,36\n"
"xrt 138,10,9216\nxm16 44,76,36\nxm16 48,80,36\nxrt 140,11,8192\nxm16 52,84,36\nxam 99,0x800\nxm16 56,88,36\nxdl 3,20,s77,0\nxrt 142,11,9216\n"
"xm16 60,92,36\nxwl 13\nxm16 64,64,40\nxrt 144,12,8192\nxm16 68,68,40\nxm16 72,72,40\nxrt 146,12,9216\nxm16 76,76,40\nxm16 80,80,40\nxrt 148,13,8192\n"
"xam 99,0xc00\nxm16 84,84,40\nxdl 3,20,s79,0\nxm16 88,88,40\nxrt 150,13,9216\nxm16 92,92,40\nxwl 15\nxm16 96,64,44\nxrt 152,14,8192\nxm16 100,68,44\n"
"xm16 104,72,44\nxrt 154,14,9216\nxm16 108,76,44\nxam 99,0x1000\nxm16 112,80,44\nxdl 2,20,s81,0\nxrt 156,15,8192\nxm16 116,84,44\nxm16 120,88,44\n"
"xrt 158,15,9216\nxm16 124,92,44\nxm16 128,64,48\nxrb 96,7,0\nxm16 132,68,48\nxm16 136,72,48\nxrb 100,7,2048\nxam 99,0x1400\nxm16 140,76,48\n"
"xdl 2,20,s82,0\nxm16 144,80,48\nxrb 104,7,4096\nxm16 148,84,48\nxm16 152,88,48\nxrb 108,7,6144\nxm16 156,92,48\nxwl 15\nxm16 160,64,52\n"
"xrb 112,7,8192\nxm16 164,68,52\nxam 99,0x1800\nxm16 168,72,52\nxdl 3,20,s83,0\nxrb 116,7,10240\nxm16 172,76,52\nxm16 176,80,52\nxrb 120,7,12288\n"
"xm16 180,84,52\nxm16 184,88,52\nxrb 124,7,14336\nxm16 188,92,52\nxm16 192,64,56\nxam 99,0x1c00\nxm16 196,68,56\nxdl 3,20,s84,0\n"
"s_add_u32 s99,s99,0x8000\ns_cmp_ge_u32 s99,s98\ns_cselect_b32 s95,0x18000,0\ns_sub_u32 s99,s99,s95\ns_add_u32 s20,s20,0x80000\ns_addc_u32 s21,s21,0\n"
"s_sub_u32 s22,s22,0x80000\nxm16 200,72,56\nxm16 204,76,56\nxm16 208,80,56\nxm16 212,84,56\nxm16 216,88,56\nxm16 220,92,56\nxm16 224,64,60\n"
"xm16 228,68,60\nxm16 232,72,60\nxm16 236,76,60\nxm16 240,80,60\nxm16 244,84,60\nxm16 248,88,60\nxm16 252,92,60\ns_waitcnt vmcnt(8) lgkmcnt(0)\n"
"s_barrier\ns_cmp_eq_u32 s71,4\ns_cbranch_scc1 Llnhook_7_%=\nLlnret_8_%=:\ns_cmp_eq_u32 s71,3\ns_cbranch_scc1 Lznhook_9_%=\nLznret_10_%=:\n"
"s_add_u32 s97,s97,0x8000\ns_mov_b32 s96,0x8000\ns_cmp_ge_u32 s97,0x18000\ns_cselect_b32 s96,0xffff0000,s96\ns_cselect_b32 s97,0,s97\nxam 62,0x18000\n"
"xm16 0,128,96\nxdl 0,16,0,0\nv_add_u32 v8,s96,v8\nxrt 64,8,0\nxm16 4,132,96\nxm16 8,136,96\nxrt 66,8,1024\nxm16 12,140,96\nxm16 16,144,96\n"
"v_add_u32 v9,s96,v9\nxrt 68,9,0\nxm16 20,148,96\nxm16 24,152,96\nxrt 70,9,1024\nxam 62,0x18400\nxm16 28,156,96\nxdl 1,16,s74,0\nxm16 32,128,100\n"
"v_add_u32 v10,s96,v10\nxrt 72,10,0\nxm16 36,132,100\nxm16 40,136,100\nxrt 74,10,1024\nxm16 44,140,100\nxm16 48,144,100\nv_add_u32 v11,s96,v11\n"
"xrt 76,11,0\nxm16 52,148,100\nxam 62,0x18800\nxm16 56,152,100\nxdl 0,16,s75,0\nxrt 78,11,1024\nxm16 60,156,100\nxm16 64,128,104\n"
"v_add_u32 v12,s96,v12\nxrt 80,12,0\nxm16 68,132,104\nxm16 72,136,104\nxrt 82,12,1024\nxm16 76,140,104\nxm16 80,144,104\nv_add_u32 v13,s96,v13\n"
"xrt 84,13,0\nxam 62,0x18c00\nxm16 84,148,104\nxdl 1,16,s76,0\nxm16 88,152,104\nxrt 86,13,1024\nxm16 92,156,104\nxm16 96,128,108\n"
"v_add_u32 v14,s96,v14\nxrt 88,14,0\nxm16 100,132,108\nxm16 104,136,108\nxrt 90,14,1024\nxm16 108,140,108\nxam 62,0x19000\nxm16 112,144,108\n"
"xdl 0,16,s77,0\nv_add_u32 v15,s96,v15\nxrt 92,15,0\nxm16 116,148,108\nxm16 120,152,108\nxrt 94,15,1024\nxm16 124,156,108\nxm16 128,128,112\n"
"xrb 32,6,32768\nxm16 132,132,112\nxm16 136,136,112\nxrb 36,6,34816\nxam 62,0x19400\nxm16 140,140,112\nxdl 1,16,s78,0\nxm16 144,144,112\n"
"xrb 40,6,36864\nxm16 148,148,112\nxm16 152,152,112\nxrb 44,6,38912\nxm16 156,156,112\nxm16 160,128,116\nxrb 48,6,40960\nxm16 164,132,116\n"
"xam 62,0x19800\nxm16 168,136,116\nxdl 0,16,s79,0\nxrb 52,6,43008\nxm16 172,140,116\nxm16 176,144,116\nxrb 56,6,45056\nxm16 180,148,116\n"
"xm16 184,152,116\nxrb 60,6,47104\nxm16 188,156,116\nxm16 192,128,120\nxam 62,0x19c00\nxm16 196,132,120\nxdl 1,16,s80,0\ns_add_u32 s16,s16,0x80\n"
"s_addc_u32 s17,s17,0\ns_sub_u32 s18,s18,0x80\nxm16 200,136,120\nxm16 204,140,120\nxm16 208,144,120\nxm16 212,148,120\nxm16 216,152,120\n"
"xm16 220,156,120\nxm16 224,128,124\nxm16 228,132,124\nxm16 232,136,124\nxm16 236,140,124\nxm16 240,144,124\nxm16 244,148,124\nxm16 248,152,124\n"
"xm16 252,156,124\nxam 99,0\nxwl 7\nxm16 0,64,32\nxdl 2,20,0,0\nxrt 128,8,8192\nxm16 4,68,32\nxm16 8,72,32\nxrt 130,8,9216\nxm16 12,76,32\n"
"xm16 16,80,32\nxrt 132,9,8192\nxm16 20,84,32\nxm16 24,88,32\nxrt 134,9,9216\nxam 99,0x400\nxm16 28,92,32\nxdl 2,20,s75,0\nxwl 10\nxm16 32,64,36\n"
"xrt 136,10,8192\nxm16 36,68,36\nxm16 40,72,36\nxrt 138,10,9216\nxm16 44,76,36\nxm16 48,80,36\nxrt 140,11,8192\nxm16 52,84,36\nxam 99,0x800\n"
"xm16 56,88,36\nxdl 3,20,s77,0\nxrt 142,11,9216\nxm16 60,92,36\nxwl 13\nxm16 64,64,40\nxrt 144,12,8192\nxm16 68,68,40\nxm16 72,72,40\nxrt 146,12,9216\n"
"xm16 76,76,40\nxm16 80,80,40\nxrt 148,13,8192\nxam 99,0xc00\nxm16 84,84,40\nxdl 3,20,s79,0\nxm16 88,88,40\nxrt 150,13,9216\nxm16 92,92,40\nxwl 15\n"
"xm16 96,64,44\nxrt 152,14,8192\nxm16 100,68,44\nxm16 104,72,44\nxrt 154,14,9216\nxm16 108,76,44\nxam 99,0x1000\nxm16 112,80,44\nxdl 2,20,s81,0\n"
"xrt 156,15,8192\nxm16 116,84,44\nxm16 120,88,44\nxrt 158,15,9216\nxm16 124,92,44\nxm16 128,64,48\nxrb 96,7,32768\nxm16 132,68,48\nxm16 136,72,48\n"
"xrb 100,7,34816\nxam 99,0x1400\nxm16 140,76,48\nxdl 2,20,s82,0\nxm16 144,80,48\nxrb 104,7,36864\nxm16 148,84,48\nxm16 152,88,48\nxrb 108,7,38912\n"
"xm16 156,92,48\nxwl 15\nxm16 160,64,52\nxrb 112,7,40960\nxm16 164,68,52\nxam 99,0x1800\nxm16 168,72,52\nxdl 3,20,s83,0\nxrb 116,7,43008\n"
"xm16 172,76,52\nxm16 176,80,52\nxrb 120,7,45056\nxm16 180,84,52\nxm16 184,88,52\nxrb 124,7,47104\nxm16 188,92,52\nxm16 192,64,56\nxam 99,0x1c00\n"
"xm16 196,68,56\nxdl 3,20,s84,0\ns_add_u32 s99,s99,0x8000\ns_cmp_ge_u32 s99,s98\ns_cselect_b32 s95,0x18000,0\ns_sub_u32 s99,s99,s95\n"
"s_add_u32 s20,s20,0x80000\ns_addc_u32 s21,s21,0\ns_sub_u32 s22,s22,0x80000\nxm16 200,72,56\nxm16 204,76,56\nxm16 208,80,56\nxm16 212,84,56\n"
"xm16 216,88,56\nxm16 220,92,56\nxm16 224,64,60\nxm16 228,68,60\nxm16 232,72,60\nxm16 236,76,60\nxm16 240,80,60\nxm16 244,84,60\nxm16 248,88,60\n"
"xm16 252,92,60\ns_waitcnt vmcnt(8) lgkmcnt(0)\ns_barrier\ns_add_u32 s97,s97,0x8000\ns_mov_b32 s96,0x8000\ns_cmp_ge_u32 s97,0x18000\n"
"s_cselect_b32 s96,0xffff0000,s96\ns_cselect_b32 s97,0,s97\nxam 62,0x20000\nxm16 0,128,96\nxdl 0,16,0,0\nv_add_u32 v8,s96,v8\nxrt 64,8,0\n"
"xm16 4,132,96\nxm16 8,136,96\nxrt 66,8,1024\nxm16 12,140,96\nxm16 16,144,96\nv_add_u32 v9,s96,v9\nxrt 68,9,0\nxm16 20,148,96\nxm16 24,152,96\n"
"xrt 70,9,1024\nxam 62,0x20400\nxm16 28,156,96\nxdl 1,16,s74,0\nxm16 32,128,100\nv_add_u32 v10,s96,v10\nxrt 72,10,0\nxm16 36,132,100\nxm16 40,136,100\n"
"xrt 74,10,1024\nxm16 44,140,100\nxm16 48,144,100\nv_add_u32 v11,s96,v11\nxrt 76,11,0\nxm16 52,148,100\nxam 62,0x20800\nxm16 56,152,100\n"
"xdl 0,16,s75,0\nxrt 78,11,1024\nxm16 60,156,100\nxm16 64,128,104\nv_add_u32 v12,s96,v12\nxrt 80,12,0\nxm16 68,132,104\nxm16 72,136,104\n"
"xrt 82,12,1024\nxm16 76,140,104\nxm16 80,144,104\nv_add_u32 v13,s96,v13\nxrt 84,13,0\nxam 62,0x20c00\nxm16 84,148,104\nxdl 1,16,s76,0\n"
"xm16 88,152,104\nxrt 86,13,1024\nxm16 92,156,104\nxm16 96,128,108\nv_add_u32 v14,s96,v14\nxrt 88,14,0\nxm16 100,132,108\nxm16 104,136,108\n"
"xrt 90,14,1024\nxm16 108,140,108\nxam 62,0x21000\nxm16 112,144,108\nxdl 0,16,s77,0\nv_add_u32 v15,s96,v15\nxrt 92,15,0\nxm16 116,148,108\n"
"xm16 120,152,108\nxrt 94,15,1024\nxm16 124,156,108\nxm16 128,128,112\nxrb 32,6,0\nxm16 132,132,112\nxm16 136,136,112\nxrb 36,6,2048\nxam 62,0x21400\n"
"xm16 140,140,112\nxdl 1,16,s78,0\nxm16 144,144,112\nxrb 40,6,4096\nxm16 148,148,112\nxm16 152,152,112\nxrb 44,6,6144\nxm16 156,156,112\n"
"xm16 160,128,116\nxrb 48,6,8192\nxm16 164,132,116\nxam 62,0x21800\nxm16 168,136,116\nxdl 0,16,s79,0\nxrb 52,6,10240\nxm16 172,140,116\n"
"xm16 176,144,116\nxrb 56,6,12288\nxm16 180,148,116\nxm16 184,152,116\nxrb 60,6,14336\nxm16 188,156,116\nxm16 192,128,120\nxam 62,0x21c00\n"
"xm16 196,132,120\nxdl 1,16,s80,0\ns_add_u32 s16,s16,0x80\ns_addc_u32 s17,s17,0\ns_sub_u32 s18,s18,0x80\nxm16 200,136,120\nxm16 204,140,120\n"
"xm16 208,144,120\nxm16 212,148,120\nxm16 216,152,120\nxm16 220,156,120\nxm16 224,128,124\nxm16 228,132,124\nxm16 232,136,124\nxm16 236,140,124\n"
"xm16 240,144,124\nxm16 244,148,124\nxm16 248,152,124\nxm16 252,156,124\ns_sub_u32 s71,s71,1\ns_cmp_gt_i32 s71,0\ns_cbranch_scc1 Lloop_17_%=\n"
"xam 99,0\nxwl 7\nxm16 0,64,32\nxdl 2,20,0,0\nxrt 128,8,8192\nxm16 4,68,32\nxm16 8,72,32\nxrt 130,8,9216\nxm16 12,76,32\nxm16 16,80,32\n"
"xrt 132,9,8192\nxm16 20,84,32\nxm16 24,88,32\nxrt 134,9,9216\nxam 99,0x400\nxm16 28,92,32\nxdl 2,20,s75,0\nxwl 10\nxm16 32,64,36\nxrt 136,10,8192\n"
"xm16 36,68,36\nxm16 40,72,36\nxrt 138,10,9216\nxm16 44,76,36\nxm16 48,80,36\nxrt 140,11,8192\nxm16 52,84,36\nxam 99,0x800\nxm16 56,88,36\n"
"xdl 3,20,s77,0\nxrt 142,11,9216\nxm16 60,92,36\nxwl 13\nxm16 64,64,40\nxrt 144,12,8192\nxm16 68,68,40\nxm16 72,72,40\nxrt 146,12,9216\nxm16 76,76,40\n"
"xm16 80,80,40\nxrt 148,13,8192\nxam 99,0xc00\nxm16 84,84,40\nxdl 3,20,s79,0\nxm16 88,88,40\nxrt 150,13,9216\nxm16 92,92,40\nxwl 15\nxm16 96,64,44\n"
"xrt 152,14,8192\nxm16 100,68,44\nxm16 104,72,44\nxrt 154,14,9216\nxm16 108,76,44\nxam 99,0x1000\nxm16 112,80,44\nxdl 2,20,s81,0\nxrt 156,15,8192\n"
"xm16 116,84,44\nxm16 120,88,44\nxrt 158,15,9216\nxm16 124,92,44\nxm16 128,64,48\nxrb 96,7,0\nxm16 132,68,48\nxm16 136,72,48\nxrb 100,7,2048\n"
"xam 99,0x1400\nxm16 140,76,48\nxdl 2,20,s82,0\nxm16 144,80,48\nxrb 104,7,4096\nxm16 148,84,48\nxm16 152,88,48\nxrb 108,7,6144\nxm16 156,92,48\n"
"xwl 15\nxm16 160,64,52\nxrb 112,7,8192\nxm16 164,68,52\nxam 99,0x1800\nxm16 168,72,52\nxdl 3,20,s83,0\nxrb 116,7,10240\nxm16 172,76,52\n"
"xm16 176,80,52\nxrb 120,7,12288\nxm16 180,84,52\nxm16 184,88,52\nxrb 124,7,14336\nxm16 188,92,52\nxm16 192,64,56\nxam 99,0x1c00\nxm16 196,68,56\n"
"xdl 3,20,s84,0\ns_add_u32 s99,s99,0x8000\ns_cmp_ge_u32 s99,s98\ns_cselect_b32 s95,0x18000,0\ns_sub_u32 s99,s99,s95\ns_add_u32 s20,s20,0x80000\n"
"s_addc_u32 s21,s21,0\ns_sub_u32 s22,s22,0x80000\nxm16 200,72,56\nxm16 204,76,56\nxm16 208,80,56\nxm16 212,84,56\nxm16 216,88,56\nxm16 220,92,56\n"
"xm16 224,64,60\nxm16 228,68,60\nxm16 232,72,60\nxm16 236,76,60\nxm16 240,80,60\nxm16 244,84,60\nxm16 248,88,60\nxm16 252,92,60\n"
"s_waitcnt vmcnt(8) lgkmcnt(0)\ns_barrier\ns_add_u32 s97,s97,0x8000\ns_mov_b32 s96,0x8000\ns_cmp_ge_u32 s97,0x18000\ns_cselect_b32 s96,0xffff0000,s96\n"
"s_cselect_b32 s97,0,s97\nxam 62,0x18000\nxm16 0,128,96\nxdl 0,16,0,0\nv_add_u32 v8,s96,v8\nxrt 64,8,0\nxm16 4,132,96\nxm16 8,136,96\nxrt 66,8,1024\n"
"xm16 12,140,96\nxm16 16,144,96\nv_add_u32 v9,s96,v9\nxrt 68,9,0\nxm16 20,148,96\nxm16 24,152,96\nxrt 70,9,1024\nxam 62,0x18400\nxm16 28,156,96\n"
"xdl 1,16,s74,0\nxm16 32,128,100\nv_add_u32 v10,s96,v10\nxrt 72,10,0\nxm16 36,132,100\nxm16 40,136,100\nxrt 74,10,1024\nxm16 44,140,100\n"
"xm16 48,144,100\nv_add_u32 v11,s96,v11\nxrt 76,11,0\nxm16 52,148,100\nxam 62,0x18800\nxm16 56,152,100\nxdl 0,16,s75,0\nxrt 78,11,1024\n"
"xm16 60,156,100\nxm16 64,128,104\nv_add_u32 v12,s96,v12\nxrt 80,12,0\nxm16 68,132,104\nxm16 72,136,104\nxrt 82,12,1024\nxm16 76,140,104\n"
"xm16 80,144,104\nv_add_u32 v13,s96,v13\nxrt 84,13,0\nxam 62,0x18c00\nxm16 84,148,104\nxdl 1,16,s76,0\nxm16 88,152,104\nxrt 86,13,1024\n"
"xm16 92,156,104\nxm16 96,128,108\nv_add_u32 v14,s96,v14\nxrt 88,14,0\nxm16 100,132,108\nxm16 104,136,108\nxrt 90,14,1024\nxm16 108,140,108\n"
"xam 62,0x19000\nxm16 112,144,108\nxdl 0,16,s77,0\nv_add_u32 v15,s96,v15\nxrt 92,15,0\nxm16 116,148,108\nxm16 120,152,108\nxrt 94,15,1024\n"
"xm16 124,156,108\nxm16 128,128,112\nxrb 32,6,32768\nxm16 132,132,112\nxm16 136,136,112\nxrb 36,6,34816\nxam 62,0x19400\nxm16 140,140,112\n"
"xdl 1,16,s78,0\nxm16 144,144,112\nxrb 40,6,36864\nxm16 148,148,112\nxm16 152,152,112\nxrb 44,6,38912\nxm16 156,156,112\nxm16 160,128,116\n"
"xrb 48,6,40960\nxm16 164,132,116\nxam 62,0x19800\nxm16 168,136,116\nxdl 0,16,s79,0\nxrb 52,6,43008\nxm16 172,140,116\nxm16 176,144,116\n"
"xrb 56,6,45056\nxm16 180,148,116\nxm16 184,152,116\nxrb 60,6,47104\nxm16 188,156,116\nxm16 192,128,120\nxam 62,0x19c00\nxm16 196,132,120\n"
"xdl 1,16,s80,0\ns_add_u32 s16,s16,0x80\ns_addc_u32 s17,s17,0\ns_sub_u32 s18,s18,0x80\nxm16 200,136,120\nxm16 204,140,120\nxm16 208,144,120\n"
"xm16 212,148,120\nxm16 216,152,120\nxm16 220,156,120\nxm16 224,128,124\nxm16 228,132,124\nxm16 232,136,124\nxm16 236,140,124\nxm16 240,144,124\n"
"xm16 244,148,124\nxm16 248,152,124\nxm16 252,156,124\nxam 99,0\nxwl 7\nxm16 0,64,32\nxdl 2,20,0,0\nxrt 128,8,8192\nxm16 4,68,32\nxm16 8,72,32\n"
"xrt 130,8,9216\nxm16 12,76,32\nxm16 16,80,32\nxrt 132,9,8192\nxm16 20,84,32\nxm16 24,88,32\nxrt 134,9,9216\nxam 99,0x400\nxm16 28,92,32\n"
"xdl 2,20,s75,0\nxwl 10\nxm16 32,64,36\nxrt 136,10,8192\nxm16 36,68,36\nxm16 40,72,36\nxrt 138,10,9216\nxm16 44,76,36\nxm16 48,80,36\nxrt 140,11,8192\n"
"xm16 52,84,36\nxam 99,0x800\nxm16 56,88,36\nxdl 3,20,s77,0\nxrt 142,11,9216\nxm16 60,92,36\nxwl 13\nxm16 64,64,40\nxrt 144,12,8192\nxm16 68,68,40\n"
"xm16 72,72,40\nxrt 146,12,9216\nxm16 76,76,40\nxm16 80,80,40\nxrt 148,13,8192\nxam 99,0xc00\nxm16 84,84,40\nxdl 3,20,s79,0\nxm16 88,88,40\n"
"xrt 150,13,9216\nxm16 92,92,40\nxwl 15\nxm16 96,64,44\nxrt 152,14,8192\nxm16 100,68,44\nxm16 104,72,44\nxrt 154,14,9216\nxm16 108,76,44\n"
"xam 99,0x1000\nxm16 112,80,44\nxdl 2,20,s81,0\nxrt 156,15,8192\nxm16 116,84,44\nxm16 120,88,44\nxrt 158,15,9216\nxm16 124,92,44\nxm16 128,64,48\n"
"xrb 96,7,32768\nxm16 132,68,48\nxm16 136,72,48\nxrb 100,7,34816\nxam 99,0x1400\nxm16 140,76,48\nxdl 2,20,s82,0\nxm16 144,80,48\nxrb 104,7,36864\n"
"xm16 148,84,48\nxm16 152,88,48\nxrb 108,7,38912\nxm16 156,92,48\nxwl 15\nxm16 160,64,52\nxrb 112,7,40960\nxm16 164,68,52\nxam 99,0x1800\n"
"xm16 168,72,52\nxdl 3,20,s83,0\nxrb 116,7,43008\nxm16 172,76,52\nxm16 176,80,52\nxrb 120,7,45056\nxm16 180,84,52\nxm16 184,88,52\nxrb 124,7,47104\n"
"xm16 188,92,52\nxm16 192,64,56\nxam 99,0x1c00\nxm16 196,68,56\nxdl 3,20,s84,0\ns_add_u32 s99,s99,0x8000\ns_cmp_ge_u32 s99,s98\n"
"s_cselect_b32 s95,0x18000,0\ns_sub_u32 s99,s99,s95\ns_add_u32 s20,s20,0x80000\ns_addc_u32 s21,s21,0\ns_sub_u32 s22,s22,0x80000\nxm16 200,72,56\n"
"xm16 204,76,56\nxm16 208,80,56\nxm16 212,84,56\nxm16 216,88,56\nxm16 220,92,56\nxm16 224,64,60\nxm16 228,68,60\nxm16 232,72,60\nxm16 236,76,60\n"
"xm16 240,80,60\nxm16 244,84,60\nxm16 248,88,60\nxm16 252,92,60\ns_waitcnt vmcnt(8) lgkmcnt(0)\ns_barrier\nv_mbcnt_lo_u32_b32 v168,-1,0\n"
"v_mbcnt_hi_u32_b32 v168,-1,v168\nv_lshlrev_b32 v168,3,v168\nbuffer_load_dwordx2 v[166:167],v168,s[44:47],s56 offen offset:0 sc1\n"
"s_add_u32 s97,s97,0x8000\ns_mov_b32 s96,0x8000\ns_cmp_ge_u32 s97,0x18000\ns_cselect_b32 s96,0xffff0000,s96\ns_cselect_b32 s97,0,s97\nxam 62,0x20000\n"
"xm16 0,128,96\nxdl 0,16,0,0\nv_add_u32 v8,s96,v8\nxrt 64,8,0\nxm16 4,132,96\nxm16 8,136,96\nxrt 66,8,1024\nxm16 12,140,96\nxm16 16,144,96\n"
"v_add_u32 v9,s96,v9\nxrt 68,9,0\nxm16 20,148,96\nxm16 24,152,96\nxrt 70,9,1024\nxam 62,0x20400\nxm16 28,156,96\nxdl 1,16,s74,0\nxm16 32,128,100\n"
"v_add_u32 v10,s96,v10\nxrt 72,10,0\nxm16 36,132,100\nxm16 40,136,100\nxrt 74,10,1024\nxm16 44,140,100\nxm16 48,144,100\nv_add_u32 v11,s96,v11\n"
"xrt 76,11,0\nxm16 52,148,100\nxam 62,0x20800\nxm16 56,152,100\nxdl 0,16,s75,0\nxrt 78,11,1024\nxm16 60,156,100\nxm16 64,128,104\n"
"v_add_u32 v12,s96,v12\nxrt 80,12,0\nxm16 68,132,104\nxm16 72,136,104\nxrt 82,12,1024\nxm16 76,140,104\nxm16 80,144,104\nv_add_u32 v13,s96,v13\n"
"xrt 84,13,0\nxam 62,0x20c00\nxm16 84,148,104\nxdl 1,16,s76,0\nxm16 88,152,104\nxrt 86,13,1024\nxm16 92,156,104\nxm16 96,128,108\n"
"v_add_u32 v14,s96,v14\nxrt 88,14,0\nxm16 100,132,108\nxm16 104,136,108\nxrt 90,14,1024\nxm16 108,140,108\nxam 62,0x21000\nxm16 112,144,108\n"
"xdl 0,16,s77,0\nv_add_u32 v15,s96,v15\nxrt 92,15,0\nxm16 116,148,108\nxm16 120,152,108\nxrt 94,15,1024\nxm16 124,156,108\nxm16 128,128,112\n"
"xrb 32,6,0\nxm16 132,132,112\nxm16 136,136,112\nxrb 36,6,2048\nxam 62,0x21400\nxm16 140,140,112\nxdl 1,16,s78,0\nxm16 144,144,112\nxrb 40,6,4096\n"
"xm16 148,148,112\nxm16 152,152,112\nxrb 44,6,6144\nxm16 156,156,112\nxm16 160,128,116\nxrb 48,6,8192\nxm16 164,132,116\nxam 62,0x21800\n"
"xm16 168,136,116\nxdl 0,16,s79,0\nxrb 52,6,10240\nxm16 172,140,116\nxm16 176,144,116\nxrb 56,6,12288\nxm16 180,148,116\nxm16 184,152,116\n"
"xrb 60,6,14336\nxm16 188,156,116\nxm16 192,128,120\nxam 62,0x21c00\nxm16 196,132,120\nxdl 1,16,s80,0\ns_add_u32 s16,s16,0x80\ns_addc_u32 s17,s17,0\n"
"s_sub_u32 s18,s18,0x80\nxm16 200,136,120\nxm16 204,140,120\nxm16 208,144,120\nxm16 212,148,120\nxm16 216,152,120\nxm16 220,156,120\nxm16 224,128,124\n"
"xm16 228,132,124\nxm16 232,136,124\nxm16 236,140,124\nxm16 240,144,124\nxm16 244,148,124\nxm16 248,152,124\nxm16 252,156,124\nxwl 7\nxm16 0,64,32\n"
"xrt 128,8,8192\nxm16 4,68,32\nxm16 8,72,32\nxrt 130,8,9216\nxm16 12,76,32\nxm16 16,80,32\nxrt 132,9,8192\nxm16 20,84,32\nxm16 24,88,32\n"
"xrt 134,9,9216\nxm16 28,92,32\nxwl 10\nxm16 32,64,36\nxrt 136,10,8192\nxm16 36,68,36\nxm16 40,72,36\nxrt 138,10,9216\nxm16 44,76,36\nxm16 48,80,36\n"
"xrt 140,11,8192\nxm16 52,84,36\nxm16 56,88,36\nxrt 142,11,9216\nxm16 60,92,36\nxwl 13\nxm16 64,64,40\nxrt 144,12,8192\nxm16 68,68,40\nxm16 72,72,40\n"
"xrt 146,12,9216\nxm16 76,76,40\nxm16 80,80,40\nxrt 148,13,8192\nxm16 84,84,40\nxm16 88,88,40\nxrt 150,13,9216\nxm16 92,92,40\nxwl 15\nxm16 96,64,44\n"
"xrt 152,14,8192\nxm16 100,68,44\nxm16 104,72,44\nxrt 154,14,9216\nxm16 108,76,44\nxm16 112,80,44\nxrt 156,15,8192\nxm16 116,84,44\nxm16 120,88,44\n"
"xrt 158,15,9216\nxm16 124,92,44\nxm16 128,64,48\nxrb 96,7,0\nxm16 132,68,48\nxm16 136,72,48\nxrb 100,7,2048\nxm16 140,76,48\nxm16 144,80,48\n"
"xrb 104,7,4096\nxm16 148,84,48\nxm16 152,88,48\nxrb 108,7,6144\nxm16 156,92,48\nxwl 15\nxm16 160,64,52\nxrb 112,7,8192\nxm16 164,68,52\n"
"xm16 168,72,52\nxrb 116,7,10240\nxm16 172,76,52\nxm16 176,80,52\nxrb 120,7,12288\nxm16 180,84,52\nxm16 184,88,52\nxrb 124,7,14336\nxm16 188,92,52\n"
"xm16 192,64,56\nxm16 196,68,56\nxm16 200,72,56\nxm16 204,76,56\nxm16 208,80,56\nxm16 212,84,56\nxm16 216,88,56\nxm16 220,92,56\nxm16 224,64,60\n"
"xm16 228,68,60\nxm16 232,72,60\nxm16 236,76,60\nxm16 240,80,60\nxm16 244,84,60\nxm16 248,88,60\nxm16 252,92,60\ns_waitcnt vmcnt(0) lgkmcnt(0)\n"
"s_barrier\ns_add_u32 s97,s97,0x8000\ns_mov_b32 s96,0x8000\ns_cmp_ge_u32 s97,0x18000\ns_cselect_b32 s96,0xffff0000,s96\ns_cselect_b32 s97,0,s97\n"
"xm16 0,128,96\nv_add_u32 v8,s96,v8\nxrt 64,8,0\nxm16 4,132,96\nxm16 8,136,96\nxrt 66,8,1024\nxm16 12,140,96\nxm16 16,144,96\nv_add_u32 v9,s96,v9\n"
"xrt 68,9,0\nxm16 20,148,96\nxm16 24,152,96\nxrt 70,9,1024\nxm16 28,156,96\nxm16 32,128,100\nv_add_u32 v10,s96,v10\nxrt 72,10,0\nxm16 36,132,100\n"
"xm16 40,136,100\nxrt 74,10,1024\nxm16 44,140,100\nxm16 48,144,100\nv_add_u32 v11,s96,v11\nxrt 76,11,0\nxm16 52,148,100\nxm16 56,152,100\n"
"xrt 78,11,1024\nxm16 60,156,100\nxm16 64,128,104\nv_add_u32 v12,s96,v12\nxrt 80,12,0\nxm16 68,132,104\nxm16 72,136,104\nxrt 82,12,1024\n"
"xm16 76,140,104\nxm16 80,144,104\nv_add_u32 v13,s96,v13\nxrt 84,13,0\nxm16 84,148,104\nxm16 88,152,104\nxrt 86,13,1024\nxm16 92,156,104\n"
"xm16 96,128,108\nv_add_u32 v14,s96,v14\nxrt 88,14,0\nxm16 100,132,108\nxm16 104,136,108\nxrt 90,14,1024\nxm16 108,140,108\nxm16 112,144,108\n"
"v_add_u32 v15,s96,v15\nxrt 92,15,0\nxm16 116,148,108\nxm16 120,152,108\nxrt 94,15,1024\nxm16 124,156,108\nxm16 128,128,112\nxrb 32,6,32768\n"
"xm16 132,132,112\nxm16 136,136,112\nxrb 36,6,34816\nxm16 140,140,112\nxm16 144,144,112\nxrb 40,6,36864\nxm16 148,148,112\nxm16 152,152,112\n"
"xrb 44,6,38912\nxm16 156,156,112\nxm16 160,128,116\nxrb 48,6,40960\nxm16 164,132,116\nxm16 168,136,116\nxrb 52,6,43008\nxm16 172,140,116\n"
"xm16 176,144,116\nxrb 56,6,45056\nxm16 180,148,116\nxm16 184,152,116\nxrb 60,6,47104\nxm16 188,156,116\nxm16 192,128,120\nxm16 196,132,120\n"
"xm16 200,136,120\nxm16 204,140,120\nxm16 208,144,120\nxm16 212,148,120\nxm16 216,152,120\nxm16 220,156,120\nxm16 224,128,124\nxm16 228,132,124\n"
"xm16 232,136,124\nxm16 236,140,124\nxm16 240,144,124\nxm16 244,148,124\nxm16 248,152,124\nxm16 252,156,124\nxwl 7\nxm16 0,64,32\nxrt 128,8,8192\n"
"xm16 4,68,32\nxm16 8,72,32\nxrt 130,8,9216\nxm16 12,76,32\nxm16 16,80,32\nxrt 132,9,8192\nxm16 20,84,32\nxm16 24,88,32\nxrt 134,9,9216\n"
"xm16 28,92,32\nxwl 10\nxm16 32,64,36\nxrt 136,10,8192\nxm16 36,68,36\nxm16 40,72,36\nxrt 138,10,9216\nxm16 44,76,36\nxm16 48,80,36\nxrt 140,11,8192\n"
"xm16 52,84,36\nxm16 56,88,36\nxrt 142,11,9216\nxm16 60,92,36\nxwl 13\nxm16 64,64,40\nxrt 144,12,8192\nxm16 68,68,40\nxm16 72,72,40\nxrt 146,12,9216\n"
"xm16 76,76,40\nxm16 80,80,40\nxrt 148,13,8192\nxm16 84,84,40\nxm16 88,88,40\nxrt 150,13,9216\nxm16 92,92,40\nxwl 15\nxm16 96,64,44\nxrt 152,14,8192\n"
"xm16 100,68,44\nxm16 104,72,44\nxrt 154,14,9216\nxm16 108,76,44\nxm16 112,80,44\nxrt 156,15,8192\nxm16 116,84,44\nxm16 120,88,44\nxrt 158,15,9216\n"
"xm16 124,92,44\nxm16 128,64,48\nxrb 96,7,32768\nxm16 132,68,48\nxm16 136,72,48\nxrb 100,7,34816\nxm16 140,76,48\nxm16 144,80,48\nxrb 104,7,36864\n"
"xm16 148,84,48\nxm16 152,88,48\nxrb 108,7,38912\nxm16 156,92,48\nxwl 15\nxm16 160,64,52\nxrb 112,7,40960\nxm16 164,68,52\nxm16 168,72,52\n"
"xrb 116,7,43008\nxm16 172,76,52\nxm16 176,80,52\nxrb 120,7,45056\nxm16 180,84,52\nxm16 184,88,52\nxrb 124,7,47104\nxm16 188,92,52\nxm16 192,64,56\n"
"xm16 196,68,56\nxm16 200,72,56\nxm16 204,76,56\nxm16 208,80,56\nxm16 212,84,56\nxm16 216,88,56\nxm16 220,92,56\nxm16 224,64,60\nxm16 228,68,60\n"
"xm16 232,72,60\nxm16 236,76,60\nxm16 240,80,60\nxm16 244,84,60\nxm16 248,88,60\nxm16 252,92,60\ns_cmp_eq_u32 s57,0\ns_cbranch_scc1 Lnosp_18_%=\n"
"s_mov_b32 s70,0x30d40\nLspin_19_%=:\ns_waitcnt vmcnt(0)\nv_cmp_eq_u64 vcc,s[6:7],v[166:167]\ns_nop 3\ns_and_b32 s69,vcc_lo,0xffff\n"
"s_cmp_eq_u32 s69,0xffff\ns_cbranch_scc1 Lspun_20_%=\ns_sleep 2\nbuffer_load_dwordx2 v[166:167],v168,s[44:47],s56 offen offset:0 sc1\n"
"s_sub_u32 s70,s70,1\ns_cmp_gt_i32 s70,0\ns_cbranch_scc1 Lspin_19_%=\ns_waitcnt vmcnt(0)\nLspun_20_%=:\ns_nop 1\nLnosp_18_%=:\ns_nop 1\nxwl 0\n"
"s_barrier\nxam 65,0xc000\nxm16 0,128,96\nxdl 160,28,0,0\nxam 65,0xc400\nxm16 4,132,96\nxdl 161,28,s85,0\nxm16 8,136,96\nxam 65,0xc800\n"
"xm16 12,140,96\nxdl 160,28,s86,0\nxm16 16,144,96\nxam 65,0xcc00\nxm16 20,148,96\nxdl 161,28,s87,0\nxm16 24,152,96\nxam 65,0x10000\nxm16 28,156,96\n"
"xdl 160,28,s88,0\nxam 65,0x10400\nxm16 32,128,100\nxdl 161,28,s89,0\nxm16 36,132,100\nxam 65,0x10800\nxm16 40,136,100\nxdl 160,28,s90,0\n"
"xm16 44,140,100\nxam 65,0x10c00\nxm16 48,144,100\nxdl 161,28,s91,0\ns_add_u32 s28,s28,0x100\ns_addc_u32 s29,s29,0\ns_sub_u32 s30,s30,0x100\n"
"xm16 52,148,100\nxam 66,0\nxm16 56,152,100\nxdl1 162,32,0,0\nxm16 60,156,100\nxam 66,0x400\nxm16 64,128,104\nxdl1 163,32,s92,0\nxam 66,0x2000\n"
"xm16 68,132,104\nxdl1 162,32,s93,0\nxm16 72,136,104\nxam 66,0x2400\nxm16 76,140,104\nxdl1 163,32,s94,0\ns_add_u32 s32,s32,0x38000\n"
"s_addc_u32 s33,s33,0\ns_sub_u32 s34,s34,0x38000\nxm16 80,144,104\nxam 65,0x14000\nxm16 84,148,104\nxdl 160,28,0,0\nxm16 88,152,104\nxam 65,0x14400\n"
"xm16 92,156,104\nxdl 161,28,s85,0\nxam 65,0x14800\nxm16 96,128,108\nxdl 160,28,s86,0\nxm16 100,132,108\nxam 65,0x14c00\nxm16 104,136,108\n"
"xdl 161,28,s87,0\nxm16 108,140,108\nxam 65,0x18000\nxm16 112,144,108\nxdl 160,28,s88,0\nxm16 116,148,108\nxam 65,0x18400\nxm16 120,152,108\n"
"xdl 161,28,s89,0\nxm16 124,156,108\nxam 65,0x18800\nxm16 128,128,112\nxdl 160,28,s90,0\nxam 65,0x18c00\nxm16 132,132,112\nxdl 161,28,s91,0\n"
"s_add_u32 s28,s28,0x100\ns_addc_u32 s29,s29,0\ns_sub_u32 s30,s30,0x100\nxm16 136,136,112\nxam 66,0x4000\nxm16 140,140,112\nxdl1 162,32,0,0\n"
"xm16 144,144,112\nxam 66,0x4400\nxm16 148,148,112\nxdl1 163,32,s92,0\nxm16 152,152,112\nxam 66,0x6000\nxm16 156,156,112\nxdl1 162,32,s93,0\n"
"xam 66,0x6400\nxm16 160,128,116\nxdl1 163,32,s94,0\ns_add_u32 s32,s32,0x38000\ns_addc_u32 s33,s33,0\ns_sub_u32 s34,s34,0x38000\nxm16 164,132,116\n"
"xam 65,0x1c000\nxm16 168,136,116\nxdl 160,28,0,0\nxm16 172,140,116\nxam 65,0x1c400\nxm16 176,144,116\nxdl 161,28,s85,0\nxm16 180,148,116\n"
"xam 65,0x1c800\nxm16 184,152,116\nxdl 160,28,s86,0\nxm16 188,156,116\nxam 65,0x1cc00\nxm16 192,128,120\nxdl 161,28,s87,0\nxam 65,0x20000\n"
"xm16 196,132,120\nxdl 160,28,s88,0\nxm16 200,136,120\nxam 65,0x20400\nxm16 204,140,120\nxdl 161,28,s89,0\nxm16 208,144,120\nxam 65,0x20800\n"
"xm16 212,148,120\nxdl 160,28,s90,0\nxm16 216,152,120\nxam 65,0x20c00\nxm16 220,156,120\nxdl 161,28,s91,0\ns_add_u32 s28,s28,0x100\n"
"s_addc_u32 s29,s29,0\ns_sub_u32 s30,s30,0x100\nxam 66,0x8000\nxm16 224,128,124\nxdl1 162,32,0,0\nxm16 228,132,124\nxam 66,0x8400\nxm16 232,136,124\n"
"xdl1 163,32,s92,0\nxm16 236,140,124\nxam 66,0xa000\nxm16 240,144,124\nxdl1 162,32,s93,0\nxm16 244,148,124\nxam 66,0xa400\nxm16 248,152,124\n"
"xdl1 163,32,s94,0\ns_add_u32 s32,s32,0x38000\ns_addc_u32 s33,s33,0\ns_sub_u32 s34,s34,0x38000\nxm16 252,156,124\ns_nop 7\nxar 32,0\nxar 33,1\n"
"xar 34,2\nxar 35,3\nxar 36,4\nxar 37,5\nxar 38,6\nxar 39,7\nxcv 128,32,33\nxcv 129,34,35\nxcv 130,36,37\nxcv 131,38,39\nxar 40,32\nxar 41,33\n"
"xar 42,34\nxar 43,35\nxar 44,36\nxar 45,37\nxar 46,38\nxar 47,39\nxcv 132,40,41\nxcv 133,42,43\nxcv 134,44,45\nxcv 135,46,47\nxar 48,8\nxar 49,9\n"
"xar 50,10\nxar 51,11\nxar 52,12\nxar 53,13\nxar 54,14\nxar 55,15\nxcv 136,48,49\nxcv 137,50,51\nxcv 138,52,53\nxcv 139,54,55\nxar 56,40\nxar 57,41\n"
"xar 58,42\nxar 59,43\nxar 60,44\nxar 61,45\nxar 62,46\nxar 63,47\nxcv 140,56,57\nxcv 141,58,59\nxcv 142,60,61\nxcv 143,62,63\nxar 32,16\nxar 33,17\n"
"xar 34,18\nxar 35,19\nxar 36,20\nxar 37,21\nxar 38,22\nxar 39,23\nxcv 144,32,33\nxcv 145,34,35\nxcv 146,36,37\nxcv 147,38,39\nxar 40,48\nxar 41,49\n"
"xar 42,50\nxar 43,51\nxar 44,52\nxar 45,53\nxar 46,54\nxar 47,55\nxcv 148,40,41\nxcv 149,42,43\nxcv 150,44,45\nxcv 151,46,47\nxar 48,24\nxar 49,25\n"
"xar 50,26\nxar 51,27\nxar 52,28\nxar 53,29\nxar 54,30\nxar 55,31\nxcv 152,48,49\nxcv 153,50,51\nxcv 154,52,53\nxcv 155,54,55\nxar 56,56\nxar 57,57\n"
"xar 58,58\nxar 59,59\nxar 60,60\nxar 61,61\nxar 62,62\nxar 63,63\nxcv 156,56,57\nxcv 157,58,59\nxcv 158,60,61\nxcv 159,62,63\nxar 32,64\nxar 33,65\n"
"xar 34,66\nxar 35,67\nxar 36,68\nxar 37,69\nxar 38,70\nxar 39,71\nxcv 160,32,33\nxcv 161,34,35\nxcv 162,36,37\nxcv 163,38,39\nxar 40,96\nxar 41,97\n"
"xar 42,98\nxar 43,99\nxar 44,100\nxar 45,101\nxar 46,102\nxar 47,103\nxcv 164,40,41\nxcv 165,42,43\nxcv 166,44,45\nxcv 167,46,47\nxar 48,72\n"
"xar 49,73\nxar 50,74\nxar 51,75\nxar 52,76\nxar 53,77\nxar 54,78\nxar 55,79\nxcv 168,48,49\nxcv 169,50,51\nxcv 170,52,53\nxcv 171,54,55\nxar 56,104\n"
"xar 57,105\nxar 58,106\nxar 59,107\nxar 60,108\nxar 61,109\nxar 62,110\nxar 63,111\nxcv 172,56,57\nxcv 173,58,59\nxcv 174,60,61\nxcv 175,62,63\n"
"xar 32,80\nxar 33,81\nxar 34,82\nxar 35,83\nxar 36,84\nxar 37,85\nxar 38,86\nxar 39,87\nxcv 176,32,33\nxcv 177,34,35\nxcv 178,36,37\nxcv 179,38,39\n"
"xar 40,112\nxar 41,113\nxar 42,114\nxar 43,115\nxar 44,116\nxar 45,117\nxar 46,118\nxar 47,119\nxcv 180,40,41\nxcv 181,42,43\nxcv 182,44,45\n"
"xcv 183,46,47\nxar 48,88\nxar 49,89\nxar 50,90\nxar 51,91\nxar 52,92\nxar 53,93\nxar 54,94\nxar 55,95\nxcv 184,48,49\nxcv 185,50,51\nxcv 186,52,53\n"
"xcv 187,54,55\nxar 56,120\nxar 57,121\nxar 58,122\nxar 59,123\nxar 60,124\nxar 61,125\nxar 62,126\nxar 63,127\nxcv 188,56,57\nxcv 189,58,59\n"
"xcv 190,60,61\nxcv 191,62,63\nxar 32,128\nxar 33,129\nxar 34,130\nxar 35,131\nxar 36,132\nxar 37,133\nxar 38,134\nxar 39,135\nxcv 192,32,33\n"
"xcv 193,34,35\nxcv 194,36,37\nxcv 195,38,39\nxar 40,160\nxar 41,161\nxar 42,162\nxar 43,163\nxar 44,164\nxar 45,165\nxar 46,166\nxar 47,167\n"
"xcv 196,40,41\nxcv 197,42,43\nxcv 198,44,45\nxcv 199,46,47\nxar 48,136\nxar 49,137\nxar 50,138\nxar 51,139\nxar 52,140\nxar 53,141\nxar 54,142\n"
"xar 55,143\nxcv 200,48,49\nxcv 201,50,51\nxcv 202,52,53\nxcv 203,54,55\nxar 56,168\nxar 57,169\nxar 58,170\nxar 59,171\nxar 60,172\nxar 61,173\n"
"xar 62,174\nxar 63,175\nxcv 204,56,57\nxcv 205,58,59\nxcv 206,60,61\nxcv 207,62,63\nxar 32,144\nxar 33,145\nxar 34,146\nxar 35,147\nxar 36,148\n"
"xar 37,149\nxar 38,150\nxar 39,151\nxcv 208,32,33\nxcv 209,34,35\nxcv 210,36,37\nxcv 211,38,39\nxar 40,176\nxar 41,177\nxar 42,178\nxar 43,179\n"
"xar 44,180\nxar 45,181\nxar 46,182\nxar 47,183\nxcv 212,40,41\nxcv 213,42,43\nxcv 214,44,45\nxcv 215,46,47\nxar 48,152\nxar 49,153\nxar 50,154\n"
"xar 51,155\nxar 52,156\nxar 53,157\nxar 54,158\nxar 55,159\nxcv 216,48,49\nxcv 217,50,51\nxcv 218,52,53\nxcv 219,54,55\nxar 56,184\nxar 57,185\n"
"xar 58,186\nxar 59,187\nxar 60,188\nxar 61,189\nxar 62,190\nxar 63,191\nxcv 220,56,57\nxcv 221,58,59\nxcv 222,60,61\nxcv 223,62,63\nxar 32,192\n"
"xar 33,193\nxar 34,194\nxar 35,195\nxar 36,196\nxar 37,197\nxar 38,198\nxar 39,199\nxcv 224,32,33\nxcv 225,34,35\nxcv 226,36,37\nxcv 227,38,39\n"
"xar 40,224\nxar 41,225\nxar 42,226\nxar 43,227\nxar 44,228\nxar 45,229\nxar 46,230\nxar 47,231\nxcv 228,40,41\nxcv 229,42,43\nxcv 230,44,45\n"
"xcv 231,46,47\nxar 48,200\nxar 49,201\nxar 50,202\nxar 51,203\nxar 52,204\nxar 53,205\nxar 54,206\nxar 55,207\nxcv 232,48,49\nxcv 233,50,51\n"
"xcv 234,52,53\nxcv 235,54,55\nxar 56,232\nxar 57,233\nxar 58,234\nxar 59,235\nxar 60,236\nxar 61,237\nxar 62,238\nxar 63,239\nxcv 236,56,57\n"
"xcv 237,58,59\nxcv 238,60,61\nxcv 239,62,63\nxar 32,208\nxar 33,209\nxar 34,210\nxar 35,211\nxar 36,212\nxar 37,213\nxar 38,214\nxar 39,215\n"
"xcv 240,32,33\nxcv 241,34,35\nxcv 242,36,37\nxcv 243,38,39\nxar 40,240\nxar 41,241\nxar 42,242\nxar 43,243\nxar 44,244\nxar 45,245\nxar 46,246\n"
"xar 47,247\nxcv 244,40,41\nxcv 245,42,43\nxcv 246,44,45\nxcv 247,46,47\nxar 48,216\nxar 49,217\nxar 50,218\nxar 51,219\nxar 52,220\nxar 53,221\n"
"xar 54,222\nxar 55,223\nxcv 248,48,49\nxcv 249,50,51\nxcv 250,52,53\nxcv 251,54,55\nxar 56,248\nxar 57,249\nxar 58,250\nxar 59,251\nxar 60,252\n"
"xar 61,253\nxar 62,254\nxar 63,255\nxcv 252,56,57\nxcv 253,58,59\nxcv 254,60,61\nxcv 255,62,63\ns_branch Lblkend_21_%=\nLlnhook_7_%=:\ns_nop 4\n"
"s_cmp_eq_u32 s54,0\ns_cbranch_scc1 Lnoln_11_%=\ns_mov_b32 s70,0x30d40\nv_mbcnt_lo_u32_b32 v168,-1,0\nv_mbcnt_hi_u32_b32 v168,-1,v168\n"
"v_lshlrev_b32 v168,3,v168\nbuffer_load_dwordx2 v[166:167],v168,s[44:47],s52 offen offset:56 sc1\nLspin_12_%=:\ns_waitcnt vmcnt(0)\n"
"v_cmp_eq_u64 vcc,s[6:7],v[166:167]\ns_nop 3\ns_and_b32 s69,vcc_lo,0x7f\ns_cmp_eq_u32 s69,0x7f\ns_cbranch_scc1 Lspun_13_%=\ns_sleep 2\n"
"buffer_load_dwordx2 v[166:167],v168,s[44:47],s52 offen offset:56 sc1\ns_sub_u32 s70,s70,1\ns_cmp_gt_i32 s70,0\ns_cbranch_scc1 Lspin_12_%=\n"
"s_waitcnt vmcnt(0)\nLspun_13_%=:\ns_nop 1\ns_mov_b32 s68,0\nbuffer_load_dwordx2 v[224:225],v236,s[48:51],s68 offen offset:0 sc1\n"
"buffer_load_dwordx2 v[226:227],v236,s[48:51],s68 offen offset:32 sc1\nbuffer_load_dwordx2 v[228:229],v236,s[48:51],s68 offen offset:64 sc1\n"
"buffer_load_dwordx2 v[230:231],v236,s[48:51],s68 offen offset:96 sc1\ns_waitcnt vmcnt(0)\nv_cmp_eq_f32 vcc,0,v238\ns_nop 1\n"
"v_cndmask_b32 v230,v230,v170,vcc\nv_cndmask_b32 v231,v231,v170,vcc\nxad 172,v224,226\nxad 172,v172,228\nxad 172,v172,230\nv_mov_b32 v174,v172\n"
"s_nop 1\nv_permlane16_swap_b32 v172,v174\nxad 172,v172,174\nv_mov_b32 v174,v172\ns_nop 1\nv_permlane32_swap_b32 v172,v174\nxad 172,v172,174\n"
"xmu 172,0x3d924925,172\nv_sub_f32 v224,v224,v172\nv_sub_f32 v226,v226,v172\nv_sub_f32 v228,v228,v172\nv_sub_f32 v230,v230,v172\nxmu 230,v230,238\n"
"xmu 173,v224,224\nxfm 173,v226,v226,v173\nxfm 173,v228,v228,v173\nxfm 173,v230,v230,v173\nxmu 173,0x42800000,173\nxad 173,v173,225\nxad 173,v173,227\n"
"xad 173,v173,229\nxad 173,v173,231\nv_mov_b32 v174,v173\ns_nop 1\nv_permlane16_swap_b32 v173,v174\nxad 173,v173,174\nv_mov_b32 v174,v173\ns_nop 1\n"
"v_permlane32_swap_b32 v173,v174\nxad 173,v173,174\nv_mov_b32 v175,0x3727c5ac\nxfm 173,v173,v239,v175\nv_rsq_f32 v173,v173\ns_nop 0\nxmu 174,v172,173\n"
"v_lshlrev_b32 v176,16,v192\nv_and_b32 v177,0xffff0000,v192\nv_lshlrev_b32 v178,16,v193\nv_and_b32 v179,0xffff0000,v193\nv_lshlrev_b32 v180,16,v194\n"
"v_and_b32 v181,0xffff0000,v194\nv_lshlrev_b32 v182,16,v195\nv_and_b32 v183,0xffff0000,v195\nxfm 176,v176,v173,-v174\nxfm 177,v177,v173,-v174\n"
"xfm 178,v178,v173,-v174\nxfm 179,v179,v173,-v174\nxfm 180,v180,v173,-v174\nxfm 181,v181,v173,-v174\nxfm 182,v182,v173,-v174\nxfm 183,v183,v173,-v174\n"
"xcv 184,176,177\nxcv 185,178,179\nxcv 186,180,181\nxcv 187,182,183\ns_mov_b32 s68,0\n"
"buffer_store_dwordx2 v[184:185],v237,s[40:43],s68 offen offset:0 sc1\nbuffer_store_dwordx2 v[186:187],v237,s[40:43],s68 offen offset:32 sc1\ns_nop 1\n"
"v_lshlrev_b32 v176,16,v196\nv_and_b32 v177,0xffff0000,v196\nv_lshlrev_b32 v178,16,v197\nv_and_b32 v179,0xffff0000,v197\nv_lshlrev_b32 v180,16,v198\n"
"v_and_b32 v181,0xffff0000,v198\nv_lshlrev_b32 v182,16,v199\nv_and_b32 v183,0xffff0000,v199\nxfm 176,v176,v173,-v174\nxfm 177,v177,v173,-v174\n"
"xfm 178,v178,v173,-v174\nxfm 179,v179,v173,-v174\nxfm 180,v180,v173,-v174\nxfm 181,v181,v173,-v174\nxfm 182,v182,v173,-v174\nxfm 183,v183,v173,-v174\n"
"xcv 184,176,177\nxcv 185,178,179\nxcv 186,180,181\nxcv 187,182,183\ns_mov_b32 s68,0\n"
"buffer_store_dwordx2 v[184:185],v237,s[40:43],s68 offen offset:64 sc1\nbuffer_store_dwordx2 v[186:187],v237,s[40:43],s68 offen offset:96 sc1\n"
"s_nop 1\ns_mov_b32 s68,0x800\nbuffer_load_dwordx2 v[224:225],v236,s[48:51],s68 offen offset:0 sc1\n"
"buffer_load_dwordx2 v[226:227],v236,s[48:51],s68 offen offset:32 sc1\nbuffer_load_dwordx2 v[228:229],v236,s[48:51],s68 offen offset:64 sc1\n"
"buffer_load_dwordx2 v[230:231],v236,s[48:51],s68 offen offset:96 sc1\ns_waitcnt vmcnt(0)\nv_cmp_eq_f32 vcc,0,v238\ns_nop 1\n"
"v_cndmask_b32 v230,v230,v170,vcc\nv_cndmask_b32 v231,v231,v170,vcc\nxad 172,v224,226\nxad 172,v172,228\nxad 172,v172,230\nv_mov_b32 v174,v172\n"
"s_nop 1\nv_permlane16_swap_b32 v172,v174\nxad 172,v172,174\nv_mov_b32 v174,v172\ns_nop 1\nv_permlane32_swap_b32 v172,v174\nxad 172,v172,174\n"
"xmu 172,0x3d924925,172\nv_sub_f32 v224,v224,v172\nv_sub_f32 v226,v226,v172\nv_sub_f32 v228,v228,v172\nv_sub_f32 v230,v230,v172\nxmu 230,v230,238\n"
"xmu 173,v224,224\nxfm 173,v226,v226,v173\nxfm 173,v228,v228,v173\nxfm 173,v230,v230,v173\nxmu 173,0x42800000,173\nxad 173,v173,225\nxad 173,v173,227\n"
"xad 173,v173,229\nxad 173,v173,231\nv_mov_b32 v174,v173\ns_nop 1\nv_permlane16_swap_b32 v173,v174\nxad 173,v173,174\nv_mov_b32 v174,v173\ns_nop 1\n"
"v_permlane32_swap_b32 v173,v174\nxad 173,v173,174\nv_mov_b32 v175,0x3727c5ac\nxfm 173,v173,v239,v175\nv_rsq_f32 v173,v173\ns_nop 0\nxmu 174,v172,173\n"
"v_lshlrev_b32 v176,16,v200\nv_and_b32 v177,0xffff0000,v200\nv_lshlrev_b32 v178,16,v201\nv_and_b32 v179,0xffff0000,v201\nv_lshlrev_b32 v180,16,v202\n"
"v_and_b32 v181,0xffff0000,v202\nv_lshlrev_b32 v182,16,v203\nv_and_b32 v183,0xffff0000,v203\nxfm 176,v176,v173,-v174\nxfm 177,v177,v173,-v174\n"
"xfm 178,v178,v173,-v174\nxfm 179,v179,v173,-v174\nxfm 180,v180,v173,-v174\nxfm 181,v181,v173,-v174\nxfm 182,v182,v173,-v174\nxfm 183,v183,v173,-v174\n"
"xcv 184,176,177\nxcv 185,178,179\nxcv 186,180,181\nxcv 187,182,183\ns_mov_b32 s68,0x7000\n"
"buffer_store_dwordx2 v[184:185],v237,s[40:43],s68 offen offset:0 sc1\nbuffer_store_dwordx2 v[186:187],v237,s[40:43],s68 offen offset:32 sc1\ns_nop 1\n"
"v_lshlrev_b32 v176,16,v204\nv_and_b32 v177,0xffff0000,v204\nv_lshlrev_b32 v178,16,v205\nv_and_b32 v179,0xffff0000,v205\nv_lshlrev_b32 v180,16,v206\n"
"v_and_b32 v181,0xffff0000,v206\nv_lshlrev_b32 v182,16,v207\nv_and_b32 v183,0xffff0000,v207\nxfm 176,v176,v173,-v174\nxfm 177,v177,v173,-v174\n"
"xfm 178,v178,v173,-v174\nxfm 179,v179,v173,-v174\nxfm 180,v180,v173,-v174\nxfm 181,v181,v173,-v174\nxfm 182,v182,v173,-v174\nxfm 183,v183,v173,-v174\n"
"xcv 184,176,177\nxcv 185,178,179\nxcv 186,180,181\nxcv 187,182,183\ns_mov_b32 s68,0x7000\n"
"buffer_store_dwordx2 v[184:185],v237,s[40:43],s68 offen offset:64 sc1\nbuffer_store_dwordx2 v[186:187],v237,s[40:43],s68 offen offset:96 sc1\n"
"s_nop 1\ns_mov_b32 s68,0x1000\nbuffer_load_dwordx2 v[224:225],v236,s[48:51],s68 offen offset:0 sc1\n"
"buffer_load_dwordx2 v[226:227],v236,s[48:51],s68 offen offset:32 sc1\nbuffer_load_dwordx2 v[228:229],v236,s[48:51],s68 offen offset:64 sc1\n"
"buffer_load_dwordx2 v[230:231],v236,s[48:51],s68 offen offset:96 sc1\ns_waitcnt vmcnt(0)\nv_cmp_eq_f32 vcc,0,v238\ns_nop 1\n"
"v_cndmask_b32 v230,v230,v170,vcc\nv_cndmask_b32 v231,v231,v170,vcc\nxad 172,v224,226\nxad 172,v172,228\nxad 172,v172,230\nv_mov_b32 v174,v172\n"
"s_nop 1\nv_permlane16_swap_b32 v172,v174\nxad 172,v172,174\nv_mov_b32 v174,v172\ns_nop 1\nv_permlane32_swap_b32 v172,v174\nxad 172,v172,174\n"
"xmu 172,0x3d924925,172\nv_sub_f32 v224,v224,v172\nv_sub_f32 v226,v226,v172\nv_sub_f32 v228,v228,v172\nv_sub_f32 v230,v230,v172\nxmu 230,v230,238\n"
"xmu 173,v224,224\nxfm 173,v226,v226,v173\nxfm 173,v228,v228,v173\nxfm 173,v230,v230,v173\nxmu 173,0x42800000,173\nxad 173,v173,225\nxad 173,v173,227\n"
"xad 173,v173,229\nxad 173,v173,231\nv_mov_b32 v174,v173\ns_nop 1\nv_permlane16_swap_b32 v173,v174\nxad 173,v173,174\nv_mov_b32 v174,v173\ns_nop 1\n"
"v_permlane32_swap_b32 v173,v174\nxad 173,v173,174\nv_mov_b32 v175,0x3727c5ac\nxfm 173,v173,v239,v175\nv_rsq_f32 v173,v173\ns_nop 0\nxmu 174,v172,173\n"
"v_lshlrev_b32 v176,16,v208\nv_and_b32 v177,0xffff0000,v208\nv_lshlrev_b32 v178,16,v209\nv_and_b32 v179,0xffff0000,v209\nv_lshlrev_b32 v180,16,v210\n"
"v_and_b32 v181,0xffff0000,v210\nv_lshlrev_b32 v182,16,v211\nv_and_b32 v183,0xffff0000,v211\nxfm 176,v176,v173,-v174\nxfm 177,v177,v173,-v174\n"
"xfm 178,v178,v173,-v174\nxfm 179,v179,v173,-v174\nxfm 180,v180,v173,-v174\nxfm 181,v181,v173,-v174\nxfm 182,v182,v173,-v174\nxfm 183,v183,v173,-v174\n"
"xcv 184,176,177\nxcv 185,178,179\nxcv 186,180,181\nxcv 187,182,183\ns_mov_b32 s68,0xe000\n"
"buffer_store_dwordx2 v[184:185],v237,s[40:43],s68 offen offset:0 sc1\nbuffer_store_dwordx2 v[186:187],v237,s[40:43],s68 offen offset:32 sc1\ns_nop 1\n"
"v_lshlrev_b32 v176,16,v212\nv_and_b32 v177,0xffff0000,v212\nv_lshlrev_b32 v178,16,v213\nv_and_b32 v179,0xffff0000,v213\nv_lshlrev_b32 v180,16,v214\n"
"v_and_b32 v181,0xffff0000,v214\nv_lshlrev_b32 v182,16,v215\nv_and_b32 v183,0xffff0000,v215\nxfm 176,v176,v173,-v174\nxfm 177,v177,v173,-v174\n"
"xfm 178,v178,v173,-v174\nxfm 179,v179,v173,-v174\nxfm 180,v180,v173,-v174\nxfm 181,v181,v173,-v174\nxfm 182,v182,v173,-v174\nxfm 183,v183,v173,-v174\n"
"xcv 184,176,177\nxcv 185,178,179\nxcv 186,180,181\nxcv 187,182,183\ns_mov_b32 s68,0xe000\n"
"buffer_store_dwordx2 v[184:185],v237,s[40:43],s68 offen offset:64 sc1\nbuffer_store_dwordx2 v[186:187],v237,s[40:43],s68 offen offset:96 sc1\n"
"s_nop 1\ns_mov_b32 s68,0x1800\nbuffer_load_dwordx2 v[224:225],v236,s[48:51],s68 offen offset:0 sc1\n"
"buffer_load_dwordx2 v[226:227],v236,s[48:51],s68 offen offset:32 sc1\nbuffer_load_dwordx2 v[228:229],v236,s[48:51],s68 offen offset:64 sc1\n"
"buffer_load_dwordx2 v[230:231],v236,s[48:51],s68 offen offset:96 sc1\ns_waitcnt vmcnt(0)\nv_cmp_eq_f32 vcc,0,v238\ns_nop 1\n"
"v_cndmask_b32 v230,v230,v170,vcc\nv_cndmask_b32 v231,v231,v170,vcc\nxad 172,v224,226\nxad 172,v172,228\nxad 172,v172,230\nv_mov_b32 v174,v172\n"
"s_nop 1\nv_permlane16_swap_b32 v172,v174\nxad 172,v172,174\nv_mov_b32 v174,v172\ns_nop 1\nv_permlane32_swap_b32 v172,v174\nxad 172,v172,174\n"
"xmu 172,0x3d924925,172\nv_sub_f32 v224,v224,v172\nv_sub_f32 v226,v226,v172\nv_sub_f32 v228,v228,v172\nv_sub_f32 v230,v230,v172\nxmu 230,v230,238\n"
"xmu 173,v224,224\nxfm 173,v226,v226,v173\nxfm 173,v228,v228,v173\nxfm 173,v230,v230,v173\nxmu 173,0x42800000,173\nxad 173,v173,225\nxad 173,v173,227\n"
"xad 173,v173,229\nxad 173,v173,231\nv_mov_b32 v174,v173\ns_nop 1\nv_permlane16_swap_b32 v173,v174\nxad 173,v173,174\nv_mov_b32 v174,v173\ns_nop 1\n"
"v_permlane32_swap_b32 v173,v174\nxad 173,v173,174\nv_mov_b32 v175,0x3727c5ac\nxfm 173,v173,v239,v175\nv_rsq_f32 v173,v173\ns_nop 0\nxmu 174,v172,173\n"
"v_lshlrev_b32 v176,16,v216\nv_and_b32 v177,0xffff0000,v216\nv_lshlrev_b32 v178,16,v217\nv_and_b32 v179,0xffff0000,v217\nv_lshlrev_b32 v180,16,v218\n"
"v_and_b32 v181,0xffff0000,v218\nv_lshlrev_b32 v182,16,v219\nv_and_b32 v183,0xffff0000,v219\nxfm 176,v176,v173,-v174\nxfm 177,v177,v173,-v174\n"
"xfm 178,v178,v173,-v174\nxfm 179,v179,v173,-v174\nxfm 180,v180,v173,-v174\nxfm 181,v181,v173,-v174\nxfm 182,v182,v173,-v174\nxfm 183,v183,v173,-v174\n"
"xcv 184,176,177\nxcv 185,178,179\nxcv 186,180,181\nxcv 187,182,183\ns_mov_b32 s68,0x15000\n"
"buffer_store_dwordx2 v[184:185],v237,s[40:43],s68 offen offset:0 sc1\nbuffer_store_dwordx2 v[186:187],v237,s[40:43],s68 offen offset:32 sc1\ns_nop 1\n"
"v_lshlrev_b32 v176,16,v220\nv_and_b32 v177,0xffff0000,v220\nv_lshlrev_b32 v178,16,v221\nv_and_b32 v179,0xffff0000,v221\nv_lshlrev_b32 v180,16,v222\n"
"v_and_b32 v181,0xffff0000,v222\nv_lshlrev_b32 v182,16,v223\nv_and_b32 v183,0xffff0000,v223\nxfm 176,v176,v173,-v174\nxfm 177,v177,v173,-v174\n"
"xfm 178,v178,v173,-v174\nxfm 179,v179,v173,-v174\nxfm 180,v180,v173,-v174\nxfm 181,v181,v173,-v174\nxfm 182,v182,v173,-v174\nxfm 183,v183,v173,-v174\n"
"xcv 184,176,177\nxcv 185,178,179\nxcv 186,180,181\nxcv 187,182,183\ns_mov_b32 s68,0x15000\n"
"buffer_store_dwordx2 v[184:185],v237,s[40:43],s68 offen offset:64 sc1\nbuffer_store_dwordx2 v[186:187],v237,s[40:43],s68 offen offset:96 sc1\n"
"s_nop 1\ns_waitcnt vmcnt(0)\nLnoln_11_%=:\ns_nop 3\ns_branch Llnret_8_%=\nLznhook_9_%=:\ns_nop 1\ns_cmp_eq_u32 s54,0\ns_cbranch_scc1 Lnozn_14_%=\n"
"s_cmp_eq_u32 s3,0\ns_cbranch_scc0 Lnosig_15_%=\nv_mov_b32 v166,s6\nv_mov_b32 v167,s7\nv_mov_b32 v169,0\ns_mov_b64 exec,1\n"
"buffer_store_dwordx2 v[166:167],v169,s[44:47],s55 offen offset:0 sc1\ns_mov_b64 exec,-1\nLnosig_15_%=:\ns_nop 1\nLnozn_14_%=:\ns_nop 1\n"
"s_branch Lznret_10_%=\nLblkend_21_%=:\ns_branch Lsp_6_%=\nLkvv_5_%=:\ns_nop 4\nv_mbcnt_lo_u32_b32 v252,-1,0\nv_mbcnt_hi_u32_b32 v252,-1,v252\n"
"s_mov_b32 s16,s28\ns_mov_b32 s20,s32\ns_mov_b32 s17,s29\ns_mov_b32 s21,s33\ns_mov_b32 s18,s30\ns_mov_b32 s22,s34\ns_mov_b32 s19,s31\n"
"s_mov_b32 s23,s35\ns_load_dwordx2 s[76:77],s[0:1],56\nxwl 0\ns_lshr_b32 s99,s100,17\ns_and_b32 s99,s99,15\ns_lshl_b32 s99,s99,19\n"
"s_add_u32 s28,s76,s99\ns_addc_u32 s29,s77,0\ns_and_b32 s29,s29,0xffff\ns_lshr_b32 s99,s100,17\ns_and_b32 s99,s99,15\ns_lshl_b32 s99,s99,19\n"
"s_sub_u32 s99,0x800000,s99\ns_lshr_b32 s98,s100,29\ns_and_b32 s98,s98,1\ns_cmp_lg_u32 s98,0\ns_cselect_b32 s30,s99,0\ns_mov_b32 s31,0x20000\n"
"s_lshr_b32 s98,s100,21\ns_and_b32 s98,s98,15\ns_lshl_b32 s98,s98,7\ns_add_u32 s98,0x5000000,s98\ns_add_u32 s32,s4,s98\ns_addc_u32 s33,s5,0\n"
"s_and_b32 s33,s33,0xffff\ns_lshr_b32 s98,s100,21\ns_and_b32 s98,s98,15\ns_lshl_b32 s98,s98,7\ns_sub_u32 s98,0x380000,s98\ns_lshr_b32 s99,s100,29\n"
"s_and_b32 s99,s99,1\ns_cmp_lg_u32 s99,0\ns_cselect_b32 s34,s98,0\ns_mov_b32 s35,0x20000\ns_lshl_b32 s99,s2,15\ns_add_u32 s36,s76,s99\n"
"s_addc_u32 s37,s77,0\ns_and_b32 s37,s37,0xffff\ns_mov_b32 s38,0x8000\ns_mov_b32 s39,0x20000\nv_lshrrev_b32 v0,3,v252\ns_lshl_b32 s99,s3,6\n"
"v_add_u32 v0,s99,v0\nv_lshlrev_b32 v0,11,v0\nv_and_b32 v191,7,v252\nv_lshrrev_b32 v190,3,v252\nv_lshrrev_b32 v190,1,v190\nv_xor_b32 v191,v191,v190\n"
"v_lshlrev_b32 v191,4,v191\nv_add_u32 v0,v0,v191\nv_xor_b32 v1,64,v0\nv_lshrrev_b32 v2,4,v252\ns_and_b32 s99,s3,1\ns_lshl_b32 s99,s99,5\n"
"v_add_u32 v2,s99,v2\nv_lshlrev_b32 v2,13,v2\ns_lshr_b32 s99,s3,1\ns_lshl_b32 s99,s99,8\nv_add_u32 v2,s99,v2\nv_and_b32 v191,15,v252\n"
"v_lshrrev_b32 v191,1,v191\nv_lshrrev_b32 v190,4,v252\nv_xor_b32 v191,v191,v190\nv_lshlrev_b32 v191,1,v191\nv_and_b32 v190,15,v252\n"
"v_and_b32 v190,1,v190\nv_or_b32 v191,v191,v190\nv_lshlrev_b32 v191,4,v191\nv_add_u32 v2,v2,v191\nv_xor_b32 v3,0x80,v2\nv_and_b32 v6,15,v252\n"
"s_lshr_b32 s99,s3,1\ns_lshl_b32 s99,s99,7\nv_add_u32 v6,s99,v6\nv_lshlrev_b32 v6,7,v6\nv_lshrrev_b32 v191,4,v252\nv_and_b32 v190,15,v252\n"
"v_lshrrev_b32 v190,1,v190\nv_xor_b32 v191,v191,v190\nv_lshlrev_b32 v191,4,v191\nv_add_u32 v6,v6,v191\nv_add_u32 v6,0x18000,v6\nv_xor_b32 v7,64,v6\n"
"v_lshrrev_b32 v250,4,v252\nv_lshlrev_b32 v250,3,v250\nv_and_b32 v191,15,v252\nv_lshrrev_b32 v191,2,v191\nv_add_u32 v250,v250,v191\n"
"v_lshlrev_b32 v250,8,v250\ns_and_b32 s99,s3,1\ns_lshl_b32 s99,s99,14\nv_add_u32 v250,s99,v250\nv_and_b32 v191,3,v252\nv_lshlrev_b32 v191,3,v191\n"
"v_add_u32 v250,v250,v191\nv_and_b32 v251,15,v252\nv_lshrrev_b32 v251,2,v251\nv_lshrrev_b32 v191,4,v252\nv_and_b32 v191,1,v191\n"
"v_lshlrev_b32 v191,2,v191\nv_or_b32 v251,v251,v191\nv_lshlrev_b32 v251,5,v251\nv_xor_b32 v8,0,v251\nv_add_u32 v8,v8,v250\nv_xor_b32 v9,32,v251\n"
"v_add_u32 v9,v9,v250\nv_xor_b32 v10,64,v251\nv_add_u32 v10,v10,v250\nv_xor_b32 v11,0x60,v251\nv_add_u32 v11,v11,v250\nv_xor_b32 v12,0x80,v251\n"
"v_add_u32 v12,v12,v250\nv_xor_b32 v13,0xa0,v251\nv_add_u32 v13,v13,v250\nv_xor_b32 v14,0xc0,v251\nv_add_u32 v14,v14,v250\nv_xor_b32 v15,0xe0,v251\n"
"v_add_u32 v15,v15,v250\nv_lshrrev_b32 v160,3,v252\ns_lshl_b32 s99,s3,5\nv_add_u32 v160,s99,v160\nv_lshlrev_b32 v160,12,v160\nv_and_b32 v191,7,v252\n"
"v_lshrrev_b32 v190,3,v252\nv_lshrrev_b32 v190,1,v190\nv_xor_b32 v191,v191,v190\nv_lshlrev_b32 v191,4,v191\nv_add_u32 v160,v160,v191\n"
"v_xor_b32 v161,64,v160\nv_lshrrev_b32 v162,3,v252\ns_lshl_b32 s99,s3,4\nv_add_u32 v162,s99,v162\nv_mul_u32_u24 v162,0x700,v162\n"
"v_and_b32 v191,7,v252\nv_lshrrev_b32 v191,1,v191\nv_lshrrev_b32 v190,3,v252\nv_lshrrev_b32 v190,1,v190\nv_and_b32 v190,1,v190\n"
"v_xor_b32 v191,v191,v190\nv_lshlrev_b32 v191,1,v191\nv_and_b32 v190,7,v252\nv_and_b32 v190,1,v190\nv_or_b32 v191,v191,v190\n"
"v_lshlrev_b32 v191,4,v191\nv_add_u32 v162,v162,v191\nv_xor_b32 v163,64,v162\nv_lshlrev_b32 v164,6,v252\ns_lshl_b32 s99,s3,12\n"
"v_add_u32 v164,s99,v164\nv_and_b32 v236,15,v252\ns_lshr_b32 s99,s3,1\ns_lshl_b32 s99,s99,6\nv_add_u32 v236,s99,v236\nv_lshlrev_b32 v236,7,v236\n"
"v_lshrrev_b32 v191,4,v252\nv_lshlrev_b32 v191,3,v191\nv_add_u32 v236,v236,v191\nv_and_b32 v237,15,v252\ns_lshr_b32 s99,s3,1\ns_lshl_b32 s99,s99,6\n"
"v_add_u32 v237,s99,v237\nv_mul_u32_u24 v237,0x700,v237\ns_and_b32 s99,s3,1\ns_lshl_b32 s99,s99,7\nv_add_u32 v237,s99,v237\nv_lshrrev_b32 v191,4,v252\n"
"v_lshlrev_b32 v191,3,v191\nv_add_u32 v237,v237,v191\nv_lshrrev_b32 v250,4,v252\nv_lshrrev_b32 v250,1,v250\nv_xor_b32 v250,1,v250\n"
"v_cvt_f32_u32 v238,v250\nv_mov_b32 v239,0x3a924925\ns_and_b32 s74,s100,15\ns_add_u32 s74,s74,9\ns_lshr_b32 s75,s74,4\ns_and_b32 s75,s75,1\n"
"s_and_b32 s99,s100,15\ns_add_u32 s99,s99,2\ns_lshr_b32 s99,s99,4\ns_xor_b32 s99,s99,1\ns_and_b32 s75,s75,s99\ns_mov_b32 s54,s75\n"
"s_and_b32 s99,s100,15\ns_sub_u32 s99,s99,7\ns_cmp_lg_u32 s75,0\ns_cselect_b32 s74,s99,0\ns_lshr_b32 s99,s100,4\ns_and_b32 s99,s99,15\n"
"s_mul_i32 s99,s99,0x38000\ns_add_u32 s99,0x5000000,s99\ns_lshl_b32 s98,s74,8\ns_add_u32 s99,s99,s98\ns_add_u32 s40,s4,s99\ns_addc_u32 s41,s5,0\n"
"s_and_b32 s41,s41,0xffff\ns_lshl_b32 s42,s74,8\ns_sub_u32 s42,0x38000,s42\ns_mov_b32 s43,0x20000\ns_lshr_b32 s99,s100,4\ns_and_b32 s99,s99,15\n"
"s_lshl_b32 s99,s99,14\ns_add_u32 s99,0x6980000,s99\ns_add_u32 s48,s4,s99\ns_addc_u32 s49,s5,0\ns_and_b32 s49,s49,0xffff\ns_mov_b32 s50,0x4000\n"
"s_mov_b32 s51,0x20000\ns_add_u32 s44,s4,0x6c00000\ns_addc_u32 s45,s5,0\ns_and_b32 s45,s45,0xffff\ns_mov_b32 s46,0x8000\ns_mov_b32 s47,0x20000\n"
"s_lshr_b32 s52,s100,4\ns_and_b32 s52,s52,15\ns_lshl_b32 s52,s52,7\ns_add_u32 s52,0x1000,s52\ns_lshr_b32 s53,s100,4\ns_and_b32 s53,s53,15\n"
"s_lshl_b32 s53,s53,4\ns_and_b32 s99,s100,15\ns_add_u32 s53,s53,s99\ns_lshl_b32 s53,s53,3\ns_add_u32 s53,0x1000,s53\ns_lshl_b32 s55,s74,4\n"
"s_lshr_b32 s99,s100,4\ns_and_b32 s99,s99,15\ns_add_u32 s55,s55,s99\ns_lshl_b32 s55,s55,3\ns_add_u32 s55,0x2000,s55\ns_lshr_b32 s56,s100,21\n"
"s_and_b32 s56,s56,15\ns_lshr_b32 s56,s56,1\ns_lshl_b32 s56,s56,7\ns_add_u32 s56,0x2000,s56\ns_lshr_b32 s57,s100,29\ns_and_b32 s57,s57,1\n"
"s_mov_b32 s74,0x8000\ns_mov_b32 s75,0x4000\ns_mov_b32 s76,0xc000\ns_mov_b32 s77,0x10000\ns_mov_b32 s78,0x18000\ns_mov_b32 s79,0x14000\n"
"s_mov_b32 s80,0x1c000\ns_mul_i32 s62,s3,0x2000\ns_mov_b32 s81,0x20000\ns_mov_b32 s82,0x28000\ns_mov_b32 s83,0x30000\ns_mov_b32 s84,0x38000\n"
"s_mul_i32 s63,s3,0x2000\ns_add_u32 s99,s63,0\ns_add_u32 s98,s99,0x18000\ns_mov_b32 s97,0\ns_mov_b32 s85,0x8000\ns_mov_b32 s86,0x10000\n"
"s_mov_b32 s87,0x18000\ns_mov_b32 s88,0x80\ns_mov_b32 s89,0x8080\ns_mov_b32 s90,0x10080\ns_mov_b32 s91,0x18080\ns_mul_i32 s65,s3,0x1000\n"
"s_mov_b32 s92,0x3800\ns_mov_b32 s93,0x1c000\ns_mov_b32 s94,0x1f800\ns_mul_i32 s66,s3,0x800\nv_mov_b32 v170,0\ns_waitcnt vmcnt(44)\ns_barrier\n"
"xrt 64,8,0\nxrt 66,8,1024\nxrt 68,9,0\nxrt 70,9,1024\nxrt 72,10,0\nxrt 74,10,1024\nxrt 76,11,0\nxrt 78,11,1024\nxrt 80,12,0\nxrt 82,12,1024\n"
"xrt 84,13,0\nxrt 86,13,1024\nxrt 88,14,0\nxrt 90,14,1024\nxrt 92,15,0\nxrt 94,15,1024\nxrb 32,6,0\nxrb 36,6,2048\nxrb 40,6,4096\nxrb 44,6,6144\n"
"xrb 48,6,8192\nxrb 52,6,10240\nxrb 56,6,12288\nxrb 60,6,14336\nxwl 7\nxm16z 0,32,64\nxrt 128,8,8192\nxm16z 4,32,68\nxm16z 8,32,72\nxrt 130,8,9216\n"
"xm16z 12,32,76\nxm16z 16,32,80\nxrt 132,9,8192\nxm16z 20,32,84\nxm16z 24,32,88\nxrt 134,9,9216\nxm16z 28,32,92\nxwl 10\nxm16z 32,36,64\n"
"xrt 136,10,8192\nxm16z 36,36,68\nxm16z 40,36,72\nxrt 138,10,9216\nxm16z 44,36,76\nxm16z 48,36,80\nxrt 140,11,8192\nxm16z 52,36,84\nxm16z 56,36,88\n"
"xrt 142,11,9216\nxm16z 60,36,92\nxwl 13\nxm16z 64,40,64\nxrt 144,12,8192\nxm16z 68,40,68\nxm16z 72,40,72\nxrt 146,12,9216\nxm16z 76,40,76\n"
"xm16z 80,40,80\nxrt 148,13,8192\nxm16z 84,40,84\nxm16z 88,40,88\nxrt 150,13,9216\nxm16z 92,40,92\nxwl 15\nxm16z 96,44,64\nxrt 152,14,8192\n"
"xm16z 100,44,68\nxm16z 104,44,72\nxrt 154,14,9216\nxm16z 108,44,76\nxm16z 112,44,80\nxrt 156,15,8192\nxm16z 116,44,84\nxm16z 120,44,88\n"
"xrt 158,15,9216\nxm16z 124,44,92\nxm16z 128,48,64\nxrb 96,7,0\nxm16z 132,48,68\nxm16z 136,48,72\nxrb 100,7,2048\nxm16z 140,48,76\nxm16z 144,48,80\n"
"xrb 104,7,4096\nxm16z 148,48,84\nxm16z 152,48,88\nxrb 108,7,6144\nxm16z 156,48,92\nxwl 15\nxm16z 160,52,64\nxrb 112,7,8192\nxm16z 164,52,68\n"
"xm16z 168,52,72\nxrb 116,7,10240\nxm16z 172,52,76\nxm16z 176,52,80\nxrb 120,7,12288\nxm16z 180,52,84\nxm16z 184,52,88\nxrb 124,7,14336\n"
"xm16z 188,52,92\nxm16z 192,56,64\nxm16z 196,56,68\nxm16z 200,56,72\nxm16z 204,56,76\nxm16z 208,56,80\nxm16z 212,56,84\nxm16z 216,56,88\n"
"xm16z 220,56,92\nxm16z 224,60,64\nxm16z 228,60,68\nxm16z 232,60,72\nxm16z 236,60,76\nxm16z 240,60,80\nxm16z 244,60,84\nxm16z 248,60,88\n"
"xm16z 252,60,92\ns_waitcnt vmcnt(28) lgkmcnt(0)\ns_barrier\ns_add_u32 s97,s97,0x8000\ns_mov_b32 s96,0x8000\ns_cmp_ge_u32 s97,0x18000\n"
"s_cselect_b32 s96,0xffff0000,s96\ns_cselect_b32 s97,0,s97\nxam 62,0x18000\nxm16 0,96,128\nxdl 0,16,0,0\nv_add_u32 v8,s96,v8\nxrt 64,8,0\n"
"xm16 4,96,132\nxm16 8,96,136\nxrt 66,8,1024\nxm16 12,96,140\nxm16 16,96,144\nv_add_u32 v9,s96,v9\nxrt 68,9,0\nxm16 20,96,148\nxm16 24,96,152\n"
"xrt 70,9,1024\nxam 62,0x18400\nxm16 28,96,156\nxdl 1,16,s74,0\nxm16 32,100,128\nv_add_u32 v10,s96,v10\nxrt 72,10,0\nxm16 36,100,132\nxm16 40,100,136\n"
"xrt 74,10,1024\nxm16 44,100,140\nxm16 48,100,144\nv_add_u32 v11,s96,v11\nxrt 76,11,0\nxm16 52,100,148\nxam 62,0x18800\nxm16 56,100,152\n"
"xdl 0,16,s75,0\nxrt 78,11,1024\nxm16 60,100,156\nxm16 64,104,128\nv_add_u32 v12,s96,v12\nxrt 80,12,0\nxm16 68,104,132\nxm16 72,104,136\n"
"xrt 82,12,1024\nxm16 76,104,140\nxm16 80,104,144\nv_add_u32 v13,s96,v13\nxrt 84,13,0\nxam 62,0x18c00\nxm16 84,104,148\nxdl 1,16,s76,0\n"
"xm16 88,104,152\nxrt 86,13,1024\nxm16 92,104,156\nxm16 96,108,128\nv_add_u32 v14,s96,v14\nxrt 88,14,0\nxm16 100,108,132\nxm16 104,108,136\n"
"xrt 90,14,1024\nxm16 108,108,140\nxam 62,0x19000\nxm16 112,108,144\nxdl 0,16,s77,0\nv_add_u32 v15,s96,v15\nxrt 92,15,0\nxm16 116,108,148\n"
"xm16 120,108,152\nxrt 94,15,1024\nxm16 124,108,156\nxm16 128,112,128\nxrb 32,6,32768\nxm16 132,112,132\nxm16 136,112,136\nxrb 36,6,34816\n"
"xam 62,0x19400\nxm16 140,112,140\nxdl 1,16,s78,0\nxm16 144,112,144\nxrb 40,6,36864\nxm16 148,112,148\nxm16 152,112,152\nxrb 44,6,38912\n"
"xm16 156,112,156\nxm16 160,116,128\nxrb 48,6,40960\nxm16 164,116,132\nxam 62,0x19800\nxm16 168,116,136\nxdl 0,16,s79,0\nxrb 52,6,43008\n"
"xm16 172,116,140\nxm16 176,116,144\nxrb 56,6,45056\nxm16 180,116,148\nxm16 184,116,152\nxrb 60,6,47104\nxm16 188,116,156\nxm16 192,120,128\n"
"xam 62,0x19c00\nxm16 196,120,132\nxdl 1,16,s80,0\ns_add_u32 s16,s16,0x80\ns_addc_u32 s17,s17,0\ns_sub_u32 s18,s18,0x80\nxm16 200,120,136\n"
"xm16 204,120,140\nxm16 208,120,144\nxm16 212,120,148\nxm16 216,120,152\nxm16 220,120,156\nxm16 224,124,128\nxm16 228,124,132\nxm16 232,124,136\n"
"xm16 236,124,140\nxm16 240,124,144\nxm16 244,124,148\nxm16 248,124,152\nxm16 252,124,156\nxam 99,0\nxwl 7\nxm16 0,32,64\nxdl 2,20,0,0\n"
"xrt 128,8,8192\nxm16 4,32,68\nxm16 8,32,72\nxrt 130,8,9216\nxm16 12,32,76\nxm16 16,32,80\nxrt 132,9,8192\nxm16 20,32,84\nxm16 24,32,88\n"
"xrt 134,9,9216\nxam 99,0x400\nxm16 28,32,92\nxdl 2,20,s74,0\nxwl 10\nxm16 32,36,64\nxrt 136,10,8192\nxm16 36,36,68\nxm16 40,36,72\nxrt 138,10,9216\n"
"xm16 44,36,76\nxm16 48,36,80\nxrt 140,11,8192\nxm16 52,36,84\nxam 99,0x800\nxm16 56,36,88\nxdl 3,20,s77,0\nxrt 142,11,9216\nxm16 60,36,92\nxwl 13\n"
"xm16 64,40,64\nxrt 144,12,8192\nxm16 68,40,68\nxm16 72,40,72\nxrt 146,12,9216\nxm16 76,40,76\nxm16 80,40,80\nxrt 148,13,8192\nxam 99,0xc00\n"
"xm16 84,40,84\nxdl 3,20,s78,0\nxm16 88,40,88\nxrt 150,13,9216\nxm16 92,40,92\nxwl 15\nxm16 96,44,64\nxrt 152,14,8192\nxm16 100,44,68\nxm16 104,44,72\n"
"xrt 154,14,9216\nxm16 108,44,76\nxam 99,0x1000\nxm16 112,44,80\nxdl 2,20,s81,0\nxrt 156,15,8192\nxm16 116,44,84\nxm16 120,44,88\nxrt 158,15,9216\n"
"xm16 124,44,92\nxm16 128,48,64\nxrb 96,7,32768\nxm16 132,48,68\nxm16 136,48,72\nxrb 100,7,34816\nxam 99,0x1400\nxm16 140,48,76\nxdl 2,20,s82,0\n"
"xm16 144,48,80\nxrb 104,7,36864\nxm16 148,48,84\nxm16 152,48,88\nxrb 108,7,38912\nxm16 156,48,92\nxwl 15\nxm16 160,52,64\nxrb 112,7,40960\n"
"xm16 164,52,68\nxam 99,0x1800\nxm16 168,52,72\nxdl 3,20,s83,0\nxrb 116,7,43008\nxm16 172,52,76\nxm16 176,52,80\nxrb 120,7,45056\nxm16 180,52,84\n"
"xm16 184,52,88\nxrb 124,7,47104\nxm16 188,52,92\nxm16 192,56,64\nxam 99,0x1c00\nxm16 196,56,68\nxdl 3,20,s84,0\ns_add_u32 s99,s99,0x8000\n"
"s_cmp_ge_u32 s99,s98\ns_cselect_b32 s95,0x18000,0\ns_sub_u32 s99,s99,s95\ns_add_u32 s20,s20,0x80000\ns_addc_u32 s21,s21,0\ns_sub_u32 s22,s22,0x80000\n"
"xm16 200,56,72\nxm16 204,56,76\nxm16 208,56,80\nxm16 212,56,84\nxm16 216,56,88\nxm16 220,56,92\nxm16 224,60,64\nxm16 228,60,68\nxm16 232,60,72\n"
"xm16 236,60,76\nxm16 240,60,80\nxm16 244,60,84\nxm16 248,60,88\nxm16 252,60,92\nbuffer_load_dword v171,v164,s[36:39],0 offen offset:0\n"
"s_mov_b32 s68,0x4000\nbuffer_load_dword v171,v164,s[36:39],s68 offen offset:0\ns_waitcnt vmcnt(8) lgkmcnt(0)\ns_barrier\ns_cmp_eq_u32 s3,0\n"
"s_cbranch_scc0 Lnosig_31_%=\nv_mov_b32 v166,s6\nv_mov_b32 v167,s7\nv_mov_b32 v169,0\ns_mov_b64 exec,1\n"
"buffer_store_dwordx2 v[166:167],v169,s[44:47],s53 offen offset:0 sc1\ns_mov_b64 exec,-1\nLnosig_31_%=:\ns_nop 1\ns_add_u32 s97,s97,0x8000\n"
"s_mov_b32 s96,0x8000\ns_cmp_ge_u32 s97,0x18000\ns_cselect_b32 s96,0xffff0000,s96\ns_cselect_b32 s97,0,s97\nxam 62,0x20000\nxm16 0,96,128\n"
"xdl 0,16,0,0\nv_add_u32 v8,s96,v8\nxrt 64,8,0\nxm16 4,96,132\nxm16 8,96,136\nxrt 66,8,1024\nxm16 12,96,140\nxm16 16,96,144\nv_add_u32 v9,s96,v9\n"
"xrt 68,9,0\nxm16 20,96,148\nxm16 24,96,152\nxrt 70,9,1024\nxam 62,0x20400\nxm16 28,96,156\nxdl 1,16,s74,0\nxm16 32,100,128\nv_add_u32 v10,s96,v10\n"
"xrt 72,10,0\nxm16 36,100,132\nxm16 40,100,136\nxrt 74,10,1024\nxm16 44,100,140\nxm16 48,100,144\nv_add_u32 v11,s96,v11\nxrt 76,11,0\nxm16 52,100,148\n"
"xam 62,0x20800\nxm16 56,100,152\nxdl 0,16,s75,0\nxrt 78,11,1024\nxm16 60,100,156\nxm16 64,104,128\nv_add_u32 v12,s96,v12\nxrt 80,12,0\n"
"xm16 68,104,132\nxm16 72,104,136\nxrt 82,12,1024\nxm16 76,104,140\nxm16 80,104,144\nv_add_u32 v13,s96,v13\nxrt 84,13,0\nxam 62,0x20c00\n"
"xm16 84,104,148\nxdl 1,16,s76,0\nxm16 88,104,152\nxrt 86,13,1024\nxm16 92,104,156\nxm16 96,108,128\nv_add_u32 v14,s96,v14\nxrt 88,14,0\n"
"xm16 100,108,132\nxm16 104,108,136\nxrt 90,14,1024\nxm16 108,108,140\nxam 62,0x21000\nxm16 112,108,144\nxdl 0,16,s77,0\nv_add_u32 v15,s96,v15\n"
"xrt 92,15,0\nxm16 116,108,148\nxm16 120,108,152\nxrt 94,15,1024\nxm16 124,108,156\nxm16 128,112,128\nxrb 32,6,0\nxm16 132,112,132\nxm16 136,112,136\n"
"xrb 36,6,2048\nxam 62,0x21400\nxm16 140,112,140\nxdl 1,16,s78,0\nxm16 144,112,144\nxrb 40,6,4096\nxm16 148,112,148\nxm16 152,112,152\nxrb 44,6,6144\n"
"xm16 156,112,156\nxm16 160,116,128\nxrb 48,6,8192\nxm16 164,116,132\nxam 62,0x21800\nxm16 168,116,136\nxdl 0,16,s79,0\nxrb 52,6,10240\n"
"xm16 172,116,140\nxm16 176,116,144\nxrb 56,6,12288\nxm16 180,116,148\nxm16 184,116,152\nxrb 60,6,14336\nxm16 188,116,156\nxm16 192,120,128\n"
"xam 62,0x21c00\nxm16 196,120,132\nxdl 1,16,s80,0\ns_add_u32 s16,s16,0x80\ns_addc_u32 s17,s17,0\ns_sub_u32 s18,s18,0x80\nxm16 200,120,136\n"
"xm16 204,120,140\nxm16 208,120,144\nxm16 212,120,148\nxm16 216,120,152\nxm16 220,120,156\nxm16 224,124,128\nxm16 228,124,132\nxm16 232,124,136\n"
"xm16 236,124,140\nxm16 240,124,144\nxm16 244,124,148\nxm16 248,124,152\nxm16 252,124,156\ns_mov_b32 s71,5\nLloop_32_%=:\nxam 99,0\nxwl 7\n"
"xm16 0,32,64\nxdl 2,20,0,0\nxrt 128,8,8192\nxm16 4,32,68\nxm16 8,32,72\nxrt 130,8,9216\nxm16 12,32,76\nxm16 16,32,80\nxrt 132,9,8192\nxm16 20,32,84\n"
"xm16 24,32,88\nxrt 134,9,9216\nxam 99,0x400\nxm16 28,32,92\nxdl 2,20,s74,0\nxwl 10\nxm16 32,36,64\nxrt 136,10,8192\nxm16 36,36,68\nxm16 40,36,72\n"
"xrt 138,10,9216\nxm16 44,36,76\nxm16 48,36,80\nxrt 140,11,8192\nxm16 52,36,84\nxam 99,0x800\nxm16 56,36,88\nxdl 3,20,s77,0\nxrt 142,11,9216\n"
"xm16 60,36,92\nxwl 13\nxm16 64,40,64\nxrt 144,12,8192\nxm16 68,40,68\nxm16 72,40,72\nxrt 146,12,9216\nxm16 76,40,76\nxm16 80,40,80\nxrt 148,13,8192\n"
"xam 99,0xc00\nxm16 84,40,84\nxdl 3,20,s78,0\nxm16 88,40,88\nxrt 150,13,9216\nxm16 92,40,92\nxwl 15\nxm16 96,44,64\nxrt 152,14,8192\nxm16 100,44,68\n"
"xm16 104,44,72\nxrt 154,14,9216\nxm16 108,44,76\nxam 99,0x1000\nxm16 112,44,80\nxdl 2,20,s81,0\nxrt 156,15,8192\nxm16 116,44,84\nxm16 120,44,88\n"
"xrt 158,15,9216\nxm16 124,44,92\nxm16 128,48,64\nxrb 96,7,0\nxm16 132,48,68\nxm16 136,48,72\nxrb 100,7,2048\nxam 99,0x1400\nxm16 140,48,76\n"
"xdl 2,20,s82,0\nxm16 144,48,80\nxrb 104,7,4096\nxm16 148,48,84\nxm16 152,48,88\nxrb 108,7,6144\nxm16 156,48,92\nxwl 15\nxm16 160,52,64\n"
"xrb 112,7,8192\nxm16 164,52,68\nxam 99,0x1800\nxm16 168,52,72\nxdl 3,20,s83,0\nxrb 116,7,10240\nxm16 172,52,76\nxm16 176,52,80\nxrb 120,7,12288\n"
"xm16 180,52,84\nxm16 184,52,88\nxrb 124,7,14336\nxm16 188,52,92\nxm16 192,56,64\nxam 99,0x1c00\nxm16 196,56,68\nxdl 3,20,s84,0\n"
"s_add_u32 s99,s99,0x8000\ns_cmp_ge_u32 s99,s98\ns_cselect_b32 s95,0x18000,0\ns_sub_u32 s99,s99,s95\ns_add_u32 s20,s20,0x80000\ns_addc_u32 s21,s21,0\n"
"s_sub_u32 s22,s22,0x80000\nxm16 200,56,72\nxm16 204,56,76\nxm16 208,56,80\nxm16 212,56,84\nxm16 216,56,88\nxm16 220,56,92\nxm16 224,60,64\n"
"xm16 228,60,68\nxm16 232,60,72\nxm16 236,60,76\nxm16 240,60,80\nxm16 244,60,84\nxm16 248,60,88\nxm16 252,60,92\ns_waitcnt vmcnt(8) lgkmcnt(0)\n"
"s_barrier\ns_cmp_eq_u32 s71,4\ns_cbranch_scc1 Llnhook_22_%=\nLlnret_23_%=:\ns_cmp_eq_u32 s71,3\ns_cbranch_scc1 Lznhook_24_%=\nLznret_25_%=:\n"
"s_add_u32 s97,s97,0x8000\ns_mov_b32 s96,0x8000\ns_cmp_ge_u32 s97,0x18000\ns_cselect_b32 s96,0xffff0000,s96\ns_cselect_b32 s97,0,s97\nxam 62,0x18000\n"
"xm16 0,96,128\nxdl 0,16,0,0\nv_add_u32 v8,s96,v8\nxrt 64,8,0\nxm16 4,96,132\nxm16 8,96,136\nxrt 66,8,1024\nxm16 12,96,140\nxm16 16,96,144\n"
"v_add_u32 v9,s96,v9\nxrt 68,9,0\nxm16 20,96,148\nxm16 24,96,152\nxrt 70,9,1024\nxam 62,0x18400\nxm16 28,96,156\nxdl 1,16,s74,0\nxm16 32,100,128\n"
"v_add_u32 v10,s96,v10\nxrt 72,10,0\nxm16 36,100,132\nxm16 40,100,136\nxrt 74,10,1024\nxm16 44,100,140\nxm16 48,100,144\nv_add_u32 v11,s96,v11\n"
"xrt 76,11,0\nxm16 52,100,148\nxam 62,0x18800\nxm16 56,100,152\nxdl 0,16,s75,0\nxrt 78,11,1024\nxm16 60,100,156\nxm16 64,104,128\n"
"v_add_u32 v12,s96,v12\nxrt 80,12,0\nxm16 68,104,132\nxm16 72,104,136\nxrt 82,12,1024\nxm16 76,104,140\nxm16 80,104,144\nv_add_u32 v13,s96,v13\n"
"xrt 84,13,0\nxam 62,0x18c00\nxm16 84,104,148\nxdl 1,16,s76,0\nxm16 88,104,152\nxrt 86,13,1024\nxm16 92,104,156\nxm16 96,108,128\n"
"v_add_u32 v14,s96,v14\nxrt 88,14,0\nxm16 100,108,132\nxm16 104,108,136\nxrt 90,14,1024\nxm16 108,108,140\nxam 62,0x19000\nxm16 112,108,144\n"
"xdl 0,16,s77,0\nv_add_u32 v15,s96,v15\nxrt 92,15,0\nxm16 116,108,148\nxm16 120,108,152\nxrt 94,15,1024\nxm16 124,108,156\nxm16 128,112,128\n"
"xrb 32,6,32768\nxm16 132,112,132\nxm16 136,112,136\nxrb 36,6,34816\nxam 62,0x19400\nxm16 140,112,140\nxdl 1,16,s78,0\nxm16 144,112,144\n"
"xrb 40,6,36864\nxm16 148,112,148\nxm16 152,112,152\nxrb 44,6,38912\nxm16 156,112,156\nxm16 160,116,128\nxrb 48,6,40960\nxm16 164,116,132\n"
"xam 62,0x19800\nxm16 168,116,136\nxdl 0,16,s79,0\nxrb 52,6,43008\nxm16 172,116,140\nxm16 176,116,144\nxrb 56,6,45056\nxm16 180,116,148\n"
"xm16 184,116,152\nxrb 60,6,47104\nxm16 188,116,156\nxm16 192,120,128\nxam 62,0x19c00\nxm16 196,120,132\nxdl 1,16,s80,0\ns_add_u32 s16,s16,0x80\n"
"s_addc_u32 s17,s17,0\ns_sub_u32 s18,s18,0x80\nxm16 200,120,136\nxm16 204,120,140\nxm16 208,120,144\nxm16 212,120,148\nxm16 216,120,152\n"
"xm16 220,120,156\nxm16 224,124,128\nxm16 228,124,132\nxm16 232,124,136\nxm16 236,124,140\nxm16 240,124,144\nxm16 244,124,148\nxm16 248,124,152\n"
"xm16 252,124,156\nxam 99,0\nxwl 7\nxm16 0,32,64\nxdl 2,20,0,0\nxrt 128,8,8192\nxm16 4,32,68\nxm16 8,32,72\nxrt 130,8,9216\nxm16 12,32,76\n"
"xm16 16,32,80\nxrt 132,9,8192\nxm16 20,32,84\nxm16 24,32,88\nxrt 134,9,9216\nxam 99,0x400\nxm16 28,32,92\nxdl 2,20,s74,0\nxwl 10\nxm16 32,36,64\n"
"xrt 136,10,8192\nxm16 36,36,68\nxm16 40,36,72\nxrt 138,10,9216\nxm16 44,36,76\nxm16 48,36,80\nxrt 140,11,8192\nxm16 52,36,84\nxam 99,0x800\n"
"xm16 56,36,88\nxdl 3,20,s77,0\nxrt 142,11,9216\nxm16 60,36,92\nxwl 13\nxm16 64,40,64\nxrt 144,12,8192\nxm16 68,40,68\nxm16 72,40,72\nxrt 146,12,9216\n"
"xm16 76,40,76\nxm16 80,40,80\nxrt 148,13,8192\nxam 99,0xc00\nxm16 84,40,84\nxdl 3,20,s78,0\nxm16 88,40,88\nxrt 150,13,9216\nxm16 92,40,92\nxwl 15\n"
"xm16 96,44,64\nxrt 152,14,8192\nxm16 100,44,68\nxm16 104,44,72\nxrt 154,14,9216\nxm16 108,44,76\nxam 99,0x1000\nxm16 112,44,80\nxdl 2,20,s81,0\n"
"xrt 156,15,8192\nxm16 116,44,84\nxm16 120,44,88\nxrt 158,15,9216\nxm16 124,44,92\nxm16 128,48,64\nxrb 96,7,32768\nxm16 132,48,68\nxm16 136,48,72\n"
"xrb 100,7,34816\nxam 99,0x1400\nxm16 140,48,76\nxdl 2,20,s82,0\nxm16 144,48,80\nxrb 104,7,36864\nxm16 148,48,84\nxm16 152,48,88\nxrb 108,7,38912\n"
"xm16 156,48,92\nxwl 15\nxm16 160,52,64\nxrb 112,7,40960\nxm16 164,52,68\nxam 99,0x1800\nxm16 168,52,72\nxdl 3,20,s83,0\nxrb 116,7,43008\n"
"xm16 172,52,76\nxm16 176,52,80\nxrb 120,7,45056\nxm16 180,52,84\nxm16 184,52,88\nxrb 124,7,47104\nxm16 188,52,92\nxm16 192,56,64\nxam 99,0x1c00\n"
"xm16 196,56,68\nxdl 3,20,s84,0\ns_add_u32 s99,s99,0x8000\ns_cmp_ge_u32 s99,s98\ns_cselect_b32 s95,0x18000,0\ns_sub_u32 s99,s99,s95\n"
"s_add_u32 s20,s20,0x80000\ns_addc_u32 s21,s21,0\ns_sub_u32 s22,s22,0x80000\nxm16 200,56,72\nxm16 204,56,76\nxm16 208,56,80\nxm16 212,56,84\n"
"xm16 216,56,88\nxm16 220,56,92\nxm16 224,60,64\nxm16 228,60,68\nxm16 232,60,72\nxm16 236,60,76\nxm16 240,60,80\nxm16 244,60,84\nxm16 248,60,88\n"
"xm16 252,60,92\ns_waitcnt vmcnt(8) lgkmcnt(0)\ns_barrier\ns_add_u32 s97,s97,0x8000\ns_mov_b32 s96,0x8000\ns_cmp_ge_u32 s97,0x18000\n"
"s_cselect_b32 s96,0xffff0000,s96\ns_cselect_b32 s97,0,s97\nxam 62,0x20000\nxm16 0,96,128\nxdl 0,16,0,0\nv_add_u32 v8,s96,v8\nxrt 64,8,0\n"
"xm16 4,96,132\nxm16 8,96,136\nxrt 66,8,1024\nxm16 12,96,140\nxm16 16,96,144\nv_add_u32 v9,s96,v9\nxrt 68,9,0\nxm16 20,96,148\nxm16 24,96,152\n"
"xrt 70,9,1024\nxam 62,0x20400\nxm16 28,96,156\nxdl 1,16,s74,0\nxm16 32,100,128\nv_add_u32 v10,s96,v10\nxrt 72,10,0\nxm16 36,100,132\nxm16 40,100,136\n"
"xrt 74,10,1024\nxm16 44,100,140\nxm16 48,100,144\nv_add_u32 v11,s96,v11\nxrt 76,11,0\nxm16 52,100,148\nxam 62,0x20800\nxm16 56,100,152\n"
"xdl 0,16,s75,0\nxrt 78,11,1024\nxm16 60,100,156\nxm16 64,104,128\nv_add_u32 v12,s96,v12\nxrt 80,12,0\nxm16 68,104,132\nxm16 72,104,136\n"
"xrt 82,12,1024\nxm16 76,104,140\nxm16 80,104,144\nv_add_u32 v13,s96,v13\nxrt 84,13,0\nxam 62,0x20c00\nxm16 84,104,148\nxdl 1,16,s76,0\n"
"xm16 88,104,152\nxrt 86,13,1024\nxm16 92,104,156\nxm16 96,108,128\nv_add_u32 v14,s96,v14\nxrt 88,14,0\nxm16 100,108,132\nxm16 104,108,136\n"
"xrt 90,14,1024\nxm16 108,108,140\nxam 62,0x21000\nxm16 112,108,144\nxdl 0,16,s77,0\nv_add_u32 v15,s96,v15\nxrt 92,15,0\nxm16 116,108,148\n"
"xm16 120,108,152\nxrt 94,15,1024\nxm16 124,108,156\nxm16 128,112,128\nxrb 32,6,0\nxm16 132,112,132\nxm16 136,112,136\nxrb 36,6,2048\nxam 62,0x21400\n"
"xm16 140,112,140\nxdl 1,16,s78,0\nxm16 144,112,144\nxrb 40,6,4096\nxm16 148,112,148\nxm16 152,112,152\nxrb 44,6,6144\nxm16 156,112,156\n"
"xm16 160,116,128\nxrb 48,6,8192\nxm16 164,116,132\nxam 62,0x21800\nxm16 168,116,136\nxdl 0,16,s79,0\nxrb 52,6,10240\nxm16 172,116,140\n"
"xm16 176,116,144\nxrb 56,6,12288\nxm16 180,116,148\nxm16 184,116,152\nxrb 60,6,14336\nxm16 188,116,156\nxm16 192,120,128\nxam 62,0x21c00\n"
"xm16 196,120,132\nxdl 1,16,s80,0\ns_add_u32 s16,s16,0x80\ns_addc_u32 s17,s17,0\ns_sub_u32 s18,s18,0x80\nxm16 200,120,136\nxm16 204,120,140\n"
"xm16 208,120,144\nxm16 212,120,148\nxm16 216,120,152\nxm16 220,120,156\nxm16 224,124,128\nxm16 228,124,132\nxm16 232,124,136\nxm16 236,124,140\n"
"xm16 240,124,144\nxm16 244,124,148\nxm16 248,124,152\nxm16 252,124,156\ns_sub_u32 s71,s71,1\ns_cmp_gt_i32 s71,0\ns_cbranch_scc1 Lloop_32_%=\n"
"xam 99,0\nxwl 7\nxm16 0,32,64\nxdl 2,20,0,0\nxrt 128,8,8192\nxm16 4,32,68\nxm16 8,32,72\nxrt 130,8,9216\nxm16 12,32,76\nxm16 16,32,80\n"
"xrt 132,9,8192\nxm16 20,32,84\nxm16 24,32,88\nxrt 134,9,9216\nxam 99,0x400\nxm16 28,32,92\nxdl 2,20,s74,0\nxwl 10\nxm16 32,36,64\nxrt 136,10,8192\n"
"xm16 36,36,68\nxm16 40,36,72\nxrt 138,10,9216\nxm16 44,36,76\nxm16 48,36,80\nxrt 140,11,8192\nxm16 52,36,84\nxam 99,0x800\nxm16 56,36,88\n"
"xdl 3,20,s77,0\nxrt 142,11,9216\nxm16 60,36,92\nxwl 13\nxm16 64,40,64\nxrt 144,12,8192\nxm16 68,40,68\nxm16 72,40,72\nxrt 146,12,9216\nxm16 76,40,76\n"
"xm16 80,40,80\nxrt 148,13,8192\nxam 99,0xc00\nxm16 84,40,84\nxdl 3,20,s78,0\nxm16 88,40,88\nxrt 150,13,9216\nxm16 92,40,92\nxwl 15\nxm16 96,44,64\n"
"xrt 152,14,8192\nxm16 100,44,68\nxm16 104,44,72\nxrt 154,14,9216\nxm16 108,44,76\nxam 99,0x1000\nxm16 112,44,80\nxdl 2,20,s81,0\nxrt 156,15,8192\n"
"xm16 116,44,84\nxm16 120,44,88\nxrt 158,15,9216\nxm16 124,44,92\nxm16 128,48,64\nxrb 96,7,0\nxm16 132,48,68\nxm16 136,48,72\nxrb 100,7,2048\n"
"xam 99,0x1400\nxm16 140,48,76\nxdl 2,20,s82,0\nxm16 144,48,80\nxrb 104,7,4096\nxm16 148,48,84\nxm16 152,48,88\nxrb 108,7,6144\nxm16 156,48,92\n"
"xwl 15\nxm16 160,52,64\nxrb 112,7,8192\nxm16 164,52,68\nxam 99,0x1800\nxm16 168,52,72\nxdl 3,20,s83,0\nxrb 116,7,10240\nxm16 172,52,76\n"
"xm16 176,52,80\nxrb 120,7,12288\nxm16 180,52,84\nxm16 184,52,88\nxrb 124,7,14336\nxm16 188,52,92\nxm16 192,56,64\nxam 99,0x1c00\nxm16 196,56,68\n"
"xdl 3,20,s84,0\ns_add_u32 s99,s99,0x8000\ns_cmp_ge_u32 s99,s98\ns_cselect_b32 s95,0x18000,0\ns_sub_u32 s99,s99,s95\ns_add_u32 s20,s20,0x80000\n"
"s_addc_u32 s21,s21,0\ns_sub_u32 s22,s22,0x80000\nxm16 200,56,72\nxm16 204,56,76\nxm16 208,56,80\nxm16 212,56,84\nxm16 216,56,88\nxm16 220,56,92\n"
"xm16 224,60,64\nxm16 228,60,68\nxm16 232,60,72\nxm16 236,60,76\nxm16 240,60,80\nxm16 244,60,84\nxm16 248,60,88\nxm16 252,60,92\n"
"s_waitcnt vmcnt(8) lgkmcnt(0)\ns_barrier\ns_add_u32 s97,s97,0x8000\ns_mov_b32 s96,0x8000\ns_cmp_ge_u32 s97,0x18000\ns_cselect_b32 s96,0xffff0000,s96\n"
"s_cselect_b32 s97,0,s97\nxam 62,0x18000\nxm16 0,96,128\nxdl 0,16,0,0\nv_add_u32 v8,s96,v8\nxrt 64,8,0\nxm16 4,96,132\nxm16 8,96,136\nxrt 66,8,1024\n"
"xm16 12,96,140\nxm16 16,96,144\nv_add_u32 v9,s96,v9\nxrt 68,9,0\nxm16 20,96,148\nxm16 24,96,152\nxrt 70,9,1024\nxam 62,0x18400\nxm16 28,96,156\n"
"xdl 1,16,s74,0\nxm16 32,100,128\nv_add_u32 v10,s96,v10\nxrt 72,10,0\nxm16 36,100,132\nxm16 40,100,136\nxrt 74,10,1024\nxm16 44,100,140\n"
"xm16 48,100,144\nv_add_u32 v11,s96,v11\nxrt 76,11,0\nxm16 52,100,148\nxam 62,0x18800\nxm16 56,100,152\nxdl 0,16,s75,0\nxrt 78,11,1024\n"
"xm16 60,100,156\nxm16 64,104,128\nv_add_u32 v12,s96,v12\nxrt 80,12,0\nxm16 68,104,132\nxm16 72,104,136\nxrt 82,12,1024\nxm16 76,104,140\n"
"xm16 80,104,144\nv_add_u32 v13,s96,v13\nxrt 84,13,0\nxam 62,0x18c00\nxm16 84,104,148\nxdl 1,16,s76,0\nxm16 88,104,152\nxrt 86,13,1024\n"
"xm16 92,104,156\nxm16 96,108,128\nv_add_u32 v14,s96,v14\nxrt 88,14,0\nxm16 100,108,132\nxm16 104,108,136\nxrt 90,14,1024\nxm16 108,108,140\n"
"xam 62,0x19000\nxm16 112,108,144\nxdl 0,16,s77,0\nv_add_u32 v15,s96,v15\nxrt 92,15,0\nxm16 116,108,148\nxm16 120,108,152\nxrt 94,15,1024\n"
"xm16 124,108,156\nxm16 128,112,128\nxrb 32,6,32768\nxm16 132,112,132\nxm16 136,112,136\nxrb 36,6,34816\nxam 62,0x19400\nxm16 140,112,140\n"
"xdl 1,16,s78,0\nxm16 144,112,144\nxrb 40,6,36864\nxm16 148,112,148\nxm16 152,112,152\nxrb 44,6,38912\nxm16 156,112,156\nxm16 160,116,128\n"
"xrb 48,6,40960\nxm16 164,116,132\nxam 62,0x19800\nxm16 168,116,136\nxdl 0,16,s79,0\nxrb 52,6,43008\nxm16 172,116,140\nxm16 176,116,144\n"
"xrb 56,6,45056\nxm16 180,116,148\nxm16 184,116,152\nxrb 60,6,47104\nxm16 188,116,156\nxm16 192,120,128\nxam 62,0x19c00\nxm16 196,120,132\n"
"xdl 1,16,s80,0\ns_add_u32 s16,s16,0x80\ns_addc_u32 s17,s17,0\ns_sub_u32 s18,s18,0x80\nxm16 200,120,136\nxm16 204,120,140\nxm16 208,120,144\n"
"xm16 212,120,148\nxm16 216,120,152\nxm16 220,120,156\nxm16 224,124,128\nxm16 228,124,132\nxm16 232,124,136\nxm16 236,124,140\nxm16 240,124,144\n"
"xm16 244,124,148\nxm16 248,124,152\nxm16 252,124,156\nxam 99,0\nxwl 7\nxm16 0,32,64\nxdl 2,20,0,0\nxrt 128,8,8192\nxm16 4,32,68\nxm16 8,32,72\n"
"xrt 130,8,9216\nxm16 12,32,76\nxm16 16,32,80\nxrt 132,9,8192\nxm16 20,32,84\nxm16 24,32,88\nxrt 134,9,9216\nxam 99,0x400\nxm16 28,32,92\n"
"xdl 2,20,s74,0\nxwl 10\nxm16 32,36,64\nxrt 136,10,8192\nxm16 36,36,68\nxm16 40,36,72\nxrt 138,10,9216\nxm16 44,36,76\nxm16 48,36,80\nxrt 140,11,8192\n"
"xm16 52,36,84\nxam 99,0x800\nxm16 56,36,88\nxdl 3,20,s77,0\nxrt 142,11,9216\nxm16 60,36,92\nxwl 13\nxm16 64,40,64\nxrt 144,12,8192\nxm16 68,40,68\n"
"xm16 72,40,72\nxrt 146,12,9216\nxm16 76,40,76\nxm16 80,40,80\nxrt 148,13,8192\nxam 99,0xc00\nxm16 84,40,84\nxdl 3,20,s78,0\nxm16 88,40,88\n"
"xrt 150,13,9216\nxm16 92,40,92\nxwl 15\nxm16 96,44,64\nxrt 152,14,8192\nxm16 100,44,68\nxm16 104,44,72\nxrt 154,14,9216\nxm16 108,44,76\n"
"xam 99,0x1000\nxm16 112,44,80\nxdl 2,20,s81,0\nxrt 156,15,8192\nxm16 116,44,84\nxm16 120,44,88\nxrt 158,15,9216\nxm16 124,44,92\nxm16 128,48,64\n"
"xrb 96,7,32768\nxm16 132,48,68\nxm16 136,48,72\nxrb 100,7,34816\nxam 99,0x1400\nxm16 140,48,76\nxdl 2,20,s82,0\nxm16 144,48,80\nxrb 104,7,36864\n"
"xm16 148,48,84\nxm16 152,48,88\nxrb 108,7,38912\nxm16 156,48,92\nxwl 15\nxm16 160,52,64\nxrb 112,7,40960\nxm16 164,52,68\nxam 99,0x1800\n"
"xm16 168,52,72\nxdl 3,20,s83,0\nxrb 116,7,43008\nxm16 172,52,76\nxm16 176,52,80\nxrb 120,7,45056\nxm16 180,52,84\nxm16 184,52,88\nxrb 124,7,47104\n"
"xm16 188,52,92\nxm16 192,56,64\nxam 99,0x1c00\nxm16 196,56,68\nxdl 3,20,s84,0\ns_add_u32 s99,s99,0x8000\ns_cmp_ge_u32 s99,s98\n"
"s_cselect_b32 s95,0x18000,0\ns_sub_u32 s99,s99,s95\ns_add_u32 s20,s20,0x80000\ns_addc_u32 s21,s21,0\ns_sub_u32 s22,s22,0x80000\nxm16 200,56,72\n"
"xm16 204,56,76\nxm16 208,56,80\nxm16 212,56,84\nxm16 216,56,88\nxm16 220,56,92\nxm16 224,60,64\nxm16 228,60,68\nxm16 232,60,72\nxm16 236,60,76\n"
"xm16 240,60,80\nxm16 244,60,84\nxm16 248,60,88\nxm16 252,60,92\ns_waitcnt vmcnt(8) lgkmcnt(0)\ns_barrier\nv_mbcnt_lo_u32_b32 v168,-1,0\n"
"v_mbcnt_hi_u32_b32 v168,-1,v168\nv_lshlrev_b32 v168,3,v168\nbuffer_load_dwordx2 v[166:167],v168,s[44:47],s56 offen offset:0 sc1\n"
"s_add_u32 s97,s97,0x8000\ns_mov_b32 s96,0x8000\ns_cmp_ge_u32 s97,0x18000\ns_cselect_b32 s96,0xffff0000,s96\ns_cselect_b32 s97,0,s97\nxam 62,0x20000\n"
"xm16 0,96,128\nxdl 0,16,0,0\nv_add_u32 v8,s96,v8\nxrt 64,8,0\nxm16 4,96,132\nxm16 8,96,136\nxrt 66,8,1024\nxm16 12,96,140\nxm16 16,96,144\n"
"v_add_u32 v9,s96,v9\nxrt 68,9,0\nxm16 20,96,148\nxm16 24,96,152\nxrt 70,9,1024\nxam 62,0x20400\nxm16 28,96,156\nxdl 1,16,s74,0\nxm16 32,100,128\n"
"v_add_u32 v10,s96,v10\nxrt 72,10,0\nxm16 36,100,132\nxm16 40,100,136\nxrt 74,10,1024\nxm16 44,100,140\nxm16 48,100,144\nv_add_u32 v11,s96,v11\n"
"xrt 76,11,0\nxm16 52,100,148\nxam 62,0x20800\nxm16 56,100,152\nxdl 0,16,s75,0\nxrt 78,11,1024\nxm16 60,100,156\nxm16 64,104,128\n"
"v_add_u32 v12,s96,v12\nxrt 80,12,0\nxm16 68,104,132\nxm16 72,104,136\nxrt 82,12,1024\nxm16 76,104,140\nxm16 80,104,144\nv_add_u32 v13,s96,v13\n"
"xrt 84,13,0\nxam 62,0x20c00\nxm16 84,104,148\nxdl 1,16,s76,0\nxm16 88,104,152\nxrt 86,13,1024\nxm16 92,104,156\nxm16 96,108,128\n"
"v_add_u32 v14,s96,v14\nxrt 88,14,0\nxm16 100,108,132\nxm16 104,108,136\nxrt 90,14,1024\nxm16 108,108,140\nxam 62,0x21000\nxm16 112,108,144\n"
"xdl 0,16,s77,0\nv_add_u32 v15,s96,v15\nxrt 92,15,0\nxm16 116,108,148\nxm16 120,108,152\nxrt 94,15,1024\nxm16 124,108,156\nxm16 128,112,128\n"
"xrb 32,6,0\nxm16 132,112,132\nxm16 136,112,136\nxrb 36,6,2048\nxam 62,0x21400\nxm16 140,112,140\nxdl 1,16,s78,0\nxm16 144,112,144\nxrb 40,6,4096\n"
"xm16 148,112,148\nxm16 152,112,152\nxrb 44,6,6144\nxm16 156,112,156\nxm16 160,116,128\nxrb 48,6,8192\nxm16 164,116,132\nxam 62,0x21800\n"
"xm16 168,116,136\nxdl 0,16,s79,0\nxrb 52,6,10240\nxm16 172,116,140\nxm16 176,116,144\nxrb 56,6,12288\nxm16 180,116,148\nxm16 184,116,152\n"
"xrb 60,6,14336\nxm16 188,116,156\nxm16 192,120,128\nxam 62,0x21c00\nxm16 196,120,132\nxdl 1,16,s80,0\ns_add_u32 s16,s16,0x80\ns_addc_u32 s17,s17,0\n"
"s_sub_u32 s18,s18,0x80\nxm16 200,120,136\nxm16 204,120,140\nxm16 208,120,144\nxm16 212,120,148\nxm16 216,120,152\nxm16 220,120,156\nxm16 224,124,128\n"
"xm16 228,124,132\nxm16 232,124,136\nxm16 236,124,140\nxm16 240,124,144\nxm16 244,124,148\nxm16 248,124,152\nxm16 252,124,156\nxwl 7\nxm16 0,32,64\n"
"xrt 128,8,8192\nxm16 4,32,68\nxm16 8,32,72\nxrt 130,8,9216\nxm16 12,32,76\nxm16 16,32,80\nxrt 132,9,8192\nxm16 20,32,84\nxm16 24,32,88\n"
"xrt 134,9,9216\nxm16 28,32,92\nxwl 10\nxm16 32,36,64\nxrt 136,10,8192\nxm16 36,36,68\nxm16 40,36,72\nxrt 138,10,9216\nxm16 44,36,76\nxm16 48,36,80\n"
"xrt 140,11,8192\nxm16 52,36,84\nxm16 56,36,88\nxrt 142,11,9216\nxm16 60,36,92\nxwl 13\nxm16 64,40,64\nxrt 144,12,8192\nxm16 68,40,68\nxm16 72,40,72\n"
"xrt 146,12,9216\nxm16 76,40,76\nxm16 80,40,80\nxrt 148,13,8192\nxm16 84,40,84\nxm16 88,40,88\nxrt 150,13,9216\nxm16 92,40,92\nxwl 15\nxm16 96,44,64\n"
"xrt 152,14,8192\nxm16 100,44,68\nxm16 104,44,72\nxrt 154,14,9216\nxm16 108,44,76\nxm16 112,44,80\nxrt 156,15,8192\nxm16 116,44,84\nxm16 120,44,88\n"
"xrt 158,15,9216\nxm16 124,44,92\nxm16 128,48,64\nxrb 96,7,0\nxm16 132,48,68\nxm16 136,48,72\nxrb 100,7,2048\nxm16 140,48,76\nxm16 144,48,80\n"
"xrb 104,7,4096\nxm16 148,48,84\nxm16 152,48,88\nxrb 108,7,6144\nxm16 156,48,92\nxwl 15\nxm16 160,52,64\nxrb 112,7,8192\nxm16 164,52,68\n"
"xm16 168,52,72\nxrb 116,7,10240\nxm16 172,52,76\nxm16 176,52,80\nxrb 120,7,12288\nxm16 180,52,84\nxm16 184,52,88\nxrb 124,7,14336\nxm16 188,52,92\n"
"xm16 192,56,64\nxm16 196,56,68\nxm16 200,56,72\nxm16 204,56,76\nxm16 208,56,80\nxm16 212,56,84\nxm16 216,56,88\nxm16 220,56,92\nxm16 224,60,64\n"
"xm16 228,60,68\nxm16 232,60,72\nxm16 236,60,76\nxm16 240,60,80\nxm16 244,60,84\nxm16 248,60,88\nxm16 252,60,92\ns_waitcnt vmcnt(0) lgkmcnt(0)\n"
"s_barrier\ns_add_u32 s97,s97,0x8000\ns_mov_b32 s96,0x8000\ns_cmp_ge_u32 s97,0x18000\ns_cselect_b32 s96,0xffff0000,s96\ns_cselect_b32 s97,0,s97\n"
"xm16 0,96,128\nv_add_u32 v8,s96,v8\nxrt 64,8,0\nxm16 4,96,132\nxm16 8,96,136\nxrt 66,8,1024\nxm16 12,96,140\nxm16 16,96,144\nv_add_u32 v9,s96,v9\n"
"xrt 68,9,0\nxm16 20,96,148\nxm16 24,96,152\nxrt 70,9,1024\nxm16 28,96,156\nxm16 32,100,128\nv_add_u32 v10,s96,v10\nxrt 72,10,0\nxm16 36,100,132\n"
"xm16 40,100,136\nxrt 74,10,1024\nxm16 44,100,140\nxm16 48,100,144\nv_add_u32 v11,s96,v11\nxrt 76,11,0\nxm16 52,100,148\nxm16 56,100,152\n"
"xrt 78,11,1024\nxm16 60,100,156\nxm16 64,104,128\nv_add_u32 v12,s96,v12\nxrt 80,12,0\nxm16 68,104,132\nxm16 72,104,136\nxrt 82,12,1024\n"
"xm16 76,104,140\nxm16 80,104,144\nv_add_u32 v13,s96,v13\nxrt 84,13,0\nxm16 84,104,148\nxm16 88,104,152\nxrt 86,13,1024\nxm16 92,104,156\n"
"xm16 96,108,128\nv_add_u32 v14,s96,v14\nxrt 88,14,0\nxm16 100,108,132\nxm16 104,108,136\nxrt 90,14,1024\nxm16 108,108,140\nxm16 112,108,144\n"
"v_add_u32 v15,s96,v15\nxrt 92,15,0\nxm16 116,108,148\nxm16 120,108,152\nxrt 94,15,1024\nxm16 124,108,156\nxm16 128,112,128\nxrb 32,6,32768\n"
"xm16 132,112,132\nxm16 136,112,136\nxrb 36,6,34816\nxm16 140,112,140\nxm16 144,112,144\nxrb 40,6,36864\nxm16 148,112,148\nxm16 152,112,152\n"
"xrb 44,6,38912\nxm16 156,112,156\nxm16 160,116,128\nxrb 48,6,40960\nxm16 164,116,132\nxm16 168,116,136\nxrb 52,6,43008\nxm16 172,116,140\n"
"xm16 176,116,144\nxrb 56,6,45056\nxm16 180,116,148\nxm16 184,116,152\nxrb 60,6,47104\nxm16 188,116,156\nxm16 192,120,128\nxm16 196,120,132\n"
"xm16 200,120,136\nxm16 204,120,140\nxm16 208,120,144\nxm16 212,120,148\nxm16 216,120,152\nxm16 220,120,156\nxm16 224,124,128\nxm16 228,124,132\n"
"xm16 232,124,136\nxm16 236,124,140\nxm16 240,124,144\nxm16 244,124,148\nxm16 248,124,152\nxm16 252,124,156\nxwl 7\nxm16 0,32,64\nxrt 128,8,8192\n"
"xm16 4,32,68\nxm16 8,32,72\nxrt 130,8,9216\nxm16 12,32,76\nxm16 16,32,80\nxrt 132,9,8192\nxm16 20,32,84\nxm16 24,32,88\nxrt 134,9,9216\n"
"xm16 28,32,92\nxwl 10\nxm16 32,36,64\nxrt 136,10,8192\nxm16 36,36,68\nxm16 40,36,72\nxrt 138,10,9216\nxm16 44,36,76\nxm16 48,36,80\nxrt 140,11,8192\n"
"xm16 52,36,84\nxm16 56,36,88\nxrt 142,11,9216\nxm16 60,36,92\nxwl 13\nxm16 64,40,64\nxrt 144,12,8192\nxm16 68,40,68\nxm16 72,40,72\nxrt 146,12,9216\n"
"xm16 76,40,76\nxm16 80,40,80\nxrt 148,13,8192\nxm16 84,40,84\nxm16 88,40,88\nxrt 150,13,9216\nxm16 92,40,92\nxwl 15\nxm16 96,44,64\nxrt 152,14,8192\n"
"xm16 100,44,68\nxm16 104,44,72\nxrt 154,14,9216\nxm16 108,44,76\nxm16 112,44,80\nxrt 156,15,8192\nxm16 116,44,84\nxm16 120,44,88\nxrt 158,15,9216\n"
"xm16 124,44,92\nxm16 128,48,64\nxrb 96,7,32768\nxm16 132,48,68\nxm16 136,48,72\nxrb 100,7,34816\nxm16 140,48,76\nxm16 144,48,80\nxrb 104,7,36864\n"
"xm16 148,48,84\nxm16 152,48,88\nxrb 108,7,38912\nxm16 156,48,92\nxwl 15\nxm16 160,52,64\nxrb 112,7,40960\nxm16 164,52,68\nxm16 168,52,72\n"
"xrb 116,7,43008\nxm16 172,52,76\nxm16 176,52,80\nxrb 120,7,45056\nxm16 180,52,84\nxm16 184,52,88\nxrb 124,7,47104\nxm16 188,52,92\nxm16 192,56,64\n"
"xm16 196,56,68\nxm16 200,56,72\nxm16 204,56,76\nxm16 208,56,80\nxm16 212,56,84\nxm16 216,56,88\nxm16 220,56,92\nxm16 224,60,64\nxm16 228,60,68\n"
"xm16 232,60,72\nxm16 236,60,76\nxm16 240,60,80\nxm16 244,60,84\nxm16 248,60,88\nxm16 252,60,92\ns_cmp_eq_u32 s57,0\ns_cbranch_scc1 Lnosp_33_%=\n"
"s_mov_b32 s70,0x30d40\nLspin_34_%=:\ns_waitcnt vmcnt(0)\nv_cmp_eq_u64 vcc,s[6:7],v[166:167]\ns_nop 3\ns_and_b32 s69,vcc_lo,0xffff\n"
"s_cmp_eq_u32 s69,0xffff\ns_cbranch_scc1 Lspun_35_%=\ns_sleep 2\nbuffer_load_dwordx2 v[166:167],v168,s[44:47],s56 offen offset:0 sc1\n"
"s_sub_u32 s70,s70,1\ns_cmp_gt_i32 s70,0\ns_cbranch_scc1 Lspin_34_%=\ns_waitcnt vmcnt(0)\nLspun_35_%=:\ns_nop 1\nLnosp_33_%=:\ns_nop 1\nxwl 0\n"
"s_barrier\nxam 65,0xc000\nxm16 0,96,128\nxdl 160,28,0,0\nxam 65,0xc400\nxm16 4,96,132\nxdl 161,28,s85,0\nxm16 8,96,136\nxam 65,0xc800\n"
"xm16 12,96,140\nxdl 160,28,s86,0\nxm16 16,96,144\nxam 65,0xcc00\nxm16 20,96,148\nxdl 161,28,s87,0\nxm16 24,96,152\nxam 65,0x10000\nxm16 28,96,156\n"
"xdl 160,28,s88,0\nxam 65,0x10400\nxm16 32,100,128\nxdl 161,28,s89,0\nxm16 36,100,132\nxam 65,0x10800\nxm16 40,100,136\nxdl 160,28,s90,0\n"
"xm16 44,100,140\nxam 65,0x10c00\nxm16 48,100,144\nxdl 161,28,s91,0\ns_add_u32 s28,s28,0x100\ns_addc_u32 s29,s29,0\ns_sub_u32 s30,s30,0x100\n"
"xm16 52,100,148\nxam 66,0\nxm16 56,100,152\nxdl1 162,32,0,0\nxm16 60,100,156\nxam 66,0x400\nxm16 64,104,128\nxdl1 163,32,s92,0\nxam 66,0x2000\n"
"xm16 68,104,132\nxdl1 162,32,s93,0\nxm16 72,104,136\nxam 66,0x2400\nxm16 76,104,140\nxdl1 163,32,s94,0\ns_add_u32 s32,s32,0x38000\n"
"s_addc_u32 s33,s33,0\ns_sub_u32 s34,s34,0x38000\nxm16 80,104,144\nxam 65,0x14000\nxm16 84,104,148\nxdl 160,28,0,0\nxm16 88,104,152\nxam 65,0x14400\n"
"xm16 92,104,156\nxdl 161,28,s85,0\nxam 65,0x14800\nxm16 96,108,128\nxdl 160,28,s86,0\nxm16 100,108,132\nxam 65,0x14c00\nxm16 104,108,136\n"
"xdl 161,28,s87,0\nxm16 108,108,140\nxam 65,0x18000\nxm16 112,108,144\nxdl 160,28,s88,0\nxm16 116,108,148\nxam 65,0x18400\nxm16 120,108,152\n"
"xdl 161,28,s89,0\nxm16 124,108,156\nxam 65,0x18800\nxm16 128,112,128\nxdl 160,28,s90,0\nxam 65,0x18c00\nxm16 132,112,132\nxdl 161,28,s91,0\n"
"s_add_u32 s28,s28,0x100\ns_addc_u32 s29,s29,0\ns_sub_u32 s30,s30,0x100\nxm16 136,112,136\nxam 66,0x4000\nxm16 140,112,140\nxdl1 162,32,0,0\n"
"xm16 144,112,144\nxam 66,0x4400\nxm16 148,112,148\nxdl1 163,32,s92,0\nxm16 152,112,152\nxam 66,0x6000\nxm16 156,112,156\nxdl1 162,32,s93,0\n"
"xam 66,0x6400\nxm16 160,116,128\nxdl1 163,32,s94,0\ns_add_u32 s32,s32,0x38000\ns_addc_u32 s33,s33,0\ns_sub_u32 s34,s34,0x38000\nxm16 164,116,132\n"
"xam 65,0x1c000\nxm16 168,116,136\nxdl 160,28,0,0\nxm16 172,116,140\nxam 65,0x1c400\nxm16 176,116,144\nxdl 161,28,s85,0\nxm16 180,116,148\n"
"xam 65,0x1c800\nxm16 184,116,152\nxdl 160,28,s86,0\nxm16 188,116,156\nxam 65,0x1cc00\nxm16 192,120,128\nxdl 161,28,s87,0\nxam 65,0x20000\n"
"xm16 196,120,132\nxdl 160,28,s88,0\nxm16 200,120,136\nxam 65,0x20400\nxm16 204,120,140\nxdl 161,28,s89,0\nxm16 208,120,144\nxam 65,0x20800\n"
"xm16 212,120,148\nxdl 160,28,s90,0\nxm16 216,120,152\nxam 65,0x20c00\nxm16 220,120,156\nxdl 161,28,s91,0\ns_add_u32 s28,s28,0x100\n"
"s_addc_u32 s29,s29,0\ns_sub_u32 s30,s30,0x100\nxam 66,0x8000\nxm16 224,124,128\nxdl1 162,32,0,0\nxm16 228,124,132\nxam 66,0x8400\nxm16 232,124,136\n"
"xdl1 163,32,s92,0\nxm16 236,124,140\nxam 66,0xa000\nxm16 240,124,144\nxdl1 162,32,s93,0\nxm16 244,124,148\nxam 66,0xa400\nxm16 248,124,152\n"
"xdl1 163,32,s94,0\ns_add_u32 s32,s32,0x38000\ns_addc_u32 s33,s33,0\ns_sub_u32 s34,s34,0x38000\nxm16 252,124,156\ns_nop 7\nxar 32,0\nxar 33,1\n"
"xar 34,2\nxar 35,3\nxar 36,32\nxar 37,33\nxar 38,34\nxar 39,35\nxcv 128,32,33\nxcv 129,34,35\nxcv 130,36,37\nxcv 131,38,39\nxar 40,4\nxar 41,5\n"
"xar 42,6\nxar 43,7\nxar 44,36\nxar 45,37\nxar 46,38\nxar 47,39\nxcv 132,40,41\nxcv 133,42,43\nxcv 134,44,45\nxcv 135,46,47\nxar 48,8\nxar 49,9\n"
"xar 50,10\nxar 51,11\nxar 52,40\nxar 53,41\nxar 54,42\nxar 55,43\nxcv 136,48,49\nxcv 137,50,51\nxcv 138,52,53\nxcv 139,54,55\nxar 56,12\nxar 57,13\n"
"xar 58,14\nxar 59,15\nxar 60,44\nxar 61,45\nxar 62,46\nxar 63,47\nxcv 140,56,57\nxcv 141,58,59\nxcv 142,60,61\nxcv 143,62,63\nxar 32,16\nxar 33,17\n"
"xar 34,18\nxar 35,19\nxar 36,48\nxar 37,49\nxar 38,50\nxar 39,51\nxcv 144,32,33\nxcv 145,34,35\nxcv 146,36,37\nxcv 147,38,39\nxar 40,20\nxar 41,21\n"
"xar 42,22\nxar 43,23\nxar 44,52\nxar 45,53\nxar 46,54\nxar 47,55\nxcv 148,40,41\nxcv 149,42,43\nxcv 150,44,45\nxcv 151,46,47\nxar 48,24\nxar 49,25\n"
"xar 50,26\nxar 51,27\nxar 52,56\nxar 53,57\nxar 54,58\nxar 55,59\nxcv 152,48,49\nxcv 153,50,51\nxcv 154,52,53\nxcv 155,54,55\nxar 56,28\nxar 57,29\n"
"xar 58,30\nxar 59,31\nxar 60,60\nxar 61,61\nxar 62,62\nxar 63,63\nxcv 156,56,57\nxcv 157,58,59\nxcv 158,60,61\nxcv 159,62,63\nxar 32,64\nxar 33,65\n"
"xar 34,66\nxar 35,67\nxar 36,96\nxar 37,97\nxar 38,98\nxar 39,99\nxcv 160,32,33\nxcv 161,34,35\nxcv 162,36,37\nxcv 163,38,39\nxar 40,68\nxar 41,69\n"
"xar 42,70\nxar 43,71\nxar 44,100\nxar 45,101\nxar 46,102\nxar 47,103\nxcv 164,40,41\nxcv 165,42,43\nxcv 166,44,45\nxcv 167,46,47\nxar 48,72\n"
"xar 49,73\nxar 50,74\nxar 51,75\nxar 52,104\nxar 53,105\nxar 54,106\nxar 55,107\nxcv 168,48,49\nxcv 169,50,51\nxcv 170,52,53\nxcv 171,54,55\n"
"xar 56,76\nxar 57,77\nxar 58,78\nxar 59,79\nxar 60,108\nxar 61,109\nxar 62,110\nxar 63,111\nxcv 172,56,57\nxcv 173,58,59\nxcv 174,60,61\n"
"xcv 175,62,63\nxar 32,80\nxar 33,81\nxar 34,82\nxar 35,83\nxar 36,112\nxar 37,113\nxar 38,114\nxar 39,115\nxcv 176,32,33\nxcv 177,34,35\n"
"xcv 178,36,37\nxcv 179,38,39\nxar 40,84\nxar 41,85\nxar 42,86\nxar 43,87\nxar 44,116\nxar 45,117\nxar 46,118\nxar 47,119\nxcv 180,40,41\n"
"xcv 181,42,43\nxcv 182,44,45\nxcv 183,46,47\nxar 48,88\nxar 49,89\nxar 50,90\nxar 51,91\nxar 52,120\nxar 53,121\nxar 54,122\nxar 55,123\n"
"xcv 184,48,49\nxcv 185,50,51\nxcv 186,52,53\nxcv 187,54,55\nxar 56,92\nxar 57,93\nxar 58,94\nxar 59,95\nxar 60,124\nxar 61,125\nxar 62,126\n"
"xar 63,127\nxcv 188,56,57\nxcv 189,58,59\nxcv 190,60,61\nxcv 191,62,63\nxar 32,128\nxar 33,129\nxar 34,130\nxar 35,131\nxar 36,160\nxar 37,161\n"
"xar 38,162\nxar 39,163\nxcv 192,32,33\nxcv 193,34,35\nxcv 194,36,37\nxcv 195,38,39\nxar 40,132\nxar 41,133\nxar 42,134\nxar 43,135\nxar 44,164\n"
"xar 45,165\nxar 46,166\nxar 47,167\nxcv 196,40,41\nxcv 197,42,43\nxcv 198,44,45\nxcv 199,46,47\nxar 48,136\nxar 49,137\nxar 50,138\nxar 51,139\n"
"xar 52,168\nxar 53,169\nxar 54,170\nxar 55,171\nxcv 200,48,49\nxcv 201,50,51\nxcv 202,52,53\nxcv 203,54,55\nxar 56,140\nxar 57,141\nxar 58,142\n"
"xar 59,143\nxar 60,172\nxar 61,173\nxar 62,174\nxar 63,175\nxcv 204,56,57\nxcv 205,58,59\nxcv 206,60,61\nxcv 207,62,63\nxar 32,144\nxar 33,145\n"
"xar 34,146\nxar 35,147\nxar 36,176\nxar 37,177\nxar 38,178\nxar 39,179\nxcv 208,32,33\nxcv 209,34,35\nxcv 210,36,37\nxcv 211,38,39\nxar 40,148\n"
"xar 41,149\nxar 42,150\nxar 43,151\nxar 44,180\nxar 45,181\nxar 46,182\nxar 47,183\nxcv 212,40,41\nxcv 213,42,43\nxcv 214,44,45\nxcv 215,46,47\n"
"xar 48,152\nxar 49,153\nxar 50,154\nxar 51,155\nxar 52,184\nxar 53,185\nxar 54,186\nxar 55,187\nxcv 216,48,49\nxcv 217,50,51\nxcv 218,52,53\n"
"xcv 219,54,55\nxar 56,156\nxar 57,157\nxar 58,158\nxar 59,159\nxar 60,188\nxar 61,189\nxar 62,190\nxar 63,191\nxcv 220,56,57\nxcv 221,58,59\n"
"xcv 222,60,61\nxcv 223,62,63\nxar 32,192\nxar 33,193\nxar 34,194\nxar 35,195\nxar 36,224\nxar 37,225\nxar 38,226\nxar 39,227\nxcv 224,32,33\n"
"xcv 225,34,35\nxcv 226,36,37\nxcv 227,38,39\nxar 40,196\nxar 41,197\nxar 42,198\nxar 43,199\nxar 44,228\nxar 45,229\nxar 46,230\nxar 47,231\n"
"xcv 228,40,41\nxcv 229,42,43\nxcv 230,44,45\nxcv 231,46,47\nxar 48,200\nxar 49,201\nxar 50,202\nxar 51,203\nxar 52,232\nxar 53,233\nxar 54,234\n"
"xar 55,235\nxcv 232,48,49\nxcv 233,50,51\nxcv 234,52,53\nxcv 235,54,55\nxar 56,204\nxar 57,205\nxar 58,206\nxar 59,207\nxar 60,236\nxar 61,237\n"
"xar 62,238\nxar 63,239\nxcv 236,56,57\nxcv 237,58,59\nxcv 238,60,61\nxcv 239,62,63\nxar 32,208\nxar 33,209\nxar 34,210\nxar 35,211\nxar 36,240\n"
"xar 37,241\nxar 38,242\nxar 39,243\nxcv 240,32,33\nxcv 241,34,35\nxcv 242,36,37\nxcv 243,38,39\nxar 40,212\nxar 41,213\nxar 42,214\nxar 43,215\n"
"xar 44,244\nxar 45,245\nxar 46,246\nxar 47,247\nxcv 244,40,41\nxcv 245,42,43\nxcv 246,44,45\nxcv 247,46,47\nxar 48,216\nxar 49,217\nxar 50,218\n"
"xar 51,219\nxar 52,248\nxar 53,249\nxar 54,250\nxar 55,251\nxcv 248,48,49\nxcv 249,50,51\nxcv 250,52,53\nxcv 251,54,55\nxar 56,220\nxar 57,221\n"
"xar 58,222\nxar 59,223\nxar 60,252\nxar 61,253\nxar 62,254\nxar 63,255\nxcv 252,56,57\nxcv 253,58,59\nxcv 254,60,61\nxcv 255,62,63\n"
"s_branch Lblkend_36_%=\nLlnhook_22_%=:\ns_nop 4\ns_cmp_eq_u32 s54,0\ns_cbranch_scc1 Lnoln_26_%=\ns_mov_b32 s70,0x30d40\nv_mbcnt_lo_u32_b32 v168,-1,0\n"
"v_mbcnt_hi_u32_b32 v168,-1,v168\nv_lshlrev_b32 v168,3,v168\nbuffer_load_dwordx2 v[166:167],v168,s[44:47],s52 offen offset:56 sc1\nLspin_27_%=:\n"
"s_waitcnt vmcnt(0)\nv_cmp_eq_u64 vcc,s[6:7],v[166:167]\ns_nop 3\ns_and_b32 s69,vcc_lo,0x7f\ns_cmp_eq_u32 s69,0x7f\ns_cbranch_scc1 Lspun_28_%=\n"
"s_sleep 2\nbuffer_load_dwordx2 v[166:167],v168,s[44:47],s52 offen offset:56 sc1\ns_sub_u32 s70,s70,1\ns_cmp_gt_i32 s70,0\ns_cbranch_scc1 Lspin_27_%=\n"
"s_waitcnt vmcnt(0)\nLspun_28_%=:\ns_nop 1\ns_mov_b32 s68,0\nbuffer_load_dwordx2 v[224:225],v236,s[48:51],s68 offen offset:0 sc1\n"
"buffer_load_dwordx2 v[226:227],v236,s[48:51],s68 offen offset:32 sc1\nbuffer_load_dwordx2 v[228:229],v236,s[48:51],s68 offen offset:64 sc1\n"
"buffer_load_dwordx2 v[230:231],v236,s[48:51],s68 offen offset:96 sc1\ns_waitcnt vmcnt(0)\nv_cmp_eq_f32 vcc,0,v238\ns_nop 1\n"
"v_cndmask_b32 v230,v230,v170,vcc\nv_cndmask_b32 v231,v231,v170,vcc\nxad 172,v224,226\nxad 172,v172,228\nxad 172,v172,230\nv_mov_b32 v174,v172\n"
"s_nop 1\nv_permlane16_swap_b32 v172,v174\nxad 172,v172,174\nv_mov_b32 v174,v172\ns_nop 1\nv_permlane32_swap_b32 v172,v174\nxad 172,v172,174\n"
"xmu 172,0x3d924925,172\nv_sub_f32 v224,v224,v172\nv_sub_f32 v226,v226,v172\nv_sub_f32 v228,v228,v172\nv_sub_f32 v230,v230,v172\nxmu 230,v230,238\n"
"xmu 173,v224,224\nxfm 173,v226,v226,v173\nxfm 173,v228,v228,v173\nxfm 173,v230,v230,v173\nxmu 173,0x42800000,173\nxad 173,v173,225\nxad 173,v173,227\n"
"xad 173,v173,229\nxad 173,v173,231\nv_mov_b32 v174,v173\ns_nop 1\nv_permlane16_swap_b32 v173,v174\nxad 173,v173,174\nv_mov_b32 v174,v173\ns_nop 1\n"
"v_permlane32_swap_b32 v173,v174\nxad 173,v173,174\nv_mov_b32 v175,0x3727c5ac\nxfm 173,v173,v239,v175\nv_rsq_f32 v173,v173\ns_nop 0\nxmu 174,v172,173\n"
"v_lshlrev_b32 v176,16,v192\nv_and_b32 v177,0xffff0000,v192\nv_lshlrev_b32 v178,16,v193\nv_and_b32 v179,0xffff0000,v193\nv_lshlrev_b32 v180,16,v194\n"
"v_and_b32 v181,0xffff0000,v194\nv_lshlrev_b32 v182,16,v195\nv_and_b32 v183,0xffff0000,v195\nxfm 176,v176,v173,-v174\nxfm 177,v177,v173,-v174\n"
"xfm 178,v178,v173,-v174\nxfm 179,v179,v173,-v174\nxfm 180,v180,v173,-v174\nxfm 181,v181,v173,-v174\nxfm 182,v182,v173,-v174\nxfm 183,v183,v173,-v174\n"
"xcv 184,176,177\nxcv 185,178,179\nxcv 186,180,181\nxcv 187,182,183\ns_mov_b32 s68,0\n"
"buffer_store_dwordx2 v[184:185],v237,s[40:43],s68 offen offset:0 sc1\nbuffer_store_dwordx2 v[186:187],v237,s[40:43],s68 offen offset:32 sc1\ns_nop 1\n"
"v_lshlrev_b32 v176,16,v196\nv_and_b32 v177,0xffff0000,v196\nv_lshlrev_b32 v178,16,v197\nv_and_b32 v179,0xffff0000,v197\nv_lshlrev_b32 v180,16,v198\n"
"v_and_b32 v181,0xffff0000,v198\nv_lshlrev_b32 v182,16,v199\nv_and_b32 v183,0xffff0000,v199\nxfm 176,v176,v173,-v174\nxfm 177,v177,v173,-v174\n"
"xfm 178,v178,v173,-v174\nxfm 179,v179,v173,-v174\nxfm 180,v180,v173,-v174\nxfm 181,v181,v173,-v174\nxfm 182,v182,v173,-v174\nxfm 183,v183,v173,-v174\n"
"xcv 184,176,177\nxcv 185,178,179\nxcv 186,180,181\nxcv 187,182,183\ns_mov_b32 s68,0\n"
"buffer_store_dwordx2 v[184:185],v237,s[40:43],s68 offen offset:64 sc1\nbuffer_store_dwordx2 v[186:187],v237,s[40:43],s68 offen offset:96 sc1\n"
"s_nop 1\ns_mov_b32 s68,0x800\nbuffer_load_dwordx2 v[224:225],v236,s[48:51],s68 offen offset:0 sc1\n"
"buffer_load_dwordx2 v[226:227],v236,s[48:51],s68 offen offset:32 sc1\nbuffer_load_dwordx2 v[228:229],v236,s[48:51],s68 offen offset:64 sc1\n"
"buffer_load_dwordx2 v[230:231],v236,s[48:51],s68 offen offset:96 sc1\ns_waitcnt vmcnt(0)\nv_cmp_eq_f32 vcc,0,v238\ns_nop 1\n"
"v_cndmask_b32 v230,v230,v170,vcc\nv_cndmask_b32 v231,v231,v170,vcc\nxad 172,v224,226\nxad 172,v172,228\nxad 172,v172,230\nv_mov_b32 v174,v172\n"
"s_nop 1\nv_permlane16_swap_b32 v172,v174\nxad 172,v172,174\nv_mov_b32 v174,v172\ns_nop 1\nv_permlane32_swap_b32 v172,v174\nxad 172,v172,174\n"
"xmu 172,0x3d924925,172\nv_sub_f32 v224,v224,v172\nv_sub_f32 v226,v226,v172\nv_sub_f32 v228,v228,v172\nv_sub_f32 v230,v230,v172\nxmu 230,v230,238\n"
"xmu 173,v224,224\nxfm 173,v226,v226,v173\nxfm 173,v228,v228,v173\nxfm 173,v230,v230,v173\nxmu 173,0x42800000,173\nxad 173,v173,225\nxad 173,v173,227\n"
"xad 173,v173,229\nxad 173,v173,231\nv_mov_b32 v174,v173\ns_nop 1\nv_permlane16_swap_b32 v173,v174\nxad 173,v173,174\nv_mov_b32 v174,v173\ns_nop 1\n"
"v_permlane32_swap_b32 v173,v174\nxad 173,v173,174\nv_mov_b32 v175,0x3727c5ac\nxfm 173,v173,v239,v175\nv_rsq_f32 v173,v173\ns_nop 0\nxmu 174,v172,173\n"
"v_lshlrev_b32 v176,16,v200\nv_and_b32 v177,0xffff0000,v200\nv_lshlrev_b32 v178,16,v201\nv_and_b32 v179,0xffff0000,v201\nv_lshlrev_b32 v180,16,v202\n"
"v_and_b32 v181,0xffff0000,v202\nv_lshlrev_b32 v182,16,v203\nv_and_b32 v183,0xffff0000,v203\nxfm 176,v176,v173,-v174\nxfm 177,v177,v173,-v174\n"
"xfm 178,v178,v173,-v174\nxfm 179,v179,v173,-v174\nxfm 180,v180,v173,-v174\nxfm 181,v181,v173,-v174\nxfm 182,v182,v173,-v174\nxfm 183,v183,v173,-v174\n"
"xcv 184,176,177\nxcv 185,178,179\nxcv 186,180,181\nxcv 187,182,183\ns_mov_b32 s68,0x7000\n"
"buffer_store_dwordx2 v[184:185],v237,s[40:43],s68 offen offset:0 sc1\nbuffer_store_dwordx2 v[186:187],v237,s[40:43],s68 offen offset:32 sc1\ns_nop 1\n"
"v_lshlrev_b32 v176,16,v204\nv_and_b32 v177,0xffff0000,v204\nv_lshlrev_b32 v178,16,v205\nv_and_b32 v179,0xffff0000,v205\nv_lshlrev_b32 v180,16,v206\n"
"v_and_b32 v181,0xffff0000,v206\nv_lshlrev_b32 v182,16,v207\nv_and_b32 v183,0xffff0000,v207\nxfm 176,v176,v173,-v174\nxfm 177,v177,v173,-v174\n"
"xfm 178,v178,v173,-v174\nxfm 179,v179,v173,-v174\nxfm 180,v180,v173,-v174\nxfm 181,v181,v173,-v174\nxfm 182,v182,v173,-v174\nxfm 183,v183,v173,-v174\n"
"xcv 184,176,177\nxcv 185,178,179\nxcv 186,180,181\nxcv 187,182,183\ns_mov_b32 s68,0x7000\n"
"buffer_store_dwordx2 v[184:185],v237,s[40:43],s68 offen offset:64 sc1\nbuffer_store_dwordx2 v[186:187],v237,s[40:43],s68 offen offset:96 sc1\n"
"s_nop 1\ns_mov_b32 s68,0x1000\nbuffer_load_dwordx2 v[224:225],v236,s[48:51],s68 offen offset:0 sc1\n"
"buffer_load_dwordx2 v[226:227],v236,s[48:51],s68 offen offset:32 sc1\nbuffer_load_dwordx2 v[228:229],v236,s[48:51],s68 offen offset:64 sc1\n"
"buffer_load_dwordx2 v[230:231],v236,s[48:51],s68 offen offset:96 sc1\ns_waitcnt vmcnt(0)\nv_cmp_eq_f32 vcc,0,v238\ns_nop 1\n"
"v_cndmask_b32 v230,v230,v170,vcc\nv_cndmask_b32 v231,v231,v170,vcc\nxad 172,v224,226\nxad 172,v172,228\nxad 172,v172,230\nv_mov_b32 v174,v172\n"
"s_nop 1\nv_permlane16_swap_b32 v172,v174\nxad 172,v172,174\nv_mov_b32 v174,v172\ns_nop 1\nv_permlane32_swap_b32 v172,v174\nxad 172,v172,174\n"
"xmu 172,0x3d924925,172\nv_sub_f32 v224,v224,v172\nv_sub_f32 v226,v226,v172\nv_sub_f32 v228,v228,v172\nv_sub_f32 v230,v230,v172\nxmu 230,v230,238\n"
"xmu 173,v224,224\nxfm 173,v226,v226,v173\nxfm 173,v228,v228,v173\nxfm 173,v230,v230,v173\nxmu 173,0x42800000,173\nxad 173,v173,225\nxad 173,v173,227\n"
"xad 173,v173,229\nxad 173,v173,231\nv_mov_b32 v174,v173\ns_nop 1\nv_permlane16_swap_b32 v173,v174\nxad 173,v173,174\nv_mov_b32 v174,v173\ns_nop 1\n"
"v_permlane32_swap_b32 v173,v174\nxad 173,v173,174\nv_mov_b32 v175,0x3727c5ac\nxfm 173,v173,v239,v175\nv_rsq_f32 v173,v173\ns_nop 0\nxmu 174,v172,173\n"
"v_lshlrev_b32 v176,16,v208\nv_and_b32 v177,0xffff0000,v208\nv_lshlrev_b32 v178,16,v209\nv_and_b32 v179,0xffff0000,v209\nv_lshlrev_b32 v180,16,v210\n"
"v_and_b32 v181,0xffff0000,v210\nv_lshlrev_b32 v182,16,v211\nv_and_b32 v183,0xffff0000,v211\nxfm 176,v176,v173,-v174\nxfm 177,v177,v173,-v174\n"
"xfm 178,v178,v173,-v174\nxfm 179,v179,v173,-v174\nxfm 180,v180,v173,-v174\nxfm 181,v181,v173,-v174\nxfm 182,v182,v173,-v174\nxfm 183,v183,v173,-v174\n"
"xcv 184,176,177\nxcv 185,178,179\nxcv 186,180,181\nxcv 187,182,183\ns_mov_b32 s68,0xe000\n"
"buffer_store_dwordx2 v[184:185],v237,s[40:43],s68 offen offset:0 sc1\nbuffer_store_dwordx2 v[186:187],v237,s[40:43],s68 offen offset:32 sc1\ns_nop 1\n"
"v_lshlrev_b32 v176,16,v212\nv_and_b32 v177,0xffff0000,v212\nv_lshlrev_b32 v178,16,v213\nv_and_b32 v179,0xffff0000,v213\nv_lshlrev_b32 v180,16,v214\n"
"v_and_b32 v181,0xffff0000,v214\nv_lshlrev_b32 v182,16,v215\nv_and_b32 v183,0xffff0000,v215\nxfm 176,v176,v173,-v174\nxfm 177,v177,v173,-v174\n"
"xfm 178,v178,v173,-v174\nxfm 179,v179,v173,-v174\nxfm 180,v180,v173,-v174\nxfm 181,v181,v173,-v174\nxfm 182,v182,v173,-v174\nxfm 183,v183,v173,-v174\n"
"xcv 184,176,177\nxcv 185,178,179\nxcv 186,180,181\nxcv 187,182,183\ns_mov_b32 s68,0xe000\n"
"buffer_store_dwordx2 v[184:185],v237,s[40:43],s68 offen offset:64 sc1\nbuffer_store_dwordx2 v[186:187],v237,s[40:43],s68 offen offset:96 sc1\n"
"s_nop 1\ns_mov_b32 s68,0x1800\nbuffer_load_dwordx2 v[224:225],v236,s[48:51],s68 offen offset:0 sc1\n"
"buffer_load_dwordx2 v[226:227],v236,s[48:51],s68 offen offset:32 sc1\nbuffer_load_dwordx2 v[228:229],v236,s[48:51],s68 offen offset:64 sc1\n"
"buffer_load_dwordx2 v[230:231],v236,s[48:51],s68 offen offset:96 sc1\ns_waitcnt vmcnt(0)\nv_cmp_eq_f32 vcc,0,v238\ns_nop 1\n"
"v_cndmask_b32 v230,v230,v170,vcc\nv_cndmask_b32 v231,v231,v170,vcc\nxad 172,v224,226\nxad 172,v172,228\nxad 172,v172,230\nv_mov_b32 v174,v172\n"
"s_nop 1\nv_permlane16_swap_b32 v172,v174\nxad 172,v172,174\nv_mov_b32 v174,v172\ns_nop 1\nv_permlane32_swap_b32 v172,v174\nxad 172,v172,174\n"
"xmu 172,0x3d924925,172\nv_sub_f32 v224,v224,v172\nv_sub_f32 v226,v226,v172\nv_sub_f32 v228,v228,v172\nv_sub_f32 v230,v230,v172\nxmu 230,v230,238\n"
"xmu 173,v224,224\nxfm 173,v226,v226,v173\nxfm 173,v228,v228,v173\nxfm 173,v230,v230,v173\nxmu 173,0x42800000,173\nxad 173,v173,225\nxad 173,v173,227\n"
"xad 173,v173,229\nxad 173,v173,231\nv_mov_b32 v174,v173\ns_nop 1\nv_permlane16_swap_b32 v173,v174\nxad 173,v173,174\nv_mov_b32 v174,v173\ns_nop 1\n"
"v_permlane32_swap_b32 v173,v174\nxad 173,v173,174\nv_mov_b32 v175,0x3727c5ac\nxfm 173,v173,v239,v175\nv_rsq_f32 v173,v173\ns_nop 0\nxmu 174,v172,173\n"
"v_lshlrev_b32 v176,16,v216\nv_and_b32 v177,0xffff0000,v216\nv_lshlrev_b32 v178,16,v217\nv_and_b32 v179,0xffff0000,v217\nv_lshlrev_b32 v180,16,v218\n"
"v_and_b32 v181,0xffff0000,v218\nv_lshlrev_b32 v182,16,v219\nv_and_b32 v183,0xffff0000,v219\nxfm 176,v176,v173,-v174\nxfm 177,v177,v173,-v174\n"
"xfm 178,v178,v173,-v174\nxfm 179,v179,v173,-v174\nxfm 180,v180,v173,-v174\nxfm 181,v181,v173,-v174\nxfm 182,v182,v173,-v174\nxfm 183,v183,v173,-v174\n"
"xcv 184,176,177\nxcv 185,178,179\nxcv 186,180,181\nxcv 187,182,183\ns_mov_b32 s68,0x15000\n"
"buffer_store_dwordx2 v[184:185],v237,s[40:43],s68 offen offset:0 sc1\nbuffer_store_dwordx2 v[186:187],v237,s[40:43],s68 offen offset:32 sc1\ns_nop 1\n"
"v_lshlrev_b32 v176,16,v220\nv_and_b32 v177,0xffff0000,v220\nv_lshlrev_b32 v178,16,v221\nv_and_b32 v179,0xffff0000,v221\nv_lshlrev_b32 v180,16,v222\n"
"v_and_b32 v181,0xffff0000,v222\nv_lshlrev_b32 v182,16,v223\nv_and_b32 v183,0xffff0000,v223\nxfm 176,v176,v173,-v174\nxfm 177,v177,v173,-v174\n"
"xfm 178,v178,v173,-v174\nxfm 179,v179,v173,-v174\nxfm 180,v180,v173,-v174\nxfm 181,v181,v173,-v174\nxfm 182,v182,v173,-v174\nxfm 183,v183,v173,-v174\n"
"xcv 184,176,177\nxcv 185,178,179\nxcv 186,180,181\nxcv 187,182,183\ns_mov_b32 s68,0x15000\n"
"buffer_store_dwordx2 v[184:185],v237,s[40:43],s68 offen offset:64 sc1\nbuffer_store_dwordx2 v[186:187],v237,s[40:43],s68 offen offset:96 sc1\n"
"s_nop 1\ns_waitcnt vmcnt(0)\nLnoln_26_%=:\ns_nop 3\ns_branch Llnret_23_%=\nLznhook_24_%=:\ns_nop 1\ns_cmp_eq_u32 s54,0\ns_cbranch_scc1 Lnozn_29_%=\n"
"s_cmp_eq_u32 s3,0\ns_cbranch_scc0 Lnosig_30_%=\nv_mov_b32 v166,s6\nv_mov_b32 v167,s7\nv_mov_b32 v169,0\ns_mov_b64 exec,1\n"
"buffer_store_dwordx2 v[166:167],v169,s[44:47],s55 offen offset:0 sc1\ns_mov_b64 exec,-1\nLnosig_30_%=:\ns_nop 1\nLnozn_29_%=:\ns_nop 1\n"
"s_branch Lznret_25_%=\nLblkend_36_%=:\nLsp_6_%=:\ns_lshr_b32 s98,s100,29\ns_and_b32 s98,s98,1\ns_cmp_eq_u32 s98,0\ns_cbranch_scc1 Lnosp_37_%=\n"
"s_nop 4\nv_mbcnt_lo_u32_b32 v48,-1,0\nv_mbcnt_hi_u32_b32 v48,-1,v48\ns_mov_b32 s16,s28\ns_mov_b32 s20,s32\ns_mov_b32 s17,s29\ns_mov_b32 s21,s33\n"
"s_mov_b32 s18,s30\ns_mov_b32 s22,s34\ns_mov_b32 s19,s31\ns_mov_b32 s23,s35\ns_load_dwordx2 s[76:77],s[0:1],40\ns_load_dwordx2 s[78:79],s[0:1],72\n"
"s_load_dwordx2 s[80:81],s[0:1],64\nxwl 0\ns_mul_i32 s87,s2,0x3800\ns_add_u32 s88,s76,s87\ns_addc_u32 s89,s77,0\ns_and_b32 s89,s89,0xffff\n"
"s_mul_i32 s87,s2,0xc40\ns_add_u32 s90,s78,s87\ns_addc_u32 s91,s79,0\ns_and_b32 s91,s91,0xffff\ns_lshr_b32 s87,s100,17\ns_and_b32 s87,s87,15\n"
"s_lshl_b32 s87,s87,8\ns_add_u32 s92,s80,s87\ns_addc_u32 s93,s81,0\ns_and_b32 s93,s93,0xffff\ns_mov_b32 s94,0x100\ns_mov_b32 s95,0x20000\n"
"v_lshrrev_b32 v0,3,v48\ns_lshl_b32 s87,s3,5\nv_add_u32 v0,s87,v0\nv_lshlrev_b32 v0,12,v0\nv_and_b32 v47,7,v48\nv_lshrrev_b32 v46,3,v48\n"
"v_lshrrev_b32 v46,1,v46\nv_xor_b32 v47,v47,v46\nv_lshlrev_b32 v47,4,v47\nv_add_u32 v0,v0,v47\nv_xor_b32 v1,64,v0\nv_lshrrev_b32 v2,3,v48\n"
"s_lshl_b32 s87,s3,4\nv_add_u32 v2,s87,v2\nv_mul_u32_u24 v2,0x700,v2\nv_and_b32 v47,7,v48\nv_lshrrev_b32 v47,1,v47\nv_lshrrev_b32 v46,3,v48\n"
"v_lshrrev_b32 v46,1,v46\nv_and_b32 v46,1,v46\nv_xor_b32 v47,v47,v46\nv_lshlrev_b32 v47,1,v47\nv_and_b32 v46,7,v48\nv_and_b32 v46,1,v46\n"
"v_or_b32 v47,v47,v46\nv_lshlrev_b32 v47,4,v47\nv_add_u32 v2,v2,v47\nv_xor_b32 v3,64,v2\nv_and_b32 v6,15,v48\ns_lshr_b32 s87,s3,1\n"
"s_lshl_b32 s87,s87,6\nv_add_u32 v6,s87,v6\nv_lshlrev_b32 v6,7,v6\nv_lshrrev_b32 v47,4,v48\nv_and_b32 v46,15,v48\nv_lshrrev_b32 v46,1,v46\n"
"v_xor_b32 v47,v47,v46\nv_lshlrev_b32 v47,4,v47\nv_add_u32 v6,v6,v47\nv_add_u32 v6,0xc000,v6\nv_xor_b32 v7,64,v6\nv_lshrrev_b32 v8,4,v48\n"
"v_lshlrev_b32 v8,3,v8\nv_and_b32 v47,15,v48\nv_lshrrev_b32 v47,2,v47\nv_add_u32 v8,v8,v47\nv_lshlrev_b32 v8,7,v8\nv_and_b32 v47,15,v48\n"
"v_lshrrev_b32 v47,2,v47\nv_lshrrev_b32 v47,1,v47\nv_lshrrev_b32 v46,4,v48\nv_and_b32 v46,1,v46\nv_lshlrev_b32 v46,1,v46\nv_or_b32 v47,v47,v46\n"
"s_and_b32 s87,s3,1\ns_lshl_b32 s87,s87,1\nv_xor_b32 v47,s87,v47\nv_lshlrev_b32 v47,5,v47\nv_add_u32 v8,v8,v47\nv_and_b32 v47,3,v48\n"
"v_lshlrev_b32 v47,3,v47\nv_add_u32 v8,v8,v47\nv_lshrrev_b32 v9,4,v48\nv_lshlrev_b32 v9,3,v9\nv_and_b32 v47,15,v48\nv_lshrrev_b32 v47,2,v47\n"
"v_add_u32 v9,v9,v47\nv_lshlrev_b32 v9,7,v9\nv_and_b32 v47,15,v48\nv_lshrrev_b32 v47,2,v47\nv_lshrrev_b32 v47,1,v47\nv_lshrrev_b32 v46,4,v48\n"
"v_and_b32 v46,1,v46\nv_lshlrev_b32 v46,1,v46\nv_or_b32 v47,v47,v46\ns_and_b32 s87,s3,1\ns_lshl_b32 s87,s87,1\ns_add_u32 s87,s87,1\n"
"v_xor_b32 v47,s87,v47\nv_lshlrev_b32 v47,5,v47\nv_add_u32 v9,v9,v47\nv_and_b32 v47,3,v48\nv_lshlrev_b32 v47,3,v47\nv_add_u32 v9,v9,v47\n"
"v_lshlrev_b32 v18,4,v48\ns_lshr_b32 s87,s3,1\ns_mul_i32 s87,s87,0x1c000\ns_and_b32 s86,s3,1\ns_lshl_b32 s86,s86,10\ns_add_u32 s87,s87,s86\n"
"v_add_u32 v18,s87,v18\nv_lshlrev_b32 v19,4,v48\ns_lshr_b32 s87,s3,1\ns_mul_i32 s87,s87,0x9c000\ns_and_b32 s86,s3,1\ns_lshl_b32 s86,s86,10\n"
"s_add_u32 s87,s87,s86\nv_add_u32 v19,s87,v19\nv_and_b32 v20,15,v48\ns_lshr_b32 s87,s3,1\ns_lshl_b32 s87,s87,6\nv_add_u32 v20,s87,v20\n"
"v_lshlrev_b32 v20,2,v20\nv_and_b32 v21,15,v48\ns_lshr_b32 s87,s3,1\ns_lshl_b32 s87,s87,6\nv_add_u32 v21,s87,v21\nv_lshlrev_b32 v21,1,v21\n"
"v_lshlrev_b32 v22,6,v48\ns_lshl_b32 s87,s3,12\nv_add_u32 v22,s87,v22\ns_lshr_b32 s86,s100,16\ns_and_b32 s86,s86,1\ns_cmp_lg_u32 s86,0\n"
"s_cselect_b32 s87,0x1000000,0\ns_lshr_b32 s86,s100,12\ns_and_b32 s86,s86,15\ns_lshl_b32 s86,s86,20\ns_add_u32 s87,s87,s86\ns_lshr_b32 s86,s100,8\n"
"s_and_b32 s86,s86,15\ns_lshl_b32 s86,s86,17\ns_add_u32 s87,s87,s86\ns_add_u32 s40,s4,s87\ns_addc_u32 s41,s5,0\ns_and_b32 s41,s41,0xffff\n"
"s_lshr_b32 s42,s100,8\ns_and_b32 s42,s42,15\ns_lshl_b32 s42,s42,17\ns_sub_u32 s42,0x100000,s42\ns_mov_b32 s43,0x20000\ns_lshr_b32 s87,s100,16\n"
"s_and_b32 s87,s87,1\ns_mov_b32 s86,0x1000\ns_cmp_lg_u32 s87,0\ns_cselect_b32 s74,s86,0x2000\ns_lshr_b32 s86,s100,16\ns_and_b32 s86,s86,1\n"
"s_mov_b32 s87,0x2000\ns_cmp_lg_u32 s86,0\ns_cselect_b32 s75,s87,0x400\nv_lshrrev_b32 v30,4,v48\nv_lshrrev_b32 v30,1,v30\nv_mul_u32_u24 v30,s75,v30\n"
"s_lshr_b32 s87,s3,1\ns_lshl_b32 s87,s87,16\ns_and_b32 s86,s3,1\ns_mul_i32 s86,s86,s74\ns_add_u32 s87,s87,s86\nv_add_u32 v30,s87,v30\n"
"v_lshrrev_b32 v47,4,v48\nv_and_b32 v47,1,v47\nv_lshlrev_b32 v47,9,v47\nv_add_u32 v30,v30,v47\nv_and_b32 v47,15,v48\nv_lshlrev_b32 v47,4,v47\n"
"v_add_u32 v30,v30,v47\ns_add_u32 s44,s4,0x6c00000\ns_addc_u32 s45,s5,0\ns_and_b32 s45,s45,0xffff\ns_mov_b32 s46,0x8000\ns_mov_b32 s47,0x20000\n"
"s_lshr_b32 s52,s100,12\ns_and_b32 s52,s52,15\ns_lshl_b32 s52,s52,4\ns_lshr_b32 s87,s100,16\ns_and_b32 s87,s87,1\ns_lshl_b32 s87,s87,3\n"
"s_add_u32 s52,s52,s87\ns_lshr_b32 s87,s100,8\ns_and_b32 s87,s87,15\ns_add_u32 s52,s52,s87\ns_lshl_b32 s52,s52,3\ns_add_u32 s52,0x3000,s52\n"
"s_lshr_b32 s87,s100,16\ns_and_b32 s87,s87,1\ns_mov_b32 s86,0x400\ns_cmp_lg_u32 s87,0\ns_cselect_b32 s54,s86,0x800\ns_lshr_b32 s86,s100,17\n"
"s_and_b32 s86,s86,15\ns_lshl_b32 s86,s86,9\ns_add_u32 s86,0x5400000,s86\ns_add_u32 s48,s4,s86\ns_addc_u32 s49,s5,0\ns_and_b32 s49,s49,0xffff\n"
"s_mov_b32 s50,0x200\ns_mov_b32 s51,0x20000\ns_lshr_b32 s86,s100,17\ns_and_b32 s86,s86,15\ns_mul_i32 s86,s86,0x38000\ns_add_u32 s86,0x4800000,s86\n"
"s_lshr_b32 s87,s100,21\ns_and_b32 s87,s87,15\ns_lshl_b32 s87,s87,11\ns_add_u32 s86,s86,s87\ns_add_u32 s28,s4,s86\ns_addc_u32 s29,s5,0\n"
"s_and_b32 s29,s29,0xffff\ns_lshr_b32 s30,s100,21\ns_and_b32 s30,s30,15\ns_lshl_b32 s30,s30,11\ns_sub_u32 s30,0x38000,s30\ns_mov_b32 s31,0x20000\n"
"s_lshr_b32 s86,s100,17\ns_and_b32 s86,s86,15\ns_mul_i32 s86,s86,0x138000\ns_add_u32 s86,0x3000000,s86\ns_lshr_b32 s87,s100,21\ns_and_b32 s87,s87,15\n"
"s_lshl_b32 s87,s87,1\ns_add_u32 s87,s87,0x80\ns_lshl_b32 s87,s87,10\ns_add_u32 s86,s86,s87\ns_add_u32 s32,s4,s86\ns_addc_u32 s33,s5,0\n"
"s_and_b32 s33,s33,0xffff\ns_lshr_b32 s34,s100,21\ns_and_b32 s34,s34,15\ns_lshl_b32 s34,s34,1\ns_add_u32 s34,s34,0x80\ns_lshl_b32 s34,s34,10\n"
"s_sub_u32 s34,0x138000,s34\ns_mov_b32 s35,0x20000\ns_lshr_b32 s53,s100,17\ns_and_b32 s53,s53,15\ns_lshl_b32 s53,s53,7\ns_add_u32 s53,0x1000,s53\n"
"s_mov_b32 s74,0x8000\ns_mov_b32 s75,0x10000\ns_mov_b32 s76,0x18000\ns_mov_b32 s77,0x80\ns_mov_b32 s78,0x8080\ns_mov_b32 s79,0x10080\n"
"s_mov_b32 s80,0x18080\ns_mul_i32 s62,s3,0x1000\ns_mov_b32 s81,0x3800\ns_mov_b32 s82,0x1c000\ns_mov_b32 s83,0x1f800\ns_mul_i32 s63,s3,0x800\n"
"s_mov_b32 s96,0\ns_mov_b32 s97,s54\ns_lshl_b32 s98,s54,1\ns_add_u32 s99,s98,s54\nv_add_u32 v4,0x10000,v6\nv_add_u32 v5,0x10000,v7\n"
"s_waitcnt vmcnt(24)\ns_barrier\nxrt 48,8,0\nxrt 50,8,512\nxrt 52,9,0\nxrt 54,9,512\nxrb 32,6,0\nxrb 36,6,2048\nxrb 40,6,4096\nxrb 44,6,6144\n"
"xrt 72,8,4096\nxrt 74,8,4608\nxrt 76,9,4096\nxrt 78,9,4608\nxrb 56,7,0\nxrb 60,7,2048\nxrb 64,7,4096\nxrb 68,7,6144\nxwl 11\nxm16z 0,48,32\n"
"xrt 96,8,8192\nxrt 98,8,8704\nxm16z 4,52,32\nxrt 100,9,8192\nxwl 13\nxm16z 8,48,36\nxrt 102,9,8704\nxm16z 12,52,36\nxrb 80,6,16384\nxrb 84,6,18432\n"
"xwl 15\nxm16z 16,48,40\nxrb 88,6,20480\nxst1 128,30,40,s96,0\nxm16z 20,52,40\nxrb 92,6,22528\nxwl 15\nxm16z 24,48,44\nxrt 120,8,12288\n"
"xrt 122,8,12800\nxm16z 28,52,44\nxrt 124,9,12288\nxwl 14\nxm16 0,72,56\nxrt 126,9,12800\nxm16 4,76,56\nxrb 104,7,16384\nxrb 108,7,18432\nxwl 15\n"
"xm16 8,72,60\nxrb 112,7,20480\nxm16 12,76,60\nxrb 116,7,22528\nxm16 16,72,64\nxst1 132,30,40,s96,256\nxm16 20,76,64\nxwl 15\nxm16 24,72,68\n"
"xm16 28,76,68\ns_waitcnt vmcnt(14) lgkmcnt(0)\ns_barrier\nxam 62,0xc000\nxm16 0,96,80\nxdl 0,16,0,0\nxrt 48,8,16384\nxrt 50,8,16896\nxm16 4,100,80\n"
"xrt 52,9,16384\nxam 62,0xc400\nxm16 8,96,84\nxdl 1,16,s74,0\nxrt 54,9,16896\nxm16 12,100,84\nxrb 32,6,32768\nxrb 36,6,34816\nxm16 16,96,88\n"
"xrb 40,6,36864\nxam 62,0xc800\nxm16 20,100,88\nxdl 0,16,s75,0\nxrb 44,6,38912\nxm16 24,96,92\nxrt 72,8,20480\nxrt 74,8,20992\nxam 62,0xcc00\n"
"xm16 28,100,92\nxdl 1,16,s76,0\nxrt 76,9,20480\nxm16 0,120,104\nxrt 78,9,20992\nxst1 136,30,40,s97,0\nxm16 4,124,104\nxrb 56,7,32768\nxrb 60,7,34816\n"
"xam 62,0x10000\nxm16 8,120,108\nxdl 0,16,s77,0\nxrb 64,7,36864\nxm16 12,124,108\nxrb 68,7,38912\nxam 62,0x10400\nxm16 16,120,112\nxdl 1,16,s78,0\n"
"xm16 20,124,112\nxm16 24,120,116\nxam 62,0x10800\nxm16 28,124,116\nxdl 0,16,s79,0\nxwl 11\nxm16 0,48,32\nxrt 96,8,24576\nxrt 98,8,25088\n"
"xam 62,0x10c00\nxm16 4,52,32\nxdl 1,16,s80,0\ns_add_u32 s16,s16,0x100\ns_addc_u32 s17,s17,0\ns_sub_u32 s18,s18,0x100\nxrt 100,9,24576\nxwl 13\n"
"xm16 8,48,36\nxrt 102,9,25088\nxm16 12,52,36\nxrb 80,6,49152\nxrb 84,6,51200\nxam 63,0\nxwl 15\nxm16 16,48,40\nxdl1 2,20,0,0\nxrb 88,6,53248\n"
"xm16 20,52,40\nxrb 92,6,55296\nxam 63,0x400\nxwl 15\nxm16 24,48,44\nxdl1 3,20,s81,0\nxrt 120,8,28672\nxrt 122,8,29184\nxm16 28,52,44\n"
"xrt 124,9,28672\nxwl 14\nxm16 0,72,56\nxrt 126,9,29184\nxst1 140,30,40,s97,256\nxam 63,0x2000\nxm16 4,76,56\nxdl1 2,20,s82,0\nxrb 104,7,49152\n"
"xrb 108,7,51200\nxwl 15\nxm16 8,72,60\nxrb 112,7,53248\nxam 63,0x2400\nxm16 12,76,60\nxdl1 3,20,s83,0\ns_add_u32 s20,s20,0x38000\n"
"s_addc_u32 s21,s21,0\ns_sub_u32 s22,s22,0x38000\nxrb 116,7,55296\nxm16 16,72,64\nxm16 20,76,64\nxwl 15\nxm16 24,72,68\nxm16 28,76,68\n"
"s_waitcnt vmcnt(16) lgkmcnt(0)\ns_barrier\nxam 62,0x14000\nxm16 0,96,80\nxdl 0,16,0,0\nxrt 48,8,32768\nxrt 50,8,33280\nxm16 4,100,80\nxrt 52,9,32768\n"
"xam 62,0x14400\nxm16 8,96,84\nxdl 1,16,s74,0\nxrt 54,9,33280\nxm16 12,100,84\nxrb 32,4,0\nxrb 36,4,2048\nxm16 16,96,88\nxrb 40,4,4096\n"
"xst1 144,30,40,s98,0\nxam 62,0x14800\nxm16 20,100,88\nxdl 0,16,s75,0\nxrb 44,4,6144\nxm16 24,96,92\nxrt 72,8,36864\nxrt 74,8,37376\nxam 62,0x14c00\n"
"xm16 28,100,92\nxdl 1,16,s76,0\nxrt 76,9,36864\nxm16 0,120,104\nxrt 78,9,37376\nxm16 4,124,104\nxrb 56,5,0\nxrb 60,5,2048\nxam 62,0x18000\n"
"xm16 8,120,108\nxdl 0,16,s77,0\nxrb 64,5,4096\nxm16 12,124,108\nxrb 68,5,6144\nxam 62,0x18400\nxm16 16,120,112\nxdl 1,16,s78,0\n"
"xst1 148,30,40,s98,256\nxm16 20,124,112\nxm16 24,120,116\nxam 62,0x18800\nxm16 28,124,116\nxdl 0,16,s79,0\nxwl 11\nxm16 0,48,32\nxrt 96,8,40960\n"
"xrt 98,8,41472\nxam 62,0x18c00\nxm16 4,52,32\nxdl 1,16,s80,0\ns_add_u32 s16,s16,0x100\ns_addc_u32 s17,s17,0\ns_sub_u32 s18,s18,0x100\n"
"xrt 100,9,40960\nxwl 13\nxm16 8,48,36\nxrt 102,9,41472\nxm16 12,52,36\nxrb 80,4,16384\nxrb 84,4,18432\nxam 63,0x4000\nxwl 15\nxm16 16,48,40\n"
"xdl1 2,20,0,0\nxrb 88,4,20480\nxm16 20,52,40\nxrb 92,4,22528\nxam 63,0x4400\nxwl 15\nxm16 24,48,44\nxdl1 3,20,s81,0\nxrt 120,8,45056\n"
"xrt 122,8,45568\nxm16 28,52,44\nxrt 124,9,45056\nxwl 14\nxm16 0,72,56\nxrt 126,9,45568\nxst1 152,30,40,s99,0\nxam 63,0x6000\nxm16 4,76,56\n"
"xdl1 2,20,s82,0\nxrb 104,5,16384\nxrb 108,5,18432\nxwl 15\nxm16 8,72,60\nxrb 112,5,20480\nxam 63,0x6400\nxm16 12,76,60\nxdl1 3,20,s83,0\n"
"s_add_u32 s20,s20,0x38000\ns_addc_u32 s21,s21,0\ns_sub_u32 s22,s22,0x38000\nxrb 116,5,22528\nxm16 16,72,64\nxm16 20,76,64\nxwl 15\nxm16 24,72,68\n"
"xm16 28,76,68\ns_waitcnt vmcnt(15) lgkmcnt(0)\ns_barrier\nxam 62,0x1c000\nxm16 0,96,80\nxdl 0,16,0,0\nxrt 48,8,0\nxrt 50,8,512\nxm16 4,100,80\n"
"xrt 52,9,0\nxam 62,0x1c400\nxm16 8,96,84\nxdl 1,16,s74,0\nxrt 54,9,512\nxm16 12,100,84\nxrb 32,6,0\nxrb 36,6,2048\nxm16 16,96,88\nxrb 40,6,4096\n"
"xam 62,0x1c800\nxm16 20,100,88\nxdl 0,16,s75,0\nxrb 44,6,6144\nxm16 24,96,92\nxrt 72,8,4096\nxrt 74,8,4608\nxam 62,0x1cc00\nxm16 28,100,92\n"
"xdl 1,16,s76,0\nxrt 76,9,4096\nxm16 0,120,104\nxrt 78,9,4608\nxst1 156,30,40,s99,256\nxm16 4,124,104\nxrb 56,7,0\nxrb 60,7,2048\nxam 62,0x20000\n"
"xm16 8,120,108\nxdl 0,16,s77,0\nxrb 64,7,4096\nxm16 12,124,108\nxrb 68,7,6144\nxam 62,0x20400\nxm16 16,120,112\nxdl 1,16,s78,0\nxm16 20,124,112\n"
"xm16 24,120,116\nxam 62,0x20800\nxm16 28,124,116\nxdl 0,16,s79,0\nxwl 11\nxm16 0,48,32\nxrt 96,8,8192\nxrt 98,8,8704\nxam 62,0x20c00\nxm16 4,52,32\n"
"xdl 1,16,s80,0\ns_add_u32 s16,s16,0x100\ns_addc_u32 s17,s17,0\ns_sub_u32 s18,s18,0x100\nxrt 100,9,8192\nxwl 13\nxm16 8,48,36\nxrt 102,9,8704\n"
"xm16 12,52,36\nxrb 80,6,16384\nxrb 84,6,18432\nxam 63,0x8000\nxwl 15\nxm16 16,48,40\nxdl1 2,20,0,0\nxrb 88,6,20480\ns_add_u32 s68,s96,0x4000\n"
"xst1 160,30,40,s68,0\nxm16 20,52,40\nxrb 92,6,22528\nxam 63,0x8400\nxwl 15\nxm16 24,48,44\nxdl1 3,20,s81,0\nxrt 120,8,12288\nxrt 122,8,12800\n"
"xm16 28,52,44\nxrt 124,9,12288\nxwl 14\nxm16 0,72,56\nxrt 126,9,12800\nxam 63,0xa000\nxm16 4,76,56\nxdl1 2,20,s82,0\nxrb 104,7,16384\n"
"xrb 108,7,18432\nxwl 15\nxm16 8,72,60\nxrb 112,7,20480\nxam 63,0xa400\nxm16 12,76,60\nxdl1 3,20,s83,0\ns_add_u32 s20,s20,0x38000\n"
"s_addc_u32 s21,s21,0\ns_sub_u32 s22,s22,0x38000\nxrb 116,7,22528\nxm16 16,72,64\ns_add_u32 s68,s96,0x4000\nxst1 164,30,40,s68,256\nxm16 20,76,64\n"
"xwl 15\nxm16 24,72,68\nxm16 28,76,68\ns_waitcnt vmcnt(15) lgkmcnt(0)\ns_barrier\nxam 62,0xc000\nxm16 0,96,80\nxdl 0,16,0,0\nxrt 48,8,16384\n"
"xrt 50,8,16896\nxm16 4,100,80\nxrt 52,9,16384\nxam 62,0xc400\nxm16 8,96,84\nxdl 1,16,s74,0\nxrt 54,9,16896\nxm16 12,100,84\nxrb 32,6,32768\n"
"xrb 36,6,34816\nxm16 16,96,88\nxrb 40,6,36864\nxam 62,0xc800\nxm16 20,100,88\nxdl 0,16,s75,0\nxrb 44,6,38912\nxm16 24,96,92\nxrt 72,8,20480\n"
"xrt 74,8,20992\nxam 62,0xcc00\nxm16 28,100,92\nxdl 1,16,s76,0\nxrt 76,9,20480\nxm16 0,120,104\nxrt 78,9,20992\ns_add_u32 s68,s97,0x4000\n"
"xst1 168,30,40,s68,0\nxm16 4,124,104\nxrb 56,7,32768\nxrb 60,7,34816\nxam 62,0x10000\nxm16 8,120,108\nxdl 0,16,s77,0\nxrb 64,7,36864\n"
"xm16 12,124,108\nxrb 68,7,38912\nxam 62,0x10400\nxm16 16,120,112\nxdl 1,16,s78,0\nxm16 20,124,112\nxm16 24,120,116\nxam 62,0x10800\nxm16 28,124,116\n"
"xdl 0,16,s79,0\nxwl 11\nxm16 0,48,32\nxrt 96,8,24576\nxrt 98,8,25088\nxam 62,0x10c00\nxm16 4,52,32\nxdl 1,16,s80,0\ns_add_u32 s16,s16,0x100\n"
"s_addc_u32 s17,s17,0\ns_sub_u32 s18,s18,0x100\nxrt 100,9,24576\nxwl 13\nxm16 8,48,36\nxrt 102,9,25088\nxm16 12,52,36\nxrb 80,6,49152\nxrb 84,6,51200\n"
"xam 63,0\nxwl 15\nxm16 16,48,40\nxdl1 2,20,0,0\nxrb 88,6,53248\nxm16 20,52,40\nxrb 92,6,55296\nxam 63,0x400\nxwl 15\nxm16 24,48,44\nxdl1 3,20,s81,0\n"
"xrt 120,8,28672\nxrt 122,8,29184\nxm16 28,52,44\nxrt 124,9,28672\nxwl 14\nxm16 0,72,56\nxrt 126,9,29184\ns_add_u32 s68,s97,0x4000\n"
"xst1 172,30,40,s68,256\nxam 63,0x2000\nxm16 4,76,56\nxdl1 2,20,s82,0\nxrb 104,7,49152\nxrb 108,7,51200\nxwl 15\nxm16 8,72,60\nxrb 112,7,53248\n"
"xam 63,0x2400\nxm16 12,76,60\nxdl1 3,20,s83,0\ns_add_u32 s20,s20,0x38000\ns_addc_u32 s21,s21,0\ns_sub_u32 s22,s22,0x38000\nxrb 116,7,55296\n"
"xm16 16,72,64\nxm16 20,76,64\nxwl 15\nxm16 24,72,68\nxm16 28,76,68\ns_waitcnt vmcnt(14) lgkmcnt(0)\ns_barrier\nxam 62,0x14000\nxm16 0,96,80\n"
"xdl 0,16,0,0\nxrt 48,8,32768\nxrt 50,8,33280\nxm16 4,100,80\nxrt 52,9,32768\nxam 62,0x14400\nxm16 8,96,84\nxdl 1,16,s74,0\nxrt 54,9,33280\n"
"xm16 12,100,84\nxrb 32,4,0\nxrb 36,4,2048\nxm16 16,96,88\nxrb 40,4,4096\ns_add_u32 s68,s98,0x4000\nxst1 176,30,40,s68,0\nxam 62,0x14800\n"
"xm16 20,100,88\nxdl 0,16,s75,0\nxrb 44,4,6144\nxm16 24,96,92\nxrt 72,8,36864\nxrt 74,8,37376\nxam 62,0x14c00\nxm16 28,100,92\nxdl 1,16,s76,0\n"
"xrt 76,9,36864\nxm16 0,120,104\nxrt 78,9,37376\nxm16 4,124,104\nxrb 56,5,0\nxrb 60,5,2048\nxam 62,0x18000\nxm16 8,120,108\nxdl 0,16,s77,0\n"
"xrb 64,5,4096\nxm16 12,124,108\nxrb 68,5,6144\nxam 62,0x18400\nxm16 16,120,112\nxdl 1,16,s78,0\ns_add_u32 s68,s98,0x4000\nxst1 180,30,40,s68,256\n"
"xm16 20,124,112\nxm16 24,120,116\nxam 62,0x18800\nxm16 28,124,116\nxdl 0,16,s79,0\nxwl 11\nxm16 0,48,32\nxrt 96,8,40960\nxrt 98,8,41472\n"
"xam 62,0x18c00\nxm16 4,52,32\nxdl 1,16,s80,0\ns_add_u32 s16,s16,0x100\ns_addc_u32 s17,s17,0\ns_sub_u32 s18,s18,0x100\nxrt 100,9,40960\nxwl 13\n"
"xm16 8,48,36\nxrt 102,9,41472\nxm16 12,52,36\nxrb 80,4,16384\nxrb 84,4,18432\nxam 63,0x4000\nxwl 15\nxm16 16,48,40\nxdl1 2,20,0,0\nxrb 88,4,20480\n"
"xm16 20,52,40\nxrb 92,4,22528\nxam 63,0x4400\nxwl 15\nxm16 24,48,44\nxdl1 3,20,s81,0\nxrt 120,8,45056\nxrt 122,8,45568\nxm16 28,52,44\n"
"xrt 124,9,45056\nxwl 14\nxm16 0,72,56\nxrt 126,9,45568\ns_add_u32 s68,s99,0x4000\nxst1 184,30,40,s68,0\nxam 63,0x6000\nxm16 4,76,56\nxdl1 2,20,s82,0\n"
"xrb 104,5,16384\nxrb 108,5,18432\nxwl 15\nxm16 8,72,60\nxrb 112,5,20480\nxam 63,0x6400\nxm16 12,76,60\nxdl1 3,20,s83,0\ns_add_u32 s20,s20,0x38000\n"
"s_addc_u32 s21,s21,0\ns_sub_u32 s22,s22,0x38000\nxrb 116,5,22528\nxm16 16,72,64\nxm16 20,76,64\nxwl 15\nxm16 24,72,68\nxm16 28,76,68\n"
"s_waitcnt vmcnt(15) lgkmcnt(0)\ns_barrier\nxam 62,0x1c000\nxm16 0,96,80\nxdl 0,16,0,0\nxrt 48,8,0\nxrt 50,8,512\nxm16 4,100,80\nxrt 52,9,0\n"
"xam 62,0x1c400\nxm16 8,96,84\nxdl 1,16,s74,0\nxrt 54,9,512\nxm16 12,100,84\nxrb 32,6,0\nxrb 36,6,2048\nxm16 16,96,88\nxrb 40,6,4096\nxam 62,0x1c800\n"
"xm16 20,100,88\nxdl 0,16,s75,0\nxrb 44,6,6144\nxm16 24,96,92\nxrt 72,8,4096\nxrt 74,8,4608\nxam 62,0x1cc00\nxm16 28,100,92\nxdl 1,16,s76,0\n"
"xrt 76,9,4096\nxm16 0,120,104\nxrt 78,9,4608\ns_add_u32 s68,s99,0x4000\nxst1 188,30,40,s68,256\nxm16 4,124,104\nxrb 56,7,0\nxrb 60,7,2048\n"
"xam 62,0x20000\nxm16 8,120,108\nxdl 0,16,s77,0\nxrb 64,7,4096\nxm16 12,124,108\nxrb 68,7,6144\nxam 62,0x20400\nxm16 16,120,112\nxdl 1,16,s78,0\n"
"xm16 20,124,112\nxm16 24,120,116\nxam 62,0x20800\nxm16 28,124,116\nxdl 0,16,s79,0\nxwl 11\nxm16 0,48,32\nxrt 96,8,8192\nxrt 98,8,8704\n"
"xam 62,0x20c00\nxm16 4,52,32\nxdl 1,16,s80,0\ns_add_u32 s16,s16,0x100\ns_addc_u32 s17,s17,0\ns_sub_u32 s18,s18,0x100\nxrt 100,9,8192\nxwl 13\n"
"xm16 8,48,36\nxrt 102,9,8704\nxm16 12,52,36\nxrb 80,6,16384\nxrb 84,6,18432\nxam 63,0x8000\nxwl 15\nxm16 16,48,40\nxdl1 2,20,0,0\nxrb 88,6,20480\n"
"s_add_u32 s68,s96,0x8000\nxst1 192,30,40,s68,0\nxm16 20,52,40\nxrb 92,6,22528\nxam 63,0x8400\nxwl 15\nxm16 24,48,44\nxdl1 3,20,s81,0\n"
"xrt 120,8,12288\nxrt 122,8,12800\nxm16 28,52,44\nxrt 124,9,12288\nxwl 14\nxm16 0,72,56\nxrt 126,9,12800\nxam 63,0xa000\nxm16 4,76,56\n"
"xdl1 2,20,s82,0\nxrb 104,7,16384\nxrb 108,7,18432\nxwl 15\nxm16 8,72,60\nxrb 112,7,20480\nxam 63,0xa400\nxm16 12,76,60\nxdl1 3,20,s83,0\n"
"s_add_u32 s20,s20,0x38000\ns_addc_u32 s21,s21,0\ns_sub_u32 s22,s22,0x38000\nxrb 116,7,22528\nxm16 16,72,64\ns_add_u32 s68,s96,0x8000\n"
"xst1 196,30,40,s68,256\nxm16 20,76,64\nxwl 15\nxm16 24,72,68\nxm16 28,76,68\ns_waitcnt vmcnt(15) lgkmcnt(0)\ns_barrier\nxam 62,0xc000\nxm16 0,96,80\n"
"xdl 0,16,0,0\nxrt 48,8,16384\nxrt 50,8,16896\nxm16 4,100,80\nxrt 52,9,16384\nxam 62,0xc400\nxm16 8,96,84\nxdl 1,16,s74,0\nxrt 54,9,16896\n"
"xm16 12,100,84\nxrb 32,6,32768\nxrb 36,6,34816\nxm16 16,96,88\nxrb 40,6,36864\nxam 62,0xc800\nxm16 20,100,88\nxdl 0,16,s75,0\nxrb 44,6,38912\n"
"xm16 24,96,92\nxrt 72,8,20480\nxrt 74,8,20992\nxam 62,0xcc00\nxm16 28,100,92\nxdl 1,16,s76,0\nxrt 76,9,20480\nxm16 0,120,104\nxrt 78,9,20992\n"
"s_add_u32 s68,s97,0x8000\nxst1 200,30,40,s68,0\nxm16 4,124,104\nxrb 56,7,32768\nxrb 60,7,34816\nxam 62,0x10000\nxm16 8,120,108\nxdl 0,16,s77,0\n"
"xrb 64,7,36864\nxm16 12,124,108\nxrb 68,7,38912\nxam 62,0x10400\nxm16 16,120,112\nxdl 1,16,s78,0\nxm16 20,124,112\nxm16 24,120,116\nxam 62,0x10800\n"
"xm16 28,124,116\nxdl 0,16,s79,0\nxwl 11\nxm16 0,48,32\nxrt 96,8,24576\nxrt 98,8,25088\nxam 62,0x10c00\nxm16 4,52,32\nxdl 1,16,s80,0\n"
"s_add_u32 s16,s16,0x100\ns_addc_u32 s17,s17,0\ns_sub_u32 s18,s18,0x100\nxrt 100,9,24576\nxwl 13\nxm16 8,48,36\nxrt 102,9,25088\nxm16 12,52,36\n"
"xrb 80,6,49152\nxrb 84,6,51200\nxam 63,0\nxwl 15\nxm16 16,48,40\nxdl1 2,20,0,0\nxrb 88,6,53248\nxm16 20,52,40\nxrb 92,6,55296\nxam 63,0x400\nxwl 15\n"
"xm16 24,48,44\nxdl1 3,20,s81,0\nxrt 120,8,28672\nxrt 122,8,29184\nxm16 28,52,44\nxrt 124,9,28672\nxwl 14\nxm16 0,72,56\nxrt 126,9,29184\n"
"s_add_u32 s68,s97,0x8000\nxst1 204,30,40,s68,256\nxam 63,0x2000\nxm16 4,76,56\nxdl1 2,20,s82,0\nxrb 104,7,49152\nxrb 108,7,51200\nxwl 15\n"
"xm16 8,72,60\nxrb 112,7,53248\nxam 63,0x2400\nxm16 12,76,60\nxdl1 3,20,s83,0\ns_add_u32 s20,s20,0x38000\ns_addc_u32 s21,s21,0\n"
"s_sub_u32 s22,s22,0x38000\nxrb 116,7,55296\nxm16 16,72,64\nxm16 20,76,64\nxwl 15\nxm16 24,72,68\nxm16 28,76,68\ns_waitcnt vmcnt(14) lgkmcnt(0)\n"
"s_barrier\nxam 62,0x14000\nxm16 0,96,80\nxdl 0,16,0,0\nxrt 48,8,32768\nxrt 50,8,33280\nxm16 4,100,80\nxrt 52,9,32768\nxam 62,0x14400\nxm16 8,96,84\n"
"xdl 1,16,s74,0\nxrt 54,9,33280\nxm16 12,100,84\nxrb 32,4,0\nxrb 36,4,2048\nxm16 16,96,88\nxrb 40,4,4096\ns_add_u32 s68,s98,0x8000\n"
"xst1 208,30,40,s68,0\nxam 62,0x14800\nxm16 20,100,88\nxdl 0,16,s75,0\nxrb 44,4,6144\nxm16 24,96,92\nxrt 72,8,36864\nxrt 74,8,37376\nxam 62,0x14c00\n"
"xm16 28,100,92\nxdl 1,16,s76,0\nxrt 76,9,36864\nxm16 0,120,104\nxrt 78,9,37376\nxm16 4,124,104\nxrb 56,5,0\nxrb 60,5,2048\nxam 62,0x18000\n"
"xm16 8,120,108\nxdl 0,16,s77,0\nxrb 64,5,4096\nxm16 12,124,108\nxrb 68,5,6144\nxam 62,0x18400\nxm16 16,120,112\nxdl 1,16,s78,0\n"
"s_add_u32 s68,s98,0x8000\nxst1 212,30,40,s68,256\nxm16 20,124,112\nxm16 24,120,116\nxam 62,0x18800\nxm16 28,124,116\nxdl 0,16,s79,0\nxwl 11\n"
"xm16 0,48,32\nxrt 96,8,40960\nxrt 98,8,41472\nxam 62,0x18c00\nxm16 4,52,32\nxdl 1,16,s80,0\ns_add_u32 s16,s16,0x100\ns_addc_u32 s17,s17,0\n"
"s_sub_u32 s18,s18,0x100\nxrt 100,9,40960\nxwl 13\nxm16 8,48,36\nxrt 102,9,41472\nxm16 12,52,36\nxrb 80,4,16384\nxrb 84,4,18432\nxam 63,0x4000\n"
"xwl 15\nxm16 16,48,40\nxdl1 2,20,0,0\nxrb 88,4,20480\nxm16 20,52,40\nxrb 92,4,22528\nxam 63,0x4400\nxwl 15\nxm16 24,48,44\nxdl1 3,20,s81,0\n"
"xrt 120,8,45056\nxrt 122,8,45568\nxm16 28,52,44\nxrt 124,9,45056\nxwl 14\nxm16 0,72,56\nxrt 126,9,45568\ns_add_u32 s68,s99,0x8000\n"
"xst1 216,30,40,s68,0\nxam 63,0x6000\nxm16 4,76,56\nxdl1 2,20,s82,0\nxrb 104,5,16384\nxrb 108,5,18432\nxwl 15\nxm16 8,72,60\nxrb 112,5,20480\n"
"xam 63,0x6400\nxm16 12,76,60\nxdl1 3,20,s83,0\ns_add_u32 s20,s20,0x38000\ns_addc_u32 s21,s21,0\ns_sub_u32 s22,s22,0x38000\nxrb 116,5,22528\n"
"xm16 16,72,64\nxm16 20,76,64\nxwl 15\nxm16 24,72,68\nxm16 28,76,68\ns_waitcnt vmcnt(15) lgkmcnt(0)\ns_barrier\nxam 62,0x1c000\nxm16 0,96,80\n"
"xdl 0,16,0,0\nxrt 48,8,0\nxrt 50,8,512\nxm16 4,100,80\nxrt 52,9,0\nxam 62,0x1c400\nxm16 8,96,84\nxdl 1,16,s74,0\nxrt 54,9,512\nxm16 12,100,84\n"
"xrb 32,6,0\nxrb 36,6,2048\nxm16 16,96,88\nxrb 40,6,4096\nxam 62,0x1c800\nxm16 20,100,88\nxdl 0,16,s75,0\nxrb 44,6,6144\nxm16 24,96,92\nxrt 72,8,4096\n"
"xrt 74,8,4608\nxam 62,0x1cc00\nxm16 28,100,92\nxdl 1,16,s76,0\nxrt 76,9,4096\nxm16 0,120,104\nxrt 78,9,4608\ns_add_u32 s68,s99,0x8000\n"
"xst1 220,30,40,s68,256\nxm16 4,124,104\nxrb 56,7,0\nxrb 60,7,2048\nxam 62,0x20000\nxm16 8,120,108\nxdl 0,16,s77,0\nxrb 64,7,4096\nxm16 12,124,108\n"
"xrb 68,7,6144\nxam 62,0x20400\nxm16 16,120,112\nxdl 1,16,s78,0\nxm16 20,124,112\nxm16 24,120,116\nxam 62,0x20800\nxm16 28,124,116\nxdl 0,16,s79,0\n"
"xwl 11\nxm16 0,48,32\nxrt 96,8,8192\nxrt 98,8,8704\nxam 62,0x20c00\nxm16 4,52,32\nxdl 1,16,s80,0\ns_add_u32 s16,s16,0x100\ns_addc_u32 s17,s17,0\n"
"s_sub_u32 s18,s18,0x100\nxrt 100,9,8192\nxwl 13\nxm16 8,48,36\nxrt 102,9,8704\nxm16 12,52,36\nxrb 80,6,16384\nxrb 84,6,18432\nxam 63,0x8000\nxwl 15\n"
"xm16 16,48,40\nxdl1 2,20,0,0\nxrb 88,6,20480\ns_add_u32 s68,s96,0xc000\nxst1 224,30,40,s68,0\nxm16 20,52,40\nxrb 92,6,22528\nxam 63,0x8400\nxwl 15\n"
"xm16 24,48,44\nxdl1 3,20,s81,0\nxrt 120,8,12288\nxrt 122,8,12800\nxm16 28,52,44\nxrt 124,9,12288\nxwl 14\nxm16 0,72,56\nxrt 126,9,12800\n"
"xam 63,0xa000\nxm16 4,76,56\nxdl1 2,20,s82,0\nxrb 104,7,16384\nxrb 108,7,18432\nxwl 15\nxm16 8,72,60\nxrb 112,7,20480\nxam 63,0xa400\nxm16 12,76,60\n"
"xdl1 3,20,s83,0\ns_add_u32 s20,s20,0x38000\ns_addc_u32 s21,s21,0\ns_sub_u32 s22,s22,0x38000\nxrb 116,7,22528\nxm16 16,72,64\n"
"s_add_u32 s68,s96,0xc000\nxst1 228,30,40,s68,256\nxm16 20,76,64\nxwl 15\nxm16 24,72,68\nxm16 28,76,68\ns_waitcnt vmcnt(15) lgkmcnt(0)\ns_barrier\n"
"xam 62,0xc000\nxm16 0,96,80\nxdl 0,16,0,0\nxrt 48,8,16384\nxrt 50,8,16896\nxm16 4,100,80\nxrt 52,9,16384\nxam 62,0xc400\nxm16 8,96,84\n"
"xdl 1,16,s74,0\nxrt 54,9,16896\nxm16 12,100,84\nxrb 32,6,32768\nxrb 36,6,34816\nxm16 16,96,88\nxrb 40,6,36864\nxam 62,0xc800\nxm16 20,100,88\n"
"xdl 0,16,s75,0\nxrb 44,6,38912\nxm16 24,96,92\nxrt 72,8,20480\nxrt 74,8,20992\nxam 62,0xcc00\nxm16 28,100,92\nxdl 1,16,s76,0\nxrt 76,9,20480\n"
"xm16 0,120,104\nxrt 78,9,20992\ns_add_u32 s68,s97,0xc000\nxst1 232,30,40,s68,0\nxm16 4,124,104\nxrb 56,7,32768\nxrb 60,7,34816\nxam 62,0x10000\n"
"xm16 8,120,108\nxdl 0,16,s77,0\nxrb 64,7,36864\nxm16 12,124,108\nxrb 68,7,38912\nxam 62,0x10400\nxm16 16,120,112\nxdl 1,16,s78,0\nxm16 20,124,112\n"
"xm16 24,120,116\nxam 62,0x10800\nxm16 28,124,116\nxdl 0,16,s79,0\nxwl 11\nxm16 0,48,32\nxrt 96,8,24576\nxrt 98,8,25088\nxam 62,0x10c00\nxm16 4,52,32\n"
"xdl 1,16,s80,0\ns_add_u32 s16,s16,0x100\ns_addc_u32 s17,s17,0\ns_sub_u32 s18,s18,0x100\nxrt 100,9,24576\nxwl 13\nxm16 8,48,36\nxrt 102,9,25088\n"
"xm16 12,52,36\nxrb 80,6,49152\nxrb 84,6,51200\nxam 63,0\nxwl 15\nxm16 16,48,40\nxdl1 2,20,0,0\nxrb 88,6,53248\nxm16 20,52,40\nxrb 92,6,55296\n"
"xam 63,0x400\nxwl 15\nxm16 24,48,44\nxdl1 3,20,s81,0\nxrt 120,8,28672\nxrt 122,8,29184\nxm16 28,52,44\nxrt 124,9,28672\nxwl 14\nxm16 0,72,56\n"
"xrt 126,9,29184\ns_add_u32 s68,s97,0xc000\nxst1 236,30,40,s68,256\nxam 63,0x2000\nxm16 4,76,56\nxdl1 2,20,s82,0\nxrb 104,7,49152\nxrb 108,7,51200\n"
"xwl 15\nxm16 8,72,60\nxrb 112,7,53248\nxam 63,0x2400\nxm16 12,76,60\nxdl1 3,20,s83,0\ns_add_u32 s20,s20,0x38000\ns_addc_u32 s21,s21,0\n"
"s_sub_u32 s22,s22,0x38000\nxrb 116,7,55296\nxm16 16,72,64\nxm16 20,76,64\nxwl 15\nxm16 24,72,68\nxm16 28,76,68\ns_waitcnt vmcnt(14) lgkmcnt(0)\n"
"s_barrier\nxam 62,0x14000\nxm16 0,96,80\nxdl 0,16,0,0\nxrt 48,8,32768\nxrt 50,8,33280\nxm16 4,100,80\nxrt 52,9,32768\nxam 62,0x14400\nxm16 8,96,84\n"
"xdl 1,16,s74,0\nxrt 54,9,33280\nxm16 12,100,84\nxrb 32,4,0\nxrb 36,4,2048\nxm16 16,96,88\nxrb 40,4,4096\ns_add_u32 s68,s98,0xc000\n"
"xst1 240,30,40,s68,0\nxam 62,0x14800\nxm16 20,100,88\nxdl 0,16,s75,0\nxrb 44,4,6144\nxm16 24,96,92\nxrt 72,8,36864\nxrt 74,8,37376\nxam 62,0x14c00\n"
"xm16 28,100,92\nxdl 1,16,s76,0\nxrt 76,9,36864\nxm16 0,120,104\nxrt 78,9,37376\nxm16 4,124,104\nxrb 56,5,0\nxrb 60,5,2048\nxam 62,0x18000\n"
"xm16 8,120,108\nxdl 0,16,s77,0\nxrb 64,5,4096\nxm16 12,124,108\nxrb 68,5,6144\nxam 62,0x18400\nxm16 16,120,112\nxdl 1,16,s78,0\n"
"s_add_u32 s68,s98,0xc000\nxst1 244,30,40,s68,256\nxm16 20,124,112\nxm16 24,120,116\nxam 62,0x18800\nxm16 28,124,116\nxdl 0,16,s79,0\nxwl 11\n"
"xm16 0,48,32\nxrt 96,8,40960\nxrt 98,8,41472\nxam 62,0x18c00\nxm16 4,52,32\nxdl 1,16,s80,0\ns_add_u32 s16,s16,0x100\ns_addc_u32 s17,s17,0\n"
"s_sub_u32 s18,s18,0x100\nxrt 100,9,40960\nxwl 13\nxm16 8,48,36\nxrt 102,9,41472\nxm16 12,52,36\nxrb 80,4,16384\nxrb 84,4,18432\nxam 63,0x4000\n"
"xwl 15\nxm16 16,48,40\nxdl1 2,20,0,0\nxrb 88,4,20480\nxm16 20,52,40\nxrb 92,4,22528\nxam 63,0x4400\nxwl 15\nxm16 24,48,44\nxdl1 3,20,s81,0\n"
"xrt 120,8,45056\nxrt 122,8,45568\nxm16 28,52,44\nxrt 124,9,45056\nxwl 14\nxm16 0,72,56\nxrt 126,9,45568\ns_add_u32 s68,s99,0xc000\n"
"xst1 248,30,40,s68,0\nxam 63,0x6000\nxm16 4,76,56\nxdl1 2,20,s82,0\nxrb 104,5,16384\nxrb 108,5,18432\nxwl 15\nxm16 8,72,60\nxrb 112,5,20480\n"
"xam 63,0x6400\nxm16 12,76,60\nxdl1 3,20,s83,0\ns_add_u32 s20,s20,0x38000\ns_addc_u32 s21,s21,0\ns_sub_u32 s22,s22,0x38000\nxrb 116,5,22528\n"
"xm16 16,72,64\nxm16 20,76,64\nxwl 15\nxm16 24,72,68\nxm16 28,76,68\ns_waitcnt vmcnt(15) lgkmcnt(0)\ns_barrier\nxam 62,0x1c000\nxm16 0,96,80\n"
"xdl 0,16,0,0\nxrt 48,8,0\nxrt 50,8,512\nxm16 4,100,80\nxrt 52,9,0\nxam 62,0x1c400\nxm16 8,96,84\nxdl 1,16,s74,0\nxrt 54,9,512\nxm16 12,100,84\n"
"xrb 32,6,0\nxrb 36,6,2048\nxm16 16,96,88\nxrb 40,6,4096\nxam 62,0x1c800\nxm16 20,100,88\nxdl 0,16,s75,0\nxrb 44,6,6144\nxm16 24,96,92\nxrt 72,8,4096\n"
"xrt 74,8,4608\nxam 62,0x1cc00\nxm16 28,100,92\nxdl 1,16,s76,0\nxrt 76,9,4096\nxm16 0,120,104\nxrt 78,9,4608\ns_add_u32 s68,s99,0xc000\n"
"xst1 252,30,40,s68,256\nxm16 4,124,104\nxrb 56,7,0\nxrb 60,7,2048\nxam 62,0x20000\nxm16 8,120,108\nxdl 0,16,s77,0\nxrb 64,7,4096\nxm16 12,124,108\n"
"xrb 68,7,6144\nxam 62,0x20400\nxm16 16,120,112\nxdl 1,16,s78,0\nxm16 20,124,112\nxm16 24,120,116\nxam 62,0x20800\nxm16 28,124,116\nxdl 0,16,s79,0\n"
"xwl 11\nxm16 0,48,32\nxrt 96,8,8192\nxrt 98,8,8704\nxam 62,0x20c00\nxm16 4,52,32\nxdl 1,16,s80,0\ns_add_u32 s16,s16,0x100\ns_addc_u32 s17,s17,0\n"
"s_sub_u32 s18,s18,0x100\nxrt 100,9,8192\nxwl 13\nxm16 8,48,36\nxrt 102,9,8704\nxm16 12,52,36\nxrb 80,6,16384\nxrb 84,6,18432\nxam 63,0x8000\nxwl 15\n"
"xm16 16,48,40\nxdl1 2,20,0,0\nxrb 88,6,20480\nxm16 20,52,40\nxrb 92,6,22528\nxam 63,0x8400\nxwl 15\nxm16 24,48,44\nxdl1 3,20,s81,0\nxrt 120,8,12288\n"
"xrt 122,8,12800\nxm16 28,52,44\nxrt 124,9,12288\nxwl 14\nxm16 0,72,56\nxrt 126,9,12800\nxam 63,0xa000\nxm16 4,76,56\nxdl1 2,20,s82,0\n"
"xrb 104,7,16384\nxrb 108,7,18432\nxwl 15\nxm16 8,72,60\nxrb 112,7,20480\nxam 63,0xa400\nxm16 12,76,60\nxdl1 3,20,s83,0\ns_add_u32 s20,s20,0x38000\n"
"s_addc_u32 s21,s21,0\ns_sub_u32 s22,s22,0x38000\nxrb 116,7,22528\nxm16 16,72,64\nxm16 20,76,64\nxwl 15\nxm16 24,72,68\nxm16 28,76,68\n"
"s_waitcnt vmcnt(13) lgkmcnt(0)\ns_barrier\nv_mbcnt_lo_u32_b32 v27,-1,0\nv_mbcnt_hi_u32_b32 v27,-1,v27\nv_lshlrev_b32 v27,3,v27\n"
"buffer_load_dwordx2 v[28:29],v27,s[44:47],s53 offen offset:0 sc1\nxam 62,0xc000\nxm16 0,96,80\nxdl 0,16,0,0\nxrt 48,8,16384\nxrt 50,8,16896\n"
"xm16 4,100,80\nxrt 52,9,16384\nxam 62,0xc400\nxm16 8,96,84\nxdl 1,16,s74,0\nxrt 54,9,16896\nxm16 12,100,84\nxrb 32,6,32768\nxrb 36,6,34816\n"
"xm16 16,96,88\nxrb 40,6,36864\nxam 62,0xc800\nxm16 20,100,88\nxdl 0,16,s75,0\nxrb 44,6,38912\nxm16 24,96,92\nxrt 72,8,20480\nxrt 74,8,20992\n"
"xam 62,0xcc00\nxm16 28,100,92\nxdl 1,16,s76,0\nxrt 76,9,20480\nxm16 0,120,104\nxrt 78,9,20992\nxm16 4,124,104\nxrb 56,7,32768\nxrb 60,7,34816\n"
"xam 62,0x10000\nxm16 8,120,108\nxdl 0,16,s77,0\nxrb 64,7,36864\nxm16 12,124,108\nxrb 68,7,38912\nxam 62,0x10400\nxm16 16,120,112\nxdl 1,16,s78,0\n"
"xm16 20,124,112\nxm16 24,120,116\nxam 62,0x10800\nxm16 28,124,116\nxdl 0,16,s79,0\ns_mov_b32 s39,0x20000\ns_mov_b32 s36,s88\ns_mov_b32 s37,s89\n"
"s_mov_b32 s38,0x3800\nbuffer_load_dword v31,v22,s[36:39],0 offen offset:0\ns_mov_b32 s36,s90\ns_mov_b32 s37,s91\ns_mov_b32 s38,0xc40\n"
"buffer_load_dword v31,v22,s[36:39],0 offen offset:0\nxwl 11\nxm16 0,48,32\nxrt 96,8,24576\nxrt 98,8,25088\nxam 62,0x10c00\nxm16 4,52,32\n"
"xdl 1,16,s80,0\ns_add_u32 s16,s16,0x100\ns_addc_u32 s17,s17,0\ns_sub_u32 s18,s18,0x100\nxrt 100,9,24576\nxwl 13\nxm16 8,48,36\nxrt 102,9,25088\n"
"xm16 12,52,36\nxrb 80,6,49152\nxrb 84,6,51200\nxam 63,0\nxwl 15\nxm16 16,48,40\nxdl1 2,20,0,0\nxrb 88,6,53248\nxm16 20,52,40\nxrb 92,6,55296\n"
"xam 63,0x400\nxwl 15\nxm16 24,48,44\nxdl1 3,20,s81,0\nxrt 120,8,28672\nxrt 122,8,29184\nxm16 28,52,44\nxrt 124,9,28672\nxwl 14\nxm16 0,72,56\n"
"xrt 126,9,29184\nxam 63,0x2000\nxm16 4,76,56\nxdl1 2,20,s82,0\nxrb 104,7,49152\nxrb 108,7,51200\nxwl 15\nxm16 8,72,60\nxrb 112,7,53248\n"
"xam 63,0x2400\nxm16 12,76,60\nxdl1 3,20,s83,0\ns_add_u32 s20,s20,0x38000\ns_addc_u32 s21,s21,0\ns_sub_u32 s22,s22,0x38000\nxrb 116,7,55296\n"
"xm16 16,72,64\nxm16 20,76,64\nxwl 15\nxm16 24,72,68\nxm16 28,76,68\ns_waitcnt vmcnt(12) lgkmcnt(0)\ns_barrier\nxm16 0,96,80\nxrt 48,8,32768\n"
"xrt 50,8,33280\nxm16 4,100,80\nxrt 52,9,32768\nxm16 8,96,84\nxrt 54,9,33280\nxm16 12,100,84\nxrb 32,4,0\nxrb 36,4,2048\nxm16 16,96,88\nxrb 40,4,4096\n"
"xm16 20,100,88\nxrb 44,4,6144\nxm16 24,96,92\nxrt 72,8,36864\nxrt 74,8,37376\nxm16 28,100,92\nxrt 76,9,36864\nxm16 0,120,104\nxrt 78,9,37376\n"
"xm16 4,124,104\nxrb 56,5,0\nxrb 60,5,2048\nxm16 8,120,108\nxrb 64,5,4096\nxm16 12,124,108\nxrb 68,5,6144\nxm16 16,120,112\nxm16 20,124,112\n"
"xm16 24,120,116\nxm16 28,124,116\nxwl 11\nxm16 0,48,32\nxrt 96,8,40960\nxrt 98,8,41472\nxm16 4,52,32\nxrt 100,9,40960\nxwl 13\nxm16 8,48,36\n"
"xrt 102,9,41472\nxm16 12,52,36\nxrb 80,4,16384\nxrb 84,4,18432\nxwl 15\nxm16 16,48,40\nxrb 88,4,20480\nxm16 20,52,40\nxrb 92,4,22528\nxwl 15\n"
"xm16 24,48,44\nxrt 120,8,45056\nxrt 122,8,45568\nxm16 28,52,44\nxrt 124,9,45056\nxwl 14\nxm16 0,72,56\nxrt 126,9,45568\nxm16 4,76,56\n"
"xrb 104,5,16384\nxrb 108,5,18432\nxwl 15\nxm16 8,72,60\nxrb 112,5,20480\nxm16 12,76,60\nxrb 116,5,22528\nxm16 16,72,64\nxm16 20,76,64\nxwl 15\n"
"xm16 24,72,68\nxm16 28,76,68\ns_waitcnt vmcnt(0) lgkmcnt(0)\ns_barrier\ns_cmp_eq_u32 s3,0\ns_cbranch_scc0 Lnosig_39_%=\nv_mov_b32 v24,s6\n"
"v_mov_b32 v25,s7\nv_mov_b32 v26,0\ns_mov_b64 exec,1\nbuffer_store_dwordx2 v[24:25],v26,s[44:47],s52 offen offset:0 sc1\ns_mov_b64 exec,-1\n"
"Lnosig_39_%=:\ns_nop 1\nxm16 0,96,80\nxrt 48,8,0\nxrt 50,8,512\nxm16 4,100,80\nxrt 52,9,0\nxm16 8,96,84\nxrt 54,9,512\nxm16 12,100,84\nxrb 32,6,0\n"
"xrb 36,6,2048\nxm16 16,96,88\nxrb 40,6,4096\nxm16 20,100,88\nxrb 44,6,6144\nxm16 24,96,92\nxrt 72,8,4096\nxrt 74,8,4608\nxm16 28,100,92\n"
"xrt 76,9,4096\nxm16 0,120,104\nxrt 78,9,4608\nxm16 4,124,104\nxrb 56,7,0\nxrb 60,7,2048\nxm16 8,120,108\nxrb 64,7,4096\nxm16 12,124,108\n"
"xrb 68,7,6144\nxm16 16,120,112\nxm16 20,124,112\nxm16 24,120,116\nxm16 28,124,116\nxwl 11\nxm16 0,48,32\nxrt 96,8,8192\nxrt 98,8,8704\nxm16 4,52,32\n"
"xrt 100,9,8192\nxwl 13\nxm16 8,48,36\nxrt 102,9,8704\nxm16 12,52,36\nxrb 80,6,16384\nxrb 84,6,18432\nxwl 15\nxm16 16,48,40\nxrb 88,6,20480\n"
"xm16 20,52,40\nxrb 92,6,22528\nxwl 15\nxm16 24,48,44\nxrt 120,8,12288\nxrt 122,8,12800\nxm16 28,52,44\nxrt 124,9,12288\nxwl 14\nxm16 0,72,56\n"
"xrt 126,9,12800\nxm16 4,76,56\nxrb 104,7,16384\nxrb 108,7,18432\nxwl 15\nxm16 8,72,60\nxrb 112,7,20480\nxm16 12,76,60\nxrb 116,7,22528\n"
"xm16 16,72,64\nxm16 20,76,64\nxwl 15\nxm16 24,72,68\nxm16 28,76,68\nxwl 11\nxm16 0,96,80\nxm16 4,100,80\nxwl 10\nxm16 8,96,84\nxm16 12,100,84\nxwl 9\n"
"xm16 16,96,88\nxm16 20,100,88\nxwl 8\nxm16 24,96,92\nxm16 28,100,92\nxwl 3\nxm16 0,120,104\nxm16 4,124,104\nxwl 2\nxm16 8,120,108\nxm16 12,124,108\n"
"xwl 1\nxm16 16,120,112\nxm16 20,124,112\nxwl 0\nxm16 24,120,116\nxm16 28,124,116\ns_mov_b32 s70,0x30d40\nLspin_40_%=:\ns_waitcnt vmcnt(0)\n"
"v_cmp_eq_u64 vcc,s[6:7],v[28:29]\ns_nop 3\ns_and_b32 s69,vcc_lo,0xffff\ns_cmp_eq_u32 s69,0xffff\ns_cbranch_scc1 Lspun_41_%=\ns_sleep 2\n"
"buffer_load_dwordx2 v[28:29],v27,s[44:47],s53 offen offset:0 sc1\ns_sub_u32 s70,s70,1\ns_cmp_gt_i32 s70,0\ns_cbranch_scc1 Lspin_40_%=\n"
"s_waitcnt vmcnt(0)\nLspun_41_%=:\ns_nop 1\nbuffer_load_dword v14,v20,s[48:51],0 offen offset:0 sc1\n"
"buffer_load_ushort v10,v21,s[92:95],0 offen offset:0\nbuffer_load_dword v15,v20,s[48:51],0 offen offset:64 sc1\n"
"buffer_load_ushort v11,v21,s[92:95],0 offen offset:32\nbuffer_load_dword v16,v20,s[48:51],0 offen offset:128 sc1\n"
"buffer_load_ushort v12,v21,s[92:95],0 offen offset:64\nbuffer_load_dword v17,v20,s[48:51],0 offen offset:192 sc1\n"
"buffer_load_ushort v13,v21,s[92:95],0 offen offset:96\ns_waitcnt vmcnt(0)\nv_sub_f32 v14,1.0,v14\nv_lshlrev_b32 v10,16,v10\nv_sub_f32 v15,1.0,v15\n"
"v_lshlrev_b32 v11,16,v11\nv_sub_f32 v16,1.0,v16\nv_lshlrev_b32 v12,16,v12\nv_sub_f32 v17,1.0,v17\nv_lshlrev_b32 v13,16,v13\ns_nop 3\ns_mov_b32 s68,0\n"
"buffer_load_dwordx4 v[128:131],v18,s[28:31],s68 offen offset:0 sc1\ns_mov_b32 s68,0x7000\n"
"buffer_load_dwordx4 v[132:135],v18,s[28:31],s68 offen offset:0 sc1\ns_mov_b32 s68,0xe000\n"
"buffer_load_dwordx4 v[136:139],v18,s[28:31],s68 offen offset:0 sc1\ns_mov_b32 s68,0x15000\n"
"buffer_load_dwordx4 v[140:143],v18,s[28:31],s68 offen offset:0 sc1\ns_waitcnt vmcnt(3)\nv_lshlrev_b32 v152,16,v128\nv_and_b32 v153,0xffff0000,v128\n"
"v_lshlrev_b32 v154,16,v129\nv_and_b32 v155,0xffff0000,v129\nv_lshlrev_b32 v156,16,v130\nv_and_b32 v157,0xffff0000,v130\nv_lshlrev_b32 v158,16,v131\n"
"v_and_b32 v159,0xffff0000,v131\nxar 144,0\nxar 145,1\nxar 146,2\nxar 147,3\nxar 148,4\nxar 149,5\nxar 150,6\nxar 151,7\nxmu 152,v152,14\n"
"xmu 153,v153,14\nxmu 154,v154,14\nxmu 155,v155,14\nxmu 156,v156,14\nxmu 157,v157,14\nxmu 158,v158,14\nxmu 159,v159,14\nxad 144,v144,10\n"
"xad 145,v145,10\nxad 146,v146,10\nxad 147,v147,10\nxad 148,v148,10\nxad 149,v149,10\nxad 150,v150,10\nxad 151,v151,10\nxmu 144,v144,152\n"
"xmu 145,v145,153\nxmu 146,v146,154\nxmu 147,v147,155\nxmu 148,v148,156\nxmu 149,v149,157\nxmu 150,v150,158\nxmu 151,v151,159\nxcv 160,144,145\n"
"xcv 161,146,147\nxcv 162,148,149\nxcv 163,150,151\ns_mov_b32 s68,0\nxst1 160,19,32,s68,0\ns_waitcnt vmcnt(3)\nv_lshlrev_b32 v152,16,v132\n"
"v_and_b32 v153,0xffff0000,v132\nv_lshlrev_b32 v154,16,v133\nv_and_b32 v155,0xffff0000,v133\nv_lshlrev_b32 v156,16,v134\n"
"v_and_b32 v157,0xffff0000,v134\nv_lshlrev_b32 v158,16,v135\nv_and_b32 v159,0xffff0000,v135\nxar 144,8\nxar 145,9\nxar 146,10\nxar 147,11\nxar 148,12\n"
"xar 149,13\nxar 150,14\nxar 151,15\nxmu 152,v152,15\nxmu 153,v153,15\nxmu 154,v154,15\nxmu 155,v155,15\nxmu 156,v156,15\nxmu 157,v157,15\n"
"xmu 158,v158,15\nxmu 159,v159,15\nxad 144,v144,11\nxad 145,v145,11\nxad 146,v146,11\nxad 147,v147,11\nxad 148,v148,11\nxad 149,v149,11\n"
"xad 150,v150,11\nxad 151,v151,11\nxmu 144,v144,152\nxmu 145,v145,153\nxmu 146,v146,154\nxmu 147,v147,155\nxmu 148,v148,156\nxmu 149,v149,157\n"
"xmu 150,v150,158\nxmu 151,v151,159\nxcv 164,144,145\nxcv 165,146,147\nxcv 166,148,149\nxcv 167,150,151\ns_mov_b32 s68,0x27000\nxst1 164,19,32,s68,0\n"
"s_waitcnt vmcnt(3)\nv_lshlrev_b32 v152,16,v136\nv_and_b32 v153,0xffff0000,v136\nv_lshlrev_b32 v154,16,v137\nv_and_b32 v155,0xffff0000,v137\n"
"v_lshlrev_b32 v156,16,v138\nv_and_b32 v157,0xffff0000,v138\nv_lshlrev_b32 v158,16,v139\nv_and_b32 v159,0xffff0000,v139\nxar 144,16\nxar 145,17\n"
"xar 146,18\nxar 147,19\nxar 148,20\nxar 149,21\nxar 150,22\nxar 151,23\nxmu 152,v152,16\nxmu 153,v153,16\nxmu 154,v154,16\nxmu 155,v155,16\n"
"xmu 156,v156,16\nxmu 157,v157,16\nxmu 158,v158,16\nxmu 159,v159,16\nxad 144,v144,12\nxad 145,v145,12\nxad 146,v146,12\nxad 147,v147,12\n"
"xad 148,v148,12\nxad 149,v149,12\nxad 150,v150,12\nxad 151,v151,12\nxmu 144,v144,152\nxmu 145,v145,153\nxmu 146,v146,154\nxmu 147,v147,155\n"
"xmu 148,v148,156\nxmu 149,v149,157\nxmu 150,v150,158\nxmu 151,v151,159\nxcv 160,144,145\nxcv 161,146,147\nxcv 162,148,149\nxcv 163,150,151\n"
"s_mov_b32 s68,0x4e000\nxst1 160,19,32,s68,0\ns_waitcnt vmcnt(3)\nv_lshlrev_b32 v152,16,v140\nv_and_b32 v153,0xffff0000,v140\n"
"v_lshlrev_b32 v154,16,v141\nv_and_b32 v155,0xffff0000,v141\nv_lshlrev_b32 v156,16,v142\nv_and_b32 v157,0xffff0000,v142\nv_lshlrev_b32 v158,16,v143\n"
"v_and_b32 v159,0xffff0000,v143\nxar 144,24\nxar 145,25\nxar 146,26\nxar 147,27\nxar 148,28\nxar 149,29\nxar 150,30\nxar 151,31\nxmu 152,v152,17\n"
"xmu 153,v153,17\nxmu 154,v154,17\nxmu 155,v155,17\nxmu 156,v156,17\nxmu 157,v157,17\nxmu 158,v158,17\nxmu 159,v159,17\nxad 144,v144,13\n"
"xad 145,v145,13\nxad 146,v146,13\nxad 147,v147,13\nxad 148,v148,13\nxad 149,v149,13\nxad 150,v150,13\nxad 151,v151,13\nxmu 144,v144,152\n"
"xmu 145,v145,153\nxmu 146,v146,154\nxmu 147,v147,155\nxmu 148,v148,156\nxmu 149,v149,157\nxmu 150,v150,158\nxmu 151,v151,159\nxcv 164,144,145\n"
"xcv 165,146,147\nxcv 166,148,149\nxcv 167,150,151\ns_mov_b32 s68,0x75000\nxst1 164,19,32,s68,0\ns_branch Latt_38_%=\nLnosp_37_%=:\ns_nop 4\n"
"v_mbcnt_lo_u32_b32 v48,-1,0\nv_mbcnt_hi_u32_b32 v48,-1,v48\ns_lshr_b32 s86,s100,16\ns_and_b32 s86,s86,1\ns_cmp_lg_u32 s86,0\n"
"s_cselect_b32 s87,0x1000000,0\ns_lshr_b32 s86,s100,12\ns_and_b32 s86,s86,15\ns_lshl_b32 s86,s86,20\ns_add_u32 s87,s87,s86\ns_lshr_b32 s86,s100,8\n"
"s_and_b32 s86,s86,15\ns_lshl_b32 s86,s86,17\ns_add_u32 s87,s87,s86\ns_add_u32 s40,s4,s87\ns_addc_u32 s41,s5,0\ns_and_b32 s41,s41,0xffff\n"
"s_lshr_b32 s42,s100,8\ns_and_b32 s42,s42,15\ns_lshl_b32 s42,s42,17\ns_sub_u32 s42,0x100000,s42\ns_mov_b32 s43,0x20000\ns_lshr_b32 s87,s100,16\n"
"s_and_b32 s87,s87,1\ns_mov_b32 s86,0x1000\ns_cmp_lg_u32 s87,0\ns_cselect_b32 s74,s86,0x2000\ns_lshr_b32 s86,s100,16\ns_and_b32 s86,s86,1\n"
"s_mov_b32 s87,0x2000\ns_cmp_lg_u32 s86,0\ns_cselect_b32 s75,s87,0x400\nv_lshrrev_b32 v30,4,v48\nv_lshrrev_b32 v30,1,v30\nv_mul_u32_u24 v30,s75,v30\n"
"s_lshr_b32 s87,s3,1\ns_lshl_b32 s87,s87,16\ns_and_b32 s86,s3,1\ns_mul_i32 s86,s86,s74\ns_add_u32 s87,s87,s86\nv_add_u32 v30,s87,v30\n"
"v_lshrrev_b32 v47,4,v48\nv_and_b32 v47,1,v47\nv_lshlrev_b32 v47,9,v47\nv_add_u32 v30,v30,v47\nv_and_b32 v47,15,v48\nv_lshlrev_b32 v47,4,v47\n"
"v_add_u32 v30,v30,v47\ns_add_u32 s44,s4,0x6c00000\ns_addc_u32 s45,s5,0\ns_and_b32 s45,s45,0xffff\ns_mov_b32 s46,0x8000\ns_mov_b32 s47,0x20000\n"
"s_lshr_b32 s52,s100,12\ns_and_b32 s52,s52,15\ns_lshl_b32 s52,s52,4\ns_lshr_b32 s87,s100,16\ns_and_b32 s87,s87,1\ns_lshl_b32 s87,s87,3\n"
"s_add_u32 s52,s52,s87\ns_lshr_b32 s87,s100,8\ns_and_b32 s87,s87,15\ns_add_u32 s52,s52,s87\ns_lshl_b32 s52,s52,3\ns_add_u32 s52,0x3000,s52\n"
"s_lshr_b32 s87,s100,16\ns_and_b32 s87,s87,1\ns_mov_b32 s86,0x400\ns_cmp_lg_u32 s87,0\ns_cselect_b32 s54,s86,0x800\ns_mov_b32 s96,0\n"
"s_mov_b32 s97,s54\ns_lshl_b32 s98,s54,1\ns_add_u32 s99,s98,s54\nxst1 128,30,40,s96,0\nxst1 132,30,40,s96,256\nxst1 136,30,40,s97,0\n"
"xst1 140,30,40,s97,256\nxst1 144,30,40,s98,0\nxst1 148,30,40,s98,256\nxst1 152,30,40,s99,0\nxst1 156,30,40,s99,256\ns_add_u32 s68,s96,0x4000\n"
"xst1 160,30,40,s68,0\ns_add_u32 s68,s96,0x4000\nxst1 164,30,40,s68,256\ns_add_u32 s68,s97,0x4000\nxst1 168,30,40,s68,0\ns_add_u32 s68,s97,0x4000\n"
"xst1 172,30,40,s68,256\ns_add_u32 s68,s98,0x4000\nxst1 176,30,40,s68,0\ns_add_u32 s68,s98,0x4000\nxst1 180,30,40,s68,256\ns_add_u32 s68,s99,0x4000\n"
"xst1 184,30,40,s68,0\ns_add_u32 s68,s99,0x4000\nxst1 188,30,40,s68,256\ns_add_u32 s68,s96,0x8000\nxst1 192,30,40,s68,0\ns_add_u32 s68,s96,0x8000\n"
"xst1 196,30,40,s68,256\ns_add_u32 s68,s97,0x8000\nxst1 200,30,40,s68,0\ns_add_u32 s68,s97,0x8000\nxst1 204,30,40,s68,256\ns_add_u32 s68,s98,0x8000\n"
"xst1 208,30,40,s68,0\ns_add_u32 s68,s98,0x8000\nxst1 212,30,40,s68,256\ns_add_u32 s68,s99,0x8000\nxst1 216,30,40,s68,0\ns_add_u32 s68,s99,0x8000\n"
"xst1 220,30,40,s68,256\ns_add_u32 s68,s96,0xc000\nxst1 224,30,40,s68,0\ns_add_u32 s68,s96,0xc000\nxst1 228,30,40,s68,256\ns_add_u32 s68,s97,0xc000\n"
"xst1 232,30,40,s68,0\ns_add_u32 s68,s97,0xc000\nxst1 236,30,40,s68,256\ns_add_u32 s68,s98,0xc000\nxst1 240,30,40,s68,0\ns_add_u32 s68,s98,0xc000\n"
"xst1 244,30,40,s68,256\ns_add_u32 s68,s99,0xc000\nxst1 248,30,40,s68,0\ns_add_u32 s68,s99,0xc000\nxst1 252,30,40,s68,256\ns_waitcnt vmcnt(0)\n"
"s_barrier\ns_cmp_eq_u32 s3,0\ns_cbranch_scc0 Lnosig_42_%=\nv_mov_b32 v24,s6\nv_mov_b32 v25,s7\nv_mov_b32 v26,0\ns_mov_b64 exec,1\n"
"buffer_store_dwordx2 v[24:25],v26,s[44:47],s52 offen offset:0 sc1\ns_mov_b64 exec,-1\nLnosig_42_%=:\ns_nop 1\nLatt_38_%=:\ns_mov_b32 s101,0\n"
"Lseg_43_%=:\ns_nop 4\nv_mbcnt_lo_u32_b32 v252,-1,0\nv_mbcnt_hi_u32_b32 v252,-1,v252\ns_and_b32 s95,s100,15\ns_lshl_b32 s95,s95,20\n"
"s_add_u32 s16,s4,s95\ns_addc_u32 s17,s5,0\ns_and_b32 s17,s17,0xffff\ns_mov_b32 s18,0x100000\ns_mov_b32 s19,0x20000\ns_and_b32 s95,s100,15\n"
"s_lshl_b32 s95,s95,20\ns_add_u32 s95,0x1000000,s95\ns_add_u32 s20,s4,s95\ns_addc_u32 s21,s5,0\ns_and_b32 s21,s21,0xffff\ns_mov_b32 s22,0x100000\n"
"s_mov_b32 s23,0x20000\ns_lshr_b32 s95,s100,25\ns_and_b32 s95,s95,7\ns_lshr_b32 s94,s100,25\ns_and_b32 s94,s94,7\ns_sub_u32 s94,15,s94\n"
"s_cmp_lg_u32 s101,0\ns_cselect_b32 s82,s95,s94\ns_and_b32 s94,s100,15\ns_lshl_b32 s94,s94,20\ns_add_u32 s94,0x2000000,s94\ns_lshl_b32 s95,s82,16\n"
"s_add_u32 s94,s94,s95\ns_add_u32 s24,s4,s94\ns_addc_u32 s25,s5,0\ns_and_b32 s25,s25,0xffff\ns_mov_b32 s26,0x10000\ns_mov_b32 s27,0x20000\n"
"s_lshr_b32 s94,s100,25\ns_and_b32 s94,s94,7\ns_add_u32 s94,s94,1\ns_lshl_b32 s94,s94,1\ns_lshr_b32 s93,s100,25\ns_and_b32 s93,s93,7\n"
"s_lshl_b32 s93,s93,1\ns_sub_u32 s93,15,s93\ns_lshr_b32 s92,s100,28\ns_and_b32 s92,s92,1\ns_cmp_lg_u32 s92,0\ns_cselect_b32 s95,17,s93\n"
"s_cmp_lg_u32 s101,0\ns_cselect_b32 s36,s94,s95\ns_lshr_b32 s95,s100,25\ns_and_b32 s95,s95,7\ns_lshl_b32 s95,s95,1\ns_sub_u32 s95,15,s95\n"
"s_lshr_b32 s94,s100,28\ns_and_b32 s94,s94,1\ns_cmp_lg_u32 s94,0\ns_cselect_b32 s37,s95,0\ns_lshr_b32 s94,s100,28\ns_and_b32 s94,s94,1\n"
"s_or_b32 s94,s101,s94\ns_cmp_lg_u32 s94,0\ns_cselect_b32 s38,0,0x80000000\ns_mov_b32 s39,s3\ns_and_b32 s94,s100,15\ns_lshl_b32 s94,s94,7\n"
"s_add_u32 s94,0x3000,s94\ns_cmp_lg_u32 s101,0\ns_cselect_b32 s74,0,s94\ns_lshl_b32 s75,s82,4\ns_and_b32 s94,s100,15\ns_add_u32 s75,s75,s94\n"
"s_lshl_b32 s75,s75,3\ns_add_u32 s75,0x1000,s75\ns_add_u32 s76,s4,0x6c00000\ns_addc_u32 s77,s5,0\ns_and_b32 s77,s77,0xffff\ns_mov_b32 s78,0x8000\n"
"s_mov_b32 s79,0x20000\ns_lshr_b32 s94,s100,17\ns_and_b32 s94,s94,15\ns_lshl_b32 s94,s94,4\ns_lshr_b32 s95,s100,21\ns_and_b32 s95,s95,15\n"
"s_add_u32 s94,s94,s95\ns_lshl_b32 s94,s94,3\ns_add_u32 s94,0x5000,s94\ns_xor_b32 s95,s101,1\ns_lshr_b32 s92,s100,29\ns_and_b32 s92,s92,1\n"
"s_and_b32 s95,s95,s92\ns_cmp_lg_u32 s95,0\ns_cselect_b32 s80,s94,0\nv_lshlrev_b32 v0,4,v252\nv_lshlrev_b32 v3,4,v252\ns_lshl_b32 s95,s3,13\n"
"v_add_u32 v3,s95,v3\nv_lshlrev_b32 v4,4,v252\ns_lshl_b32 s95,s3,12\nv_add_u32 v4,s95,v4\nv_lshlrev_b32 v5,4,v252\nv_add_u32 v5,0x10000,v5\n"
"v_and_b32 v11,31,v252\ns_lshl_b32 s95,s3,5\nv_add_u32 v11,s95,v11\nv_lshrrev_b32 v247,5,v252\nv_lshlrev_b32 v247,2,v247\nv_sub_u32 v11,v11,v247\n"
"v_lshlrev_b32 v15,4,v252\ns_lshl_b32 s95,s3,14\nv_add_u32 v15,s95,v15\ns_cmp_eq_u32 s74,0\ns_cbranch_scc1 Lnowait_48_%=\ns_mov_b32 s53,0x30d40\n"
"v_mbcnt_lo_u32_b32 v218,-1,0\nv_mbcnt_hi_u32_b32 v218,-1,v218\nv_lshlrev_b32 v218,3,v218\n"
"buffer_load_dwordx2 v[216:217],v218,s[76:79],s74 offen offset:0 sc1\nLspin_49_%=:\ns_waitcnt vmcnt(0)\nv_cmp_eq_u64 vcc,s[6:7],v[216:217]\ns_nop 3\n"
"s_and_b32 s52,vcc_lo,0xffff\ns_cmp_eq_u32 s52,0xffff\ns_cbranch_scc1 Lspun_50_%=\ns_sleep 2\n"
"buffer_load_dwordx2 v[216:217],v218,s[76:79],s74 offen offset:0 sc1\ns_sub_u32 s53,s53,1\ns_cmp_gt_i32 s53,0\ns_cbranch_scc1 Lspin_49_%=\n"
"s_waitcnt vmcnt(0)\nLspun_50_%=:\ns_nop 1\ns_mov_b32 s53,0x30d40\nv_mbcnt_lo_u32_b32 v218,-1,0\nv_mbcnt_hi_u32_b32 v218,-1,v218\n"
"v_lshlrev_b32 v218,3,v218\nbuffer_load_dwordx2 v[216:217],v218,s[76:79],s75 offen offset:0 sc1\nLspin_51_%=:\ns_waitcnt vmcnt(0)\n"
"v_cmp_eq_u64 vcc,s[6:7],v[216:217]\ns_nop 3\ns_and_b32 s52,vcc_lo,1\ns_cmp_eq_u32 s52,1\ns_cbranch_scc1 Lspun_52_%=\ns_sleep 2\n"
"buffer_load_dwordx2 v[216:217],v218,s[76:79],s75 offen offset:0 sc1\ns_sub_u32 s53,s53,1\ns_cmp_gt_i32 s53,0\ns_cbranch_scc1 Lspin_51_%=\n"
"s_waitcnt vmcnt(0)\nLspun_52_%=:\ns_nop 1\nLnowait_48_%=:\ns_nop 1\ns_barrier\ns_lshl_b32 s54,s39,13\ns_lshl_b32 s55,s39,12\n"
"s_add_u32 s55,s55,0x10000\ns_lshl_b32 s64,s37,15\ns_mov_b32 s65,s64\ns_mov_b32 s50,0\ns_mov_b32 s42,s54\ns_mov_b32 s45,0\ns_add_u32 s68,s50,0\n"
"s_cmp_lt_u32 s68,s36\ns_cselect_b32 s40,s64,0x40000000\ns_add_u32 s41,s40,0x1000\ns_add_u32 s64,s64,0x8000\ns_mov_b32 m0,s42\ns_nop 0\n"
"xdl1 3,16,s40,0\ns_nop 0\nxdl1 3,16,s40,1024\ns_nop 0\nxdl1 3,16,s40,2048\ns_nop 0\nxdl1 3,16,s40,3072\nxam 42,0x1000\ns_nop 0\nxdl1 3,16,s41,0\n"
"s_nop 0\nxdl1 3,16,s41,1024\ns_nop 0\nxdl1 3,16,s41,2048\ns_nop 0\nxdl1 3,16,s41,3072\ns_xor_b32 s42,s42,0x8000\ns_add_u32 s68,s50,1\n"
"s_cmp_lt_u32 s68,s36\ns_cselect_b32 s40,s64,0x40000000\ns_add_u32 s41,s40,0x1000\ns_add_u32 s64,s64,0x8000\ns_mov_b32 m0,s42\ns_nop 0\n"
"xdl1 3,16,s40,0\ns_nop 0\nxdl1 3,16,s40,1024\ns_nop 0\nxdl1 3,16,s40,2048\ns_nop 0\nxdl1 3,16,s40,3072\nxam 42,0x1000\ns_nop 0\nxdl1 3,16,s41,0\n"
"s_nop 0\nxdl1 3,16,s41,1024\ns_nop 0\nxdl1 3,16,s41,2048\ns_nop 0\nxdl1 3,16,s41,3072\ns_xor_b32 s42,s42,0x8000\ns_add_u32 s69,s50,0\n"
"s_cmp_lt_u32 s69,s36\ns_cselect_b32 s43,s65,0x40000000\ns_add_u32 s44,s43,0x4000\ns_add_u32 s65,s65,0x8000\ns_add_u32 s46,s45,s55\n"
"s_add_u32 s45,s45,0x4000\ns_cmp_ge_u32 s45,0x14000\ns_cselect_b32 s69,0x14000,0\ns_sub_u32 s45,s45,s69\ns_add_u32 s47,s45,s55\n"
"s_add_u32 s45,s45,0x4000\ns_cmp_ge_u32 s45,0x14000\ns_cselect_b32 s69,0x14000,0\ns_sub_u32 s45,s45,s69\ns_mov_b32 m0,s46\ns_nop 0\nxdl1 4,20,s43,0\n"
"s_nop 0\nxdl1 4,20,s43,1024\ns_nop 0\nxdl1 4,20,s43,2048\ns_nop 0\nxdl1 4,20,s43,3072\ns_mov_b32 m0,s47\ns_nop 0\nxdl1 4,20,s44,0\ns_nop 0\n"
"xdl1 4,20,s44,1024\ns_nop 0\nxdl1 4,20,s44,2048\ns_nop 0\nxdl1 4,20,s44,3072\ns_mov_b32 s52,0\n"
"buffer_load_dwordx4 v[16:19],v15,s[24:27],s52 offen offset:0 sc1\nbuffer_load_dwordx4 v[20:23],v15,s[24:27],s52 offen offset:1024 sc1\n"
"buffer_load_dwordx4 v[24:27],v15,s[24:27],s52 offen offset:2048 sc1\nbuffer_load_dwordx4 v[28:31],v15,s[24:27],s52 offen offset:3072 sc1\n"
"s_mov_b32 s52,0x1000\nbuffer_load_dwordx4 v[32:35],v15,s[24:27],s52 offen offset:0 sc1\n"
"buffer_load_dwordx4 v[36:39],v15,s[24:27],s52 offen offset:1024 sc1\nbuffer_load_dwordx4 v[40:43],v15,s[24:27],s52 offen offset:2048 sc1\n"
"buffer_load_dwordx4 v[44:47],v15,s[24:27],s52 offen offset:3072 sc1\ns_mov_b32 s52,0x2000\n"
"buffer_load_dwordx4 v[48:51],v15,s[24:27],s52 offen offset:0 sc1\nbuffer_load_dwordx4 v[52:55],v15,s[24:27],s52 offen offset:1024 sc1\n"
"buffer_load_dwordx4 v[56:59],v15,s[24:27],s52 offen offset:2048 sc1\nbuffer_load_dwordx4 v[60:63],v15,s[24:27],s52 offen offset:3072 sc1\n"
"s_mov_b32 s52,0x3000\nbuffer_load_dwordx4 v[64:67],v15,s[24:27],s52 offen offset:0 sc1\n"
"buffer_load_dwordx4 v[68:71],v15,s[24:27],s52 offen offset:1024 sc1\nbuffer_load_dwordx4 v[72:75],v15,s[24:27],s52 offen offset:2048 sc1\n"
"buffer_load_dwordx4 v[76:79],v15,s[24:27],s52 offen offset:3072 sc1\ns_mov_b32 s48,0\ns_mov_b32 s49,0x4000\nv_mov_b32 v1,v5\n"
"v_mov_b32 v12,0xff800000\nv_mov_b32 v6,0\nv_mov_b32 v7,0\nv_mov_b32 v8,0\nv_mov_b32 v192,0\nv_mov_b32 v193,0\nv_mov_b32 v194,0\nv_mov_b32 v195,0\n"
"v_mov_b32 v196,0\nv_mov_b32 v197,0\nv_mov_b32 v198,0\nv_mov_b32 v199,0\nv_mov_b32 v200,0\nv_mov_b32 v201,0\nv_mov_b32 v202,0\nv_mov_b32 v203,0\n"
"v_mov_b32 v204,0\nv_mov_b32 v205,0\nv_mov_b32 v206,0\nv_mov_b32 v207,0\nv_accvgpr_write_b32 a0,0\nv_accvgpr_write_b32 a1,0\nv_accvgpr_write_b32 a2,0\n"
"v_accvgpr_write_b32 a3,0\nv_accvgpr_write_b32 a4,0\nv_accvgpr_write_b32 a5,0\nv_accvgpr_write_b32 a6,0\nv_accvgpr_write_b32 a7,0\n"
"v_accvgpr_write_b32 a8,0\nv_accvgpr_write_b32 a9,0\nv_accvgpr_write_b32 a10,0\nv_accvgpr_write_b32 a11,0\nv_accvgpr_write_b32 a12,0\n"
"v_accvgpr_write_b32 a13,0\nv_accvgpr_write_b32 a14,0\nv_accvgpr_write_b32 a15,0\nv_accvgpr_write_b32 a16,0\nv_accvgpr_write_b32 a17,0\n"
"v_accvgpr_write_b32 a18,0\nv_accvgpr_write_b32 a19,0\nv_accvgpr_write_b32 a20,0\nv_accvgpr_write_b32 a21,0\nv_accvgpr_write_b32 a22,0\n"
"v_accvgpr_write_b32 a23,0\nv_accvgpr_write_b32 a24,0\nv_accvgpr_write_b32 a25,0\nv_accvgpr_write_b32 a26,0\nv_accvgpr_write_b32 a27,0\n"
"v_accvgpr_write_b32 a28,0\nv_accvgpr_write_b32 a29,0\nv_accvgpr_write_b32 a30,0\nv_accvgpr_write_b32 a31,0\nv_accvgpr_write_b32 a32,0\n"
"v_accvgpr_write_b32 a33,0\nv_accvgpr_write_b32 a34,0\nv_accvgpr_write_b32 a35,0\nv_accvgpr_write_b32 a36,0\nv_accvgpr_write_b32 a37,0\n"
"v_accvgpr_write_b32 a38,0\nv_accvgpr_write_b32 a39,0\nv_accvgpr_write_b32 a40,0\nv_accvgpr_write_b32 a41,0\nv_accvgpr_write_b32 a42,0\n"
"v_accvgpr_write_b32 a43,0\nv_accvgpr_write_b32 a44,0\nv_accvgpr_write_b32 a45,0\nv_accvgpr_write_b32 a46,0\nv_accvgpr_write_b32 a47,0\n"
"v_accvgpr_write_b32 a48,0\nv_accvgpr_write_b32 a49,0\nv_accvgpr_write_b32 a50,0\nv_accvgpr_write_b32 a51,0\nv_accvgpr_write_b32 a52,0\n"
"v_accvgpr_write_b32 a53,0\nv_accvgpr_write_b32 a54,0\nv_accvgpr_write_b32 a55,0\nv_accvgpr_write_b32 a56,0\nv_accvgpr_write_b32 a57,0\n"
"v_accvgpr_write_b32 a58,0\nv_accvgpr_write_b32 a59,0\nv_accvgpr_write_b32 a60,0\nv_accvgpr_write_b32 a61,0\nv_accvgpr_write_b32 a62,0\n"
"v_accvgpr_write_b32 a63,0\nv_accvgpr_write_b32 a64,0\nv_accvgpr_write_b32 a65,0\nv_accvgpr_write_b32 a66,0\nv_accvgpr_write_b32 a67,0\n"
"v_accvgpr_write_b32 a68,0\nv_accvgpr_write_b32 a69,0\nv_accvgpr_write_b32 a70,0\nv_accvgpr_write_b32 a71,0\nv_accvgpr_write_b32 a72,0\n"
"v_accvgpr_write_b32 a73,0\nv_accvgpr_write_b32 a74,0\nv_accvgpr_write_b32 a75,0\nv_accvgpr_write_b32 a76,0\nv_accvgpr_write_b32 a77,0\n"
"v_accvgpr_write_b32 a78,0\nv_accvgpr_write_b32 a79,0\nv_accvgpr_write_b32 a80,0\nv_accvgpr_write_b32 a81,0\nv_accvgpr_write_b32 a82,0\n"
"v_accvgpr_write_b32 a83,0\nv_accvgpr_write_b32 a84,0\nv_accvgpr_write_b32 a85,0\nv_accvgpr_write_b32 a86,0\nv_accvgpr_write_b32 a87,0\n"
"v_accvgpr_write_b32 a88,0\nv_accvgpr_write_b32 a89,0\nv_accvgpr_write_b32 a90,0\nv_accvgpr_write_b32 a91,0\nv_accvgpr_write_b32 a92,0\n"
"v_accvgpr_write_b32 a93,0\nv_accvgpr_write_b32 a94,0\nv_accvgpr_write_b32 a95,0\nv_accvgpr_write_b32 a96,0\nv_accvgpr_write_b32 a97,0\n"
"v_accvgpr_write_b32 a98,0\nv_accvgpr_write_b32 a99,0\nv_accvgpr_write_b32 a100,0\nv_accvgpr_write_b32 a101,0\nv_accvgpr_write_b32 a102,0\n"
"v_accvgpr_write_b32 a103,0\nv_accvgpr_write_b32 a104,0\nv_accvgpr_write_b32 a105,0\nv_accvgpr_write_b32 a106,0\nv_accvgpr_write_b32 a107,0\n"
"v_accvgpr_write_b32 a108,0\nv_accvgpr_write_b32 a109,0\nv_accvgpr_write_b32 a110,0\nv_accvgpr_write_b32 a111,0\nv_accvgpr_write_b32 a112,0\n"
"v_accvgpr_write_b32 a113,0\nv_accvgpr_write_b32 a114,0\nv_accvgpr_write_b32 a115,0\nv_accvgpr_write_b32 a116,0\nv_accvgpr_write_b32 a117,0\n"
"v_accvgpr_write_b32 a118,0\nv_accvgpr_write_b32 a119,0\nv_accvgpr_write_b32 a120,0\nv_accvgpr_write_b32 a121,0\nv_accvgpr_write_b32 a122,0\n"
"v_accvgpr_write_b32 a123,0\nv_accvgpr_write_b32 a124,0\nv_accvgpr_write_b32 a125,0\nv_accvgpr_write_b32 a126,0\nv_accvgpr_write_b32 a127,0\n"
"s_sub_u32 s51,s36,1\ns_waitcnt vmcnt(0)\ns_barrier\nxrb 160,0,0\nxrb 164,0,16384\nxrb 168,0,1024\nxrb 172,0,17408\nxrb 176,0,2048\nxrb 180,0,18432\n"
"xwl 5\nxm32v 80,160,16,192\nxrb 184,0,3072\nxwl 5\nxm32v 96,164,16,192\nxrb 188,0,19456\nxwl 5\nxm32v 80,168,20,80\nxrb 160,0,4096\nxwl 5\n"
"xm32v 96,172,20,96\nxrb 164,0,20480\nxwl 5\nxm32v 80,176,24,80\nxrb 168,0,5120\nxwl 5\nxm32v 96,180,24,96\nxrb 172,0,21504\nxwl 5\n"
"xm32v 80,184,28,80\nxrb 176,0,6144\nxwl 5\nxm32v 96,188,28,96\nxrb 180,0,22528\nxwl 5\nxm32v 80,160,32,80\nxrb 184,0,7168\nxwl 5\nxm32v 96,164,32,96\n"
"xrb 188,0,23552\nxwl 5\nxm32v 80,168,36,80\nxrb 160,0,8192\nxwl 5\nxm32v 96,172,36,96\nxrb 164,0,24576\nxwl 5\nxm32v 80,176,40,80\nxrb 168,0,9216\n"
"xwl 5\nxm32v 96,180,40,96\nxrb 172,0,25600\nxwl 5\nxm32v 80,184,44,80\nxrb 176,0,10240\nxwl 5\nxm32v 96,188,44,96\nxrb 180,0,26624\nxwl 5\n"
"xm32v 80,160,48,80\nxrb 184,0,11264\nxwl 5\nxm32v 96,164,48,96\nxrb 188,0,27648\nxwl 5\nxm32v 80,168,52,80\nxrb 160,0,12288\nxwl 5\n"
"xm32v 96,172,52,96\nxrb 164,0,28672\nxwl 5\nxm32v 80,176,56,80\nxrb 168,0,13312\nxwl 5\nxm32v 96,180,56,96\nxrb 172,0,29696\nxwl 5\n"
"xm32v 80,184,60,80\nxrb 176,0,14336\nxwl 5\nxm32v 96,188,60,96\nxrb 180,0,30720\nxwl 5\nxm32v 80,160,64,80\nxrb 184,0,15360\nxwl 5\n"
"xm32v 96,164,64,96\nxrb 188,0,31744\nxwl 5\nxm32v 80,168,68,80\nxwl 4\nxm32v 96,172,68,96\nxwl 3\nxm32v 80,176,72,80\nxwl 2\nxm32v 96,180,72,96\n"
"xwl 1\nxm32v 80,184,76,80\nxwl 0\nxm32v 96,188,76,96\nxwl 0\ns_barrier\ns_cmp_eq_u32 s80,0\ns_cbranch_scc1 Lnousig_53_%=\ns_cmp_eq_u32 s39,0\n"
"s_cbranch_scc0 Lnosig_54_%=\nv_mov_b32 v216,s6\nv_mov_b32 v217,s7\nv_mov_b32 v219,0\ns_mov_b64 exec,1\n"
"buffer_store_dwordx2 v[216:217],v219,s[76:79],s80 offen offset:0 sc1\ns_mov_b64 exec,-1\nLnosig_54_%=:\ns_nop 1\nLnousig_53_%=:\ns_nop 1\n"
"s_add_u32 s68,s50,2\ns_cmp_lt_u32 s68,s36\ns_cselect_b32 s40,s64,0x40000000\ns_add_u32 s41,s40,0x1000\ns_add_u32 s64,s64,0x8000\ns_mov_b32 m0,s42\n"
"s_nop 0\nxdl1 3,16,s40,0\ns_nop 0\nxdl1 3,16,s40,1024\ns_nop 0\nxdl1 3,16,s40,2048\ns_nop 0\nxdl1 3,16,s40,3072\nxam 42,0x1000\ns_nop 0\n"
"xdl1 3,16,s41,0\ns_nop 0\nxdl1 3,16,s41,1024\ns_nop 0\nxdl1 3,16,s41,2048\ns_nop 0\nxdl1 3,16,s41,3072\ns_xor_b32 s42,s42,0x8000\n"
"s_add_u32 s69,s50,1\ns_cmp_lt_u32 s69,s36\ns_cselect_b32 s43,s65,0x40000000\ns_add_u32 s44,s43,0x4000\ns_add_u32 s65,s65,0x8000\n"
"s_add_u32 s46,s45,s55\ns_add_u32 s45,s45,0x4000\ns_cmp_ge_u32 s45,0x14000\ns_cselect_b32 s69,0x14000,0\ns_sub_u32 s45,s45,s69\ns_add_u32 s47,s45,s55\n"
"s_add_u32 s45,s45,0x4000\ns_cmp_ge_u32 s45,0x14000\ns_cselect_b32 s69,0x14000,0\ns_sub_u32 s45,s45,s69\nv_xor_b32 v0,0x8000,v0\ns_add_u32 s67,s50,2\n"
"s_sub_i32 s67,s67,s36\ns_or_b32 s67,s67,s38\ns_cmp_lt_i32 s67,0\ns_cbranch_scc1 Lnomask0_55_%=\ns_lshl_b32 s53,s67,6\nv_subrev_u32 v13,s53,v11\n"
"v_cmp_gt_i32 s[56:57],0,v13\nv_cmp_gt_i32 s[58:59],1,v13\nv_cmp_gt_i32 s[60:61],2,v13\nv_cndmask_b32 v80,v80,v12,s[56:57]\n"
"v_cmp_gt_i32 s[62:63],3,v13\nv_cndmask_b32 v81,v81,v12,s[58:59]\nv_cmp_gt_i32 s[56:57],8,v13\nv_cndmask_b32 v82,v82,v12,s[60:61]\n"
"v_cmp_gt_i32 s[58:59],9,v13\nv_cndmask_b32 v83,v83,v12,s[62:63]\nv_cmp_gt_i32 s[60:61],10,v13\nv_cndmask_b32 v84,v84,v12,s[56:57]\n"
"v_cmp_gt_i32 s[62:63],11,v13\nv_cndmask_b32 v85,v85,v12,s[58:59]\nv_cmp_gt_i32 s[56:57],16,v13\nv_cndmask_b32 v86,v86,v12,s[60:61]\n"
"v_cmp_gt_i32 s[58:59],17,v13\nv_cndmask_b32 v87,v87,v12,s[62:63]\nv_cmp_gt_i32 s[60:61],18,v13\nv_cndmask_b32 v88,v88,v12,s[56:57]\n"
"v_cmp_gt_i32 s[62:63],19,v13\nv_cndmask_b32 v89,v89,v12,s[58:59]\nv_cmp_gt_i32 s[56:57],24,v13\nv_cndmask_b32 v90,v90,v12,s[60:61]\n"
"v_cmp_gt_i32 s[58:59],25,v13\nv_cndmask_b32 v91,v91,v12,s[62:63]\nv_cmp_gt_i32 s[60:61],26,v13\nv_cndmask_b32 v92,v92,v12,s[56:57]\n"
"v_cmp_gt_i32 s[62:63],27,v13\nv_cndmask_b32 v93,v93,v12,s[58:59]\nv_cmp_gt_i32 s[56:57],32,v13\nv_cndmask_b32 v94,v94,v12,s[60:61]\n"
"v_cmp_gt_i32 s[58:59],33,v13\nv_cndmask_b32 v95,v95,v12,s[62:63]\nv_cmp_gt_i32 s[60:61],34,v13\nv_cndmask_b32 v96,v96,v12,s[56:57]\n"
"v_cmp_gt_i32 s[62:63],35,v13\nv_cndmask_b32 v97,v97,v12,s[58:59]\nv_cmp_gt_i32 s[56:57],40,v13\nv_cndmask_b32 v98,v98,v12,s[60:61]\n"
"v_cmp_gt_i32 s[58:59],41,v13\nv_cndmask_b32 v99,v99,v12,s[62:63]\nv_cmp_gt_i32 s[60:61],42,v13\nv_cndmask_b32 v100,v100,v12,s[56:57]\n"
"v_cmp_gt_i32 s[62:63],43,v13\nv_cndmask_b32 v101,v101,v12,s[58:59]\nv_cmp_gt_i32 s[56:57],48,v13\nv_cndmask_b32 v102,v102,v12,s[60:61]\n"
"v_cmp_gt_i32 s[58:59],49,v13\nv_cndmask_b32 v103,v103,v12,s[62:63]\nv_cmp_gt_i32 s[60:61],50,v13\nv_cndmask_b32 v104,v104,v12,s[56:57]\n"
"v_cmp_gt_i32 s[62:63],51,v13\nv_cndmask_b32 v105,v105,v12,s[58:59]\nv_cmp_gt_i32 s[56:57],56,v13\nv_cndmask_b32 v106,v106,v12,s[60:61]\n"
"v_cmp_gt_i32 s[58:59],57,v13\nv_cndmask_b32 v107,v107,v12,s[62:63]\nv_cmp_gt_i32 s[60:61],58,v13\nv_cndmask_b32 v108,v108,v12,s[56:57]\n"
"v_cmp_gt_i32 s[62:63],59,v13\nv_cndmask_b32 v109,v109,v12,s[58:59]\nv_cndmask_b32 v110,v110,v12,s[60:61]\nv_cndmask_b32 v111,v111,v12,s[62:63]\n"
"Lnomask0_55_%=:\nxx3 9,80,81,82\nxx3 9,9,83,84\nxx3 9,9,85,86\nxx3 9,9,87,88\nxx3 9,9,89,90\nxx3 9,9,91,92\nxx3 9,9,93,94\nv_max_f32 v9,v9,v95\n"
"xx3 10,96,97,98\nxx3 10,10,99,100\nxx3 10,10,101,102\nxx3 10,10,103,104\nxx3 10,10,105,106\nxx3 10,10,107,108\nxx3 10,10,109,110\n"
"v_max_f32 v10,v10,v111\nv_max_f32 v9,v9,v10\nv_mov_b32 v13,v9\nv_mov_b32 v14,v9\ns_nop 1\nv_permlane32_swap_b32 v13,v14\nv_max_f32 v13,v13,v14\n"
"v_sub_f32 v80,v80,v13\nv_sub_f32 v81,v81,v13\nv_sub_f32 v82,v82,v13\nv_sub_f32 v83,v83,v13\nv_sub_f32 v84,v84,v13\nv_sub_f32 v85,v85,v13\n"
"v_sub_f32 v86,v86,v13\nv_sub_f32 v87,v87,v13\nv_sub_f32 v88,v88,v13\nv_sub_f32 v89,v89,v13\nv_sub_f32 v90,v90,v13\nv_sub_f32 v91,v91,v13\n"
"v_sub_f32 v92,v92,v13\nv_sub_f32 v93,v93,v13\nv_sub_f32 v94,v94,v13\nv_sub_f32 v95,v95,v13\nv_sub_f32 v96,v96,v13\nv_sub_f32 v97,v97,v13\n"
"v_sub_f32 v98,v98,v13\nv_sub_f32 v99,v99,v13\nv_sub_f32 v100,v100,v13\nv_sub_f32 v101,v101,v13\nv_sub_f32 v102,v102,v13\nv_sub_f32 v103,v103,v13\n"
"v_sub_f32 v104,v104,v13\nv_sub_f32 v105,v105,v13\nv_sub_f32 v106,v106,v13\nv_sub_f32 v107,v107,v13\nv_sub_f32 v108,v108,v13\nv_sub_f32 v109,v109,v13\n"
"v_sub_f32 v110,v110,v13\nv_sub_f32 v111,v111,v13\nxad 6,v6,13\nv_sub_f32 v192,0,v6\nv_sub_f32 v193,0,v6\nv_sub_f32 v194,0,v6\nv_sub_f32 v195,0,v6\n"
"v_sub_f32 v196,0,v6\nv_sub_f32 v197,0,v6\nv_sub_f32 v198,0,v6\nv_sub_f32 v199,0,v6\nv_sub_f32 v200,0,v6\nv_sub_f32 v201,0,v6\nv_sub_f32 v202,0,v6\n"
"v_sub_f32 v203,0,v6\nv_sub_f32 v204,0,v6\nv_sub_f32 v205,0,v6\nv_sub_f32 v206,0,v6\nv_sub_f32 v207,0,v6\nxrb 160,0,0\nxrb 164,0,16384\n"
"xrb 168,0,1024\nxrb 172,0,17408\nxrb 176,0,2048\nxrb 180,0,18432\ns_cmp_eq_u32 s51,0\ns_cbranch_scc1 Ltail0_57_%=\nLbodyA_56_%=:\nxwl 4\n"
"xm32v 112,160,16,192\nxrb 184,0,3072\nxex 80,80\ns_mov_b32 m0,s46\nxm32v 128,164,16,192\nxdl1 4,20,s43,0\nxrb 188,0,19456\nxad 7,v80,7\nxwl 4\n"
"xm32v 112,168,20,112\nxrb 160,0,4096\nxex 81,81\nv_add_u32 v2,s49,v5\nxm32v 128,172,20,128\nxdl1 4,20,s43,1024\nxrb 164,0,20480\nxex 82,82\nxwl 4\n"
"xm32v 112,176,24,112\nxrb 168,0,5120\nxex 83,83\nxcv 144,80,81\nxm32v 128,180,24,128\nxdl1 4,20,s43,2048\nxrb 172,0,21504\nxex 84,84\nxwl 4\n"
"xm32v 112,184,28,112\nxrb 176,0,6144\nxex 85,85\nxcv 145,82,83\nxm32v 128,188,28,128\nxdl1 4,20,s43,3072\nxrb 180,0,22528\nxex 86,86\nxwl 4\n"
"xm32v 112,160,32,112\nxrb 184,0,7168\nxex 87,87\nxcv 146,84,85\ns_mov_b32 m0,s47\nxm32v 128,164,32,128\nxdl1 4,20,s44,0\nxrb 188,0,23552\n"
"xcv 147,86,87\nxwl 4\nxm32v 112,168,36,112\nxrb 160,0,8192\nxex 88,88\ns_add_u32 s49,s49,0x8000\nxm32v 128,172,36,128\nxdl1 4,20,s44,1024\n"
"xrb 164,0,24576\nxex 89,89\nxwl 4\nxm32v 112,176,40,112\nxrb 168,0,9216\nxex 90,90\nxcv 148,88,89\nxm32v 128,180,40,128\nxdl1 4,20,s44,2048\n"
"xrb 172,0,25600\nxex 91,91\nxwl 4\nxm32v 112,184,44,112\nxrb 176,0,10240\nxex 92,92\nxcv 149,90,91\nxm32v 128,188,44,128\nxdl1 4,20,s44,3072\n"
"xrb 180,0,26624\nxex 93,93\nxwl 4\nxm32v 112,160,48,112\nxrb 184,0,11264\nxex 94,94\nxcv 150,92,93\nxm32v 128,164,48,128\nxrb 188,0,27648\nxex 95,95\n"
"xad 8,v81,8\nxwl 4\nxm32v 112,168,52,112\nxrb 160,0,12288\nxcv 151,94,95\ns_cmp_ge_u32 s49,0x14000\ns_cselect_b32 s70,0x14000,0\n"
"xm32v 128,172,52,128\nxrb 164,0,28672\ns_sub_u32 s49,s49,s70\nxex 96,96\nxwl 4\nxm32v 112,176,56,112\nxrb 168,0,13312\ns_add_u32 s67,s50,3\n"
"s_sub_i32 s67,s67,s36\ns_or_b32 s67,s67,s38\nxm32v 128,180,56,128\nxrb 172,0,29696\nxex 97,97\nxad 7,v82,7\nxwl 4\nxm32v 112,184,60,112\n"
"xrb 176,0,14336\nxex 98,98\nxcv 152,96,97\nxm32v 128,188,60,128\nxrb 180,0,30720\nxex 99,99\nxad 8,v83,8\nxwl 4\nxm32v 112,160,64,112\n"
"xrb 184,0,15360\nxex 100,100\nxcv 153,98,99\nxm32v 128,164,64,128\nxrb 188,0,31744\nxex 101,101\nxad 7,v84,7\nxwl 4\nxm32v 112,168,68,112\n"
"xrb 160,1,0\nxex 102,102\nxcv 154,100,101\nxm32v 128,172,68,128\nxrb 164,1,1024\nxex 103,103\nxad 8,v85,8\nxwl 4\nxm32v 112,176,72,112\n"
"xrb 168,1,2048\nxcv 155,102,103\nxex 104,104\nxm32v 128,180,72,128\nxrb 172,1,3072\nxex 105,105\nxad 7,v86,7\nxwl 4\nxm32v 112,184,76,112\n"
"xrb 176,1,4096\ns_add_u32 s68,s50,3\ns_cmp_lt_u32 s68,s36\ns_cselect_b32 s40,s64,0x40000000\nxm32v 128,188,76,128\nxrb 180,1,5120\n"
"s_add_u32 s41,s40,0x1000\ns_add_u32 s64,s64,0x8000\nxcv 156,104,105\nxwl 4\nxm32 0,160,144\nxrb 184,1,6144\nxex 106,106\nv_xor_b32 v0,0x8000,v0\n"
"xm32 16,164,144\nxrb 188,1,7168\nxex 107,107\nxad 8,v87,8\nxwl 4\nxm32 32,168,144\nxrb 160,1,8192\nxex 108,108\nxcv 157,106,107\nxm32 48,172,144\n"
"xrb 164,1,9216\nxex 109,109\nxad 7,v88,7\nxwl 4\nxm32 64,176,144\nxrb 168,1,10240\nxex 110,110\nxcv 158,108,109\ns_cmp_lt_i32 s67,0\n"
"s_cbranch_scc1 Lnomask_60_%=\ns_lshl_b32 s53,s67,6\nv_subrev_u32 v13,s53,v11\nv_cmp_gt_i32 s[56:57],0,v13\nv_cmp_gt_i32 s[58:59],1,v13\n"
"v_cmp_gt_i32 s[60:61],2,v13\nv_cndmask_b32 v112,v112,v12,s[56:57]\nv_cmp_gt_i32 s[62:63],3,v13\nv_cndmask_b32 v113,v113,v12,s[58:59]\n"
"v_cmp_gt_i32 s[56:57],8,v13\nv_cndmask_b32 v114,v114,v12,s[60:61]\nv_cmp_gt_i32 s[58:59],9,v13\nv_cndmask_b32 v115,v115,v12,s[62:63]\n"
"v_cmp_gt_i32 s[60:61],10,v13\nv_cndmask_b32 v116,v116,v12,s[56:57]\nv_cmp_gt_i32 s[62:63],11,v13\nv_cndmask_b32 v117,v117,v12,s[58:59]\n"
"v_cmp_gt_i32 s[56:57],16,v13\nv_cndmask_b32 v118,v118,v12,s[60:61]\nv_cmp_gt_i32 s[58:59],17,v13\nv_cndmask_b32 v119,v119,v12,s[62:63]\n"
"v_cmp_gt_i32 s[60:61],18,v13\nv_cndmask_b32 v120,v120,v12,s[56:57]\nv_cmp_gt_i32 s[62:63],19,v13\nv_cndmask_b32 v121,v121,v12,s[58:59]\n"
"v_cmp_gt_i32 s[56:57],24,v13\nv_cndmask_b32 v122,v122,v12,s[60:61]\nv_cmp_gt_i32 s[58:59],25,v13\nv_cndmask_b32 v123,v123,v12,s[62:63]\n"
"v_cmp_gt_i32 s[60:61],26,v13\nv_cndmask_b32 v124,v124,v12,s[56:57]\nv_cmp_gt_i32 s[62:63],27,v13\nv_cndmask_b32 v125,v125,v12,s[58:59]\n"
"v_cmp_gt_i32 s[56:57],32,v13\nv_cndmask_b32 v126,v126,v12,s[60:61]\nv_cmp_gt_i32 s[58:59],33,v13\nv_cndmask_b32 v127,v127,v12,s[62:63]\n"
"v_cmp_gt_i32 s[60:61],34,v13\nv_cndmask_b32 v128,v128,v12,s[56:57]\nv_cmp_gt_i32 s[62:63],35,v13\nv_cndmask_b32 v129,v129,v12,s[58:59]\n"
"v_cmp_gt_i32 s[56:57],40,v13\nv_cndmask_b32 v130,v130,v12,s[60:61]\nv_cmp_gt_i32 s[58:59],41,v13\nv_cndmask_b32 v131,v131,v12,s[62:63]\n"
"v_cmp_gt_i32 s[60:61],42,v13\nv_cndmask_b32 v132,v132,v12,s[56:57]\nv_cmp_gt_i32 s[62:63],43,v13\nv_cndmask_b32 v133,v133,v12,s[58:59]\n"
"v_cmp_gt_i32 s[56:57],48,v13\nv_cndmask_b32 v134,v134,v12,s[60:61]\nv_cmp_gt_i32 s[58:59],49,v13\nv_cndmask_b32 v135,v135,v12,s[62:63]\n"
"v_cmp_gt_i32 s[60:61],50,v13\nv_cndmask_b32 v136,v136,v12,s[56:57]\nv_cmp_gt_i32 s[62:63],51,v13\nv_cndmask_b32 v137,v137,v12,s[58:59]\n"
"v_cmp_gt_i32 s[56:57],56,v13\nv_cndmask_b32 v138,v138,v12,s[60:61]\nv_cmp_gt_i32 s[58:59],57,v13\nv_cndmask_b32 v139,v139,v12,s[62:63]\n"
"v_cmp_gt_i32 s[60:61],58,v13\nv_cndmask_b32 v140,v140,v12,s[56:57]\nv_cmp_gt_i32 s[62:63],59,v13\nv_cndmask_b32 v141,v141,v12,s[58:59]\n"
"v_cndmask_b32 v142,v142,v12,s[60:61]\nv_cndmask_b32 v143,v143,v12,s[62:63]\nLnomask_60_%=:\nxm32 80,180,144\nxrb 172,1,11264\nxex 111,111\n"
"xx3 9,112,113,114\nxwl 4\nxm32 96,184,144\nxrb 176,1,12288\nxcv 159,110,111\nxx3 9,9,115,116\nxx3 9,9,117,118\nxm32 112,188,144\nxrb 180,1,13312\n"
"xx3 9,9,119,120\nxx3 9,9,121,122\nxx3 9,9,123,124\nxwl 4\nxm32 0,160,148\nxrb 184,1,14336\nxx3 9,9,125,126\nv_max_f32 v9,v9,v127\nxx3 10,128,129,130\n"
"xm32 16,164,148\nxrb 188,1,15360\nxx3 10,10,131,132\nxx3 10,10,133,134\nxx3 10,10,135,136\nxwl 4\nxm32 32,168,148\nxrb 160,2,0\nxx3 10,10,137,138\n"
"xx3 10,10,139,140\nxx3 10,10,141,142\nxm32 48,172,148\nxrb 164,2,1024\nv_max_f32 v10,v10,v143\nxad 8,v89,8\nxad 7,v90,7\nxwl 4\nxm32 64,176,148\n"
"xrb 168,2,2048\nv_max_f32 v9,v9,v10\nxad 8,v91,8\nxad 7,v92,7\nxm32 80,180,148\nxrb 172,2,3072\nxad 8,v93,8\nxad 7,v94,7\n"
"v_cmp_lt_f32 vcc,0x41000000,v9\nxwl 4\nxm32 96,184,148\nxrb 176,2,4096\nxad 8,v95,8\nxad 7,v96,7\ns_add_u32 s69,s50,2\nxm32 112,188,148\n"
"s_waitcnt vmcnt(0)\ns_barrier\nxrb 180,2,5120\nxad 8,v97,8\nxad 7,v98,7\ns_add_u32 s48,s48,0x8000\ns_mov_b32 m0,s42\nxwl 4\nxm32 0,160,152\n"
"xdl1 3,16,s40,0\nxrb 184,2,6144\nxad 8,v99,8\nxm32 16,164,152\nxrb 188,2,7168\nxad 7,v100,7\nxad 8,v101,8\nxwl 4\nxm32 32,168,152\n"
"xdl1 3,16,s40,1024\nxrb 160,2,8192\nxad 7,v102,7\nxad 8,v103,8\nxm32 48,172,152\nxrb 164,2,9216\nxad 7,v104,7\nxad 8,v105,8\nxwl 4\nxm32 64,176,152\n"
"xdl1 3,16,s40,2048\nxrb 168,2,10240\nxad 7,v106,7\nxad 8,v107,8\nxm32 80,180,152\nxrb 172,2,11264\nxad 7,v108,7\nxad 8,v109,8\nxwl 4\n"
"xm32 96,184,152\nxdl1 3,16,s40,3072\nxrb 176,2,12288\nxad 7,v110,7\nxad 8,v111,8\nxm32 112,188,152\nxrb 180,2,13312\ns_cmp_lt_u32 s69,s36\n"
"s_cselect_b32 s43,s65,0x40000000\ns_add_u32 s44,s43,0x4000\nxam 42,0x1000\nxwl 4\nxm32 0,160,156\nxdl1 3,16,s41,0\nxrb 184,2,14336\n"
"s_add_u32 s65,s65,0x8000\nxm32 16,164,156\nxrb 188,2,15360\ns_add_u32 s46,s45,s55\ns_add_u32 s45,s45,0x4000\nxwl 4\nxm32 32,168,156\n"
"xdl1 3,16,s41,1024\nxrb 160,0,0\ns_cmp_ge_u32 s45,0x14000\ns_cselect_b32 s69,0x14000,0\nxm32 48,172,156\nxrb 164,0,16384\ns_sub_u32 s45,s45,s69\n"
"s_add_u32 s47,s45,s55\ns_add_u32 s45,s45,0x4000\nxwl 4\nxm32 64,176,156\nxdl1 3,16,s41,2048\nxrb 168,0,1024\ns_cmp_ge_u32 s45,0x14000\n"
"s_cselect_b32 s69,0x14000,0\nxm32 80,180,156\nxrb 172,0,17408\ns_sub_u32 s45,s45,s69\ns_cmp_ge_u32 s48,0x14000\ns_cselect_b32 s71,0x14000,0\nxwl 4\n"
"xm32 96,184,156\nxdl1 3,16,s41,3072\nxrb 176,0,2048\ns_sub_u32 s48,s48,s71\nv_add_u32 v1,s48,v5\nxm32 112,188,156\nxrb 180,0,18432\n"
"s_xor_b32 s42,s42,0x8000\ns_add_u32 s50,s50,1\ns_sub_u32 s51,s51,1\ns_cbranch_vccz Lnoslow_61_%=\ns_nop 15\nv_mov_b32 v13,v9\nv_mov_b32 v14,v9\n"
"s_nop 1\nv_permlane32_swap_b32 v13,v14\nv_max_f32 v13,v13,v14\nv_max_f32 v13,0,v13\nv_sub_f32 v14,0,v13\nxex 14,14\nv_sub_f32 v112,v112,v13\n"
"v_sub_f32 v113,v113,v13\nv_sub_f32 v114,v114,v13\nv_sub_f32 v115,v115,v13\nv_sub_f32 v116,v116,v13\nv_sub_f32 v117,v117,v13\nv_sub_f32 v118,v118,v13\n"
"v_sub_f32 v119,v119,v13\nv_sub_f32 v120,v120,v13\nv_sub_f32 v121,v121,v13\nv_sub_f32 v122,v122,v13\nv_sub_f32 v123,v123,v13\nv_sub_f32 v124,v124,v13\n"
"v_sub_f32 v125,v125,v13\nv_sub_f32 v126,v126,v13\nv_sub_f32 v127,v127,v13\nv_sub_f32 v128,v128,v13\nv_sub_f32 v129,v129,v13\nv_sub_f32 v130,v130,v13\n"
"v_sub_f32 v131,v131,v13\nv_sub_f32 v132,v132,v13\nv_sub_f32 v133,v133,v13\nv_sub_f32 v134,v134,v13\nv_sub_f32 v135,v135,v13\nv_sub_f32 v136,v136,v13\n"
"v_sub_f32 v137,v137,v13\nv_sub_f32 v138,v138,v13\nv_sub_f32 v139,v139,v13\nv_sub_f32 v140,v140,v13\nv_sub_f32 v141,v141,v13\nv_sub_f32 v142,v142,v13\n"
"v_sub_f32 v143,v143,v13\nxad 6,v6,13\nv_sub_f32 v192,0,v6\nv_sub_f32 v193,0,v6\nv_sub_f32 v194,0,v6\nv_sub_f32 v195,0,v6\nv_sub_f32 v196,0,v6\n"
"v_sub_f32 v197,0,v6\nv_sub_f32 v198,0,v6\nv_sub_f32 v199,0,v6\nv_sub_f32 v200,0,v6\nv_sub_f32 v201,0,v6\nv_sub_f32 v202,0,v6\nv_sub_f32 v203,0,v6\n"
"v_sub_f32 v204,0,v6\nv_sub_f32 v205,0,v6\nv_sub_f32 v206,0,v6\nv_sub_f32 v207,0,v6\nxmu 7,v7,14\nxmu 8,v8,14\nxar 208,0\nxar 209,1\nxar 210,2\n"
"xar 211,3\nxar 212,4\nxar 213,5\nxar 214,6\nxar 215,7\nxmu 208,v208,14\nxmu 209,v209,14\nxmu 210,v210,14\nxmu 211,v211,14\nxmu 212,v212,14\n"
"xmu 213,v213,14\nxmu 214,v214,14\nxmu 215,v215,14\nv_accvgpr_write_b32 a0,v208\nv_accvgpr_write_b32 a1,v209\nv_accvgpr_write_b32 a2,v210\n"
"v_accvgpr_write_b32 a3,v211\nv_accvgpr_write_b32 a4,v212\nv_accvgpr_write_b32 a5,v213\nv_accvgpr_write_b32 a6,v214\nv_accvgpr_write_b32 a7,v215\n"
"xar 208,8\nxar 209,9\nxar 210,10\nxar 211,11\nxar 212,12\nxar 213,13\nxar 214,14\nxar 215,15\nxmu 208,v208,14\nxmu 209,v209,14\nxmu 210,v210,14\n"
"xmu 211,v211,14\nxmu 212,v212,14\nxmu 213,v213,14\nxmu 214,v214,14\nxmu 215,v215,14\nv_accvgpr_write_b32 a8,v208\nv_accvgpr_write_b32 a9,v209\n"
"v_accvgpr_write_b32 a10,v210\nv_accvgpr_write_b32 a11,v211\nv_accvgpr_write_b32 a12,v212\nv_accvgpr_write_b32 a13,v213\nv_accvgpr_write_b32 a14,v214\n"
"v_accvgpr_write_b32 a15,v215\nxar 208,16\nxar 209,17\nxar 210,18\nxar 211,19\nxar 212,20\nxar 213,21\nxar 214,22\nxar 215,23\nxmu 208,v208,14\n"
"xmu 209,v209,14\nxmu 210,v210,14\nxmu 211,v211,14\nxmu 212,v212,14\nxmu 213,v213,14\nxmu 214,v214,14\nxmu 215,v215,14\nv_accvgpr_write_b32 a16,v208\n"
"v_accvgpr_write_b32 a17,v209\nv_accvgpr_write_b32 a18,v210\nv_accvgpr_write_b32 a19,v211\nv_accvgpr_write_b32 a20,v212\nv_accvgpr_write_b32 a21,v213\n"
"v_accvgpr_write_b32 a22,v214\nv_accvgpr_write_b32 a23,v215\nxar 208,24\nxar 209,25\nxar 210,26\nxar 211,27\nxar 212,28\nxar 213,29\nxar 214,30\n"
"xar 215,31\nxmu 208,v208,14\nxmu 209,v209,14\nxmu 210,v210,14\nxmu 211,v211,14\nxmu 212,v212,14\nxmu 213,v213,14\nxmu 214,v214,14\nxmu 215,v215,14\n"
"v_accvgpr_write_b32 a24,v208\nv_accvgpr_write_b32 a25,v209\nv_accvgpr_write_b32 a26,v210\nv_accvgpr_write_b32 a27,v211\nv_accvgpr_write_b32 a28,v212\n"
"v_accvgpr_write_b32 a29,v213\nv_accvgpr_write_b32 a30,v214\nv_accvgpr_write_b32 a31,v215\nxar 208,32\nxar 209,33\nxar 210,34\nxar 211,35\nxar 212,36\n"
"xar 213,37\nxar 214,38\nxar 215,39\nxmu 208,v208,14\nxmu 209,v209,14\nxmu 210,v210,14\nxmu 211,v211,14\nxmu 212,v212,14\nxmu 213,v213,14\n"
"xmu 214,v214,14\nxmu 215,v215,14\nv_accvgpr_write_b32 a32,v208\nv_accvgpr_write_b32 a33,v209\nv_accvgpr_write_b32 a34,v210\n"
"v_accvgpr_write_b32 a35,v211\nv_accvgpr_write_b32 a36,v212\nv_accvgpr_write_b32 a37,v213\nv_accvgpr_write_b32 a38,v214\nv_accvgpr_write_b32 a39,v215\n"
"xar 208,40\nxar 209,41\nxar 210,42\nxar 211,43\nxar 212,44\nxar 213,45\nxar 214,46\nxar 215,47\nxmu 208,v208,14\nxmu 209,v209,14\nxmu 210,v210,14\n"
"xmu 211,v211,14\nxmu 212,v212,14\nxmu 213,v213,14\nxmu 214,v214,14\nxmu 215,v215,14\nv_accvgpr_write_b32 a40,v208\nv_accvgpr_write_b32 a41,v209\n"
"v_accvgpr_write_b32 a42,v210\nv_accvgpr_write_b32 a43,v211\nv_accvgpr_write_b32 a44,v212\nv_accvgpr_write_b32 a45,v213\nv_accvgpr_write_b32 a46,v214\n"
"v_accvgpr_write_b32 a47,v215\nxar 208,48\nxar 209,49\nxar 210,50\nxar 211,51\nxar 212,52\nxar 213,53\nxar 214,54\nxar 215,55\nxmu 208,v208,14\n"
"xmu 209,v209,14\nxmu 210,v210,14\nxmu 211,v211,14\nxmu 212,v212,14\nxmu 213,v213,14\nxmu 214,v214,14\nxmu 215,v215,14\nv_accvgpr_write_b32 a48,v208\n"
"v_accvgpr_write_b32 a49,v209\nv_accvgpr_write_b32 a50,v210\nv_accvgpr_write_b32 a51,v211\nv_accvgpr_write_b32 a52,v212\nv_accvgpr_write_b32 a53,v213\n"
"v_accvgpr_write_b32 a54,v214\nv_accvgpr_write_b32 a55,v215\nxar 208,56\nxar 209,57\nxar 210,58\nxar 211,59\nxar 212,60\nxar 213,61\nxar 214,62\n"
"xar 215,63\nxmu 208,v208,14\nxmu 209,v209,14\nxmu 210,v210,14\nxmu 211,v211,14\nxmu 212,v212,14\nxmu 213,v213,14\nxmu 214,v214,14\nxmu 215,v215,14\n"
"v_accvgpr_write_b32 a56,v208\nv_accvgpr_write_b32 a57,v209\nv_accvgpr_write_b32 a58,v210\nv_accvgpr_write_b32 a59,v211\nv_accvgpr_write_b32 a60,v212\n"
"v_accvgpr_write_b32 a61,v213\nv_accvgpr_write_b32 a62,v214\nv_accvgpr_write_b32 a63,v215\nxar 208,64\nxar 209,65\nxar 210,66\nxar 211,67\nxar 212,68\n"
"xar 213,69\nxar 214,70\nxar 215,71\nxmu 208,v208,14\nxmu 209,v209,14\nxmu 210,v210,14\nxmu 211,v211,14\nxmu 212,v212,14\nxmu 213,v213,14\n"
"xmu 214,v214,14\nxmu 215,v215,14\nv_accvgpr_write_b32 a64,v208\nv_accvgpr_write_b32 a65,v209\nv_accvgpr_write_b32 a66,v210\n"
"v_accvgpr_write_b32 a67,v211\nv_accvgpr_write_b32 a68,v212\nv_accvgpr_write_b32 a69,v213\nv_accvgpr_write_b32 a70,v214\nv_accvgpr_write_b32 a71,v215\n"
"xar 208,72\nxar 209,73\nxar 210,74\nxar 211,75\nxar 212,76\nxar 213,77\nxar 214,78\nxar 215,79\nxmu 208,v208,14\nxmu 209,v209,14\nxmu 210,v210,14\n"
"xmu 211,v211,14\nxmu 212,v212,14\nxmu 213,v213,14\nxmu 214,v214,14\nxmu 215,v215,14\nv_accvgpr_write_b32 a72,v208\nv_accvgpr_write_b32 a73,v209\n"
"v_accvgpr_write_b32 a74,v210\nv_accvgpr_write_b32 a75,v211\nv_accvgpr_write_b32 a76,v212\nv_accvgpr_write_b32 a77,v213\nv_accvgpr_write_b32 a78,v214\n"
"v_accvgpr_write_b32 a79,v215\nxar 208,80\nxar 209,81\nxar 210,82\nxar 211,83\nxar 212,84\nxar 213,85\nxar 214,86\nxar 215,87\nxmu 208,v208,14\n"
"xmu 209,v209,14\nxmu 210,v210,14\nxmu 211,v211,14\nxmu 212,v212,14\nxmu 213,v213,14\nxmu 214,v214,14\nxmu 215,v215,14\nv_accvgpr_write_b32 a80,v208\n"
"v_accvgpr_write_b32 a81,v209\nv_accvgpr_write_b32 a82,v210\nv_accvgpr_write_b32 a83,v211\nv_accvgpr_write_b32 a84,v212\nv_accvgpr_write_b32 a85,v213\n"
"v_accvgpr_write_b32 a86,v214\nv_accvgpr_write_b32 a87,v215\nxar 208,88\nxar 209,89\nxar 210,90\nxar 211,91\nxar 212,92\nxar 213,93\nxar 214,94\n"
"xar 215,95\nxmu 208,v208,14\nxmu 209,v209,14\nxmu 210,v210,14\nxmu 211,v211,14\nxmu 212,v212,14\nxmu 213,v213,14\nxmu 214,v214,14\nxmu 215,v215,14\n"
"v_accvgpr_write_b32 a88,v208\nv_accvgpr_write_b32 a89,v209\nv_accvgpr_write_b32 a90,v210\nv_accvgpr_write_b32 a91,v211\nv_accvgpr_write_b32 a92,v212\n"
"v_accvgpr_write_b32 a93,v213\nv_accvgpr_write_b32 a94,v214\nv_accvgpr_write_b32 a95,v215\nxar 208,96\nxar 209,97\nxar 210,98\nxar 211,99\n"
"xar 212,100\nxar 213,101\nxar 214,102\nxar 215,103\nxmu 208,v208,14\nxmu 209,v209,14\nxmu 210,v210,14\nxmu 211,v211,14\nxmu 212,v212,14\n"
"xmu 213,v213,14\nxmu 214,v214,14\nxmu 215,v215,14\nv_accvgpr_write_b32 a96,v208\nv_accvgpr_write_b32 a97,v209\nv_accvgpr_write_b32 a98,v210\n"
"v_accvgpr_write_b32 a99,v211\nv_accvgpr_write_b32 a100,v212\nv_accvgpr_write_b32 a101,v213\nv_accvgpr_write_b32 a102,v214\n"
"v_accvgpr_write_b32 a103,v215\nxar 208,104\nxar 209,105\nxar 210,106\nxar 211,107\nxar 212,108\nxar 213,109\nxar 214,110\nxar 215,111\n"
"xmu 208,v208,14\nxmu 209,v209,14\nxmu 210,v210,14\nxmu 211,v211,14\nxmu 212,v212,14\nxmu 213,v213,14\nxmu 214,v214,14\nxmu 215,v215,14\n"
"v_accvgpr_write_b32 a104,v208\nv_accvgpr_write_b32 a105,v209\nv_accvgpr_write_b32 a106,v210\nv_accvgpr_write_b32 a107,v211\n"
"v_accvgpr_write_b32 a108,v212\nv_accvgpr_write_b32 a109,v213\nv_accvgpr_write_b32 a110,v214\nv_accvgpr_write_b32 a111,v215\nxar 208,112\nxar 209,113\n"
"xar 210,114\nxar 211,115\nxar 212,116\nxar 213,117\nxar 214,118\nxar 215,119\nxmu 208,v208,14\nxmu 209,v209,14\nxmu 210,v210,14\nxmu 211,v211,14\n"
"xmu 212,v212,14\nxmu 213,v213,14\nxmu 214,v214,14\nxmu 215,v215,14\nv_accvgpr_write_b32 a112,v208\nv_accvgpr_write_b32 a113,v209\n"
"v_accvgpr_write_b32 a114,v210\nv_accvgpr_write_b32 a115,v211\nv_accvgpr_write_b32 a116,v212\nv_accvgpr_write_b32 a117,v213\n"
"v_accvgpr_write_b32 a118,v214\nv_accvgpr_write_b32 a119,v215\nxar 208,120\nxar 209,121\nxar 210,122\nxar 211,123\nxar 212,124\nxar 213,125\n"
"xar 214,126\nxar 215,127\nxmu 208,v208,14\nxmu 209,v209,14\nxmu 210,v210,14\nxmu 211,v211,14\nxmu 212,v212,14\nxmu 213,v213,14\nxmu 214,v214,14\n"
"xmu 215,v215,14\nv_accvgpr_write_b32 a120,v208\nv_accvgpr_write_b32 a121,v209\nv_accvgpr_write_b32 a122,v210\nv_accvgpr_write_b32 a123,v211\n"
"v_accvgpr_write_b32 a124,v212\nv_accvgpr_write_b32 a125,v213\nv_accvgpr_write_b32 a126,v214\nv_accvgpr_write_b32 a127,v215\ns_nop 3\nLnoslow_61_%=:\n"
"s_cmp_eq_u32 s51,0\ns_cbranch_scc1 Ltail1_58_%=\nxwl 4\nxm32v 80,160,16,192\nxrb 184,0,3072\nxex 112,112\ns_mov_b32 m0,s46\nxm32v 96,164,16,192\n"
"xdl1 4,20,s43,0\nxrb 188,0,19456\nxad 7,v112,7\nxwl 4\nxm32v 80,168,20,80\nxrb 160,0,4096\nxex 113,113\nv_add_u32 v2,s49,v5\nxm32v 96,172,20,96\n"
"xdl1 4,20,s43,1024\nxrb 164,0,20480\nxex 114,114\nxwl 4\nxm32v 80,176,24,80\nxrb 168,0,5120\nxex 115,115\nxcv 144,112,113\nxm32v 96,180,24,96\n"
"xdl1 4,20,s43,2048\nxrb 172,0,21504\nxex 116,116\nxwl 4\nxm32v 80,184,28,80\nxrb 176,0,6144\nxex 117,117\nxcv 145,114,115\nxm32v 96,188,28,96\n"
"xdl1 4,20,s43,3072\nxrb 180,0,22528\nxex 118,118\nxwl 4\nxm32v 80,160,32,80\nxrb 184,0,7168\nxex 119,119\nxcv 146,116,117\ns_mov_b32 m0,s47\n"
"xm32v 96,164,32,96\nxdl1 4,20,s44,0\nxrb 188,0,23552\nxcv 147,118,119\nxwl 4\nxm32v 80,168,36,80\nxrb 160,0,8192\nxex 120,120\n"
"s_add_u32 s49,s49,0x8000\nxm32v 96,172,36,96\nxdl1 4,20,s44,1024\nxrb 164,0,24576\nxex 121,121\nxwl 4\nxm32v 80,176,40,80\nxrb 168,0,9216\n"
"xex 122,122\nxcv 148,120,121\nxm32v 96,180,40,96\nxdl1 4,20,s44,2048\nxrb 172,0,25600\nxex 123,123\nxwl 4\nxm32v 80,184,44,80\nxrb 176,0,10240\n"
"xex 124,124\nxcv 149,122,123\nxm32v 96,188,44,96\nxdl1 4,20,s44,3072\nxrb 180,0,26624\nxex 125,125\nxwl 4\nxm32v 80,160,48,80\nxrb 184,0,11264\n"
"xex 126,126\nxcv 150,124,125\nxm32v 96,164,48,96\nxrb 188,0,27648\nxex 127,127\nxad 8,v113,8\nxwl 4\nxm32v 80,168,52,80\nxrb 160,0,12288\n"
"xcv 151,126,127\ns_cmp_ge_u32 s49,0x14000\ns_cselect_b32 s70,0x14000,0\nxm32v 96,172,52,96\nxrb 164,0,28672\ns_sub_u32 s49,s49,s70\nxex 128,128\n"
"xwl 4\nxm32v 80,176,56,80\nxrb 168,0,13312\ns_add_u32 s67,s50,3\ns_sub_i32 s67,s67,s36\ns_or_b32 s67,s67,s38\nxm32v 96,180,56,96\nxrb 172,0,29696\n"
"xex 129,129\nxad 7,v114,7\nxwl 4\nxm32v 80,184,60,80\nxrb 176,0,14336\nxex 130,130\nxcv 152,128,129\nxm32v 96,188,60,96\nxrb 180,0,30720\n"
"xex 131,131\nxad 8,v115,8\nxwl 4\nxm32v 80,160,64,80\nxrb 184,0,15360\nxex 132,132\nxcv 153,130,131\nxm32v 96,164,64,96\nxrb 188,0,31744\n"
"xex 133,133\nxad 7,v116,7\nxwl 4\nxm32v 80,168,68,80\nxrb 160,1,0\nxex 134,134\nxcv 154,132,133\nxm32v 96,172,68,96\nxrb 164,1,1024\nxex 135,135\n"
"xad 8,v117,8\nxwl 4\nxm32v 80,176,72,80\nxrb 168,1,2048\nxcv 155,134,135\nxex 136,136\nxm32v 96,180,72,96\nxrb 172,1,3072\nxex 137,137\nxad 7,v118,7\n"
"xwl 4\nxm32v 80,184,76,80\nxrb 176,1,4096\ns_add_u32 s68,s50,3\ns_cmp_lt_u32 s68,s36\ns_cselect_b32 s40,s64,0x40000000\nxm32v 96,188,76,96\n"
"xrb 180,1,5120\ns_add_u32 s41,s40,0x1000\ns_add_u32 s64,s64,0x8000\nxcv 156,136,137\nxwl 4\nxm32 0,160,144\nxrb 184,1,6144\nxex 138,138\n"
"v_xor_b32 v0,0x8000,v0\nxm32 16,164,144\nxrb 188,1,7168\nxex 139,139\nxad 8,v119,8\nxwl 4\nxm32 32,168,144\nxrb 160,1,8192\nxex 140,140\n"
"xcv 157,138,139\nxm32 48,172,144\nxrb 164,1,9216\nxex 141,141\nxad 7,v120,7\nxwl 4\nxm32 64,176,144\nxrb 168,1,10240\nxex 142,142\nxcv 158,140,141\n"
"s_cmp_lt_i32 s67,0\ns_cbranch_scc1 Lnomask_62_%=\ns_lshl_b32 s53,s67,6\nv_subrev_u32 v13,s53,v11\nv_cmp_gt_i32 s[56:57],0,v13\n"
"v_cmp_gt_i32 s[58:59],1,v13\nv_cmp_gt_i32 s[60:61],2,v13\nv_cndmask_b32 v80,v80,v12,s[56:57]\nv_cmp_gt_i32 s[62:63],3,v13\n"
"v_cndmask_b32 v81,v81,v12,s[58:59]\nv_cmp_gt_i32 s[56:57],8,v13\nv_cndmask_b32 v82,v82,v12,s[60:61]\nv_cmp_gt_i32 s[58:59],9,v13\n"
"v_cndmask_b32 v83,v83,v12,s[62:63]\nv_cmp_gt_i32 s[60:61],10,v13\nv_cndmask_b32 v84,v84,v12,s[56:57]\nv_cmp_gt_i32 s[62:63],11,v13\n"
"v_cndmask_b32 v85,v85,v12,s[58:59]\nv_cmp_gt_i32 s[56:57],16,v13\nv_cndmask_b32 v86,v86,v12,s[60:61]\nv_cmp_gt_i32 s[58:59],17,v13\n"
"v_cndmask_b32 v87,v87,v12,s[62:63]\nv_cmp_gt_i32 s[60:61],18,v13\nv_cndmask_b32 v88,v88,v12,s[56:57]\nv_cmp_gt_i32 s[62:63],19,v13\n"
"v_cndmask_b32 v89,v89,v12,s[58:59]\nv_cmp_gt_i32 s[56:57],24,v13\nv_cndmask_b32 v90,v90,v12,s[60:61]\nv_cmp_gt_i32 s[58:59],25,v13\n"
"v_cndmask_b32 v91,v91,v12,s[62:63]\nv_cmp_gt_i32 s[60:61],26,v13\nv_cndmask_b32 v92,v92,v12,s[56:57]\nv_cmp_gt_i32 s[62:63],27,v13\n"
"v_cndmask_b32 v93,v93,v12,s[58:59]\nv_cmp_gt_i32 s[56:57],32,v13\nv_cndmask_b32 v94,v94,v12,s[60:61]\nv_cmp_gt_i32 s[58:59],33,v13\n"
"v_cndmask_b32 v95,v95,v12,s[62:63]\nv_cmp_gt_i32 s[60:61],34,v13\nv_cndmask_b32 v96,v96,v12,s[56:57]\nv_cmp_gt_i32 s[62:63],35,v13\n"
"v_cndmask_b32 v97,v97,v12,s[58:59]\nv_cmp_gt_i32 s[56:57],40,v13\nv_cndmask_b32 v98,v98,v12,s[60:61]\nv_cmp_gt_i32 s[58:59],41,v13\n"
"v_cndmask_b32 v99,v99,v12,s[62:63]\nv_cmp_gt_i32 s[60:61],42,v13\nv_cndmask_b32 v100,v100,v12,s[56:57]\nv_cmp_gt_i32 s[62:63],43,v13\n"
"v_cndmask_b32 v101,v101,v12,s[58:59]\nv_cmp_gt_i32 s[56:57],48,v13\nv_cndmask_b32 v102,v102,v12,s[60:61]\nv_cmp_gt_i32 s[58:59],49,v13\n"
"v_cndmask_b32 v103,v103,v12,s[62:63]\nv_cmp_gt_i32 s[60:61],50,v13\nv_cndmask_b32 v104,v104,v12,s[56:57]\nv_cmp_gt_i32 s[62:63],51,v13\n"
"v_cndmask_b32 v105,v105,v12,s[58:59]\nv_cmp_gt_i32 s[56:57],56,v13\nv_cndmask_b32 v106,v106,v12,s[60:61]\nv_cmp_gt_i32 s[58:59],57,v13\n"
"v_cndmask_b32 v107,v107,v12,s[62:63]\nv_cmp_gt_i32 s[60:61],58,v13\nv_cndmask_b32 v108,v108,v12,s[56:57]\nv_cmp_gt_i32 s[62:63],59,v13\n"
"v_cndmask_b32 v109,v109,v12,s[58:59]\nv_cndmask_b32 v110,v110,v12,s[60:61]\nv_cndmask_b32 v111,v111,v12,s[62:63]\nLnomask_62_%=:\nxm32 80,180,144\n"
"xrb 172,1,11264\nxex 143,143\nxx3 9,80,81,82\nxwl 4\nxm32 96,184,144\nxrb 176,1,12288\nxcv 159,142,143\nxx3 9,9,83,84\nxx3 9,9,85,86\n"
"xm32 112,188,144\nxrb 180,1,13312\nxx3 9,9,87,88\nxx3 9,9,89,90\nxx3 9,9,91,92\nxwl 4\nxm32 0,160,148\nxrb 184,1,14336\nxx3 9,9,93,94\n"
"v_max_f32 v9,v9,v95\nxx3 10,96,97,98\nxm32 16,164,148\nxrb 188,1,15360\nxx3 10,10,99,100\nxx3 10,10,101,102\nxx3 10,10,103,104\nxwl 4\n"
"xm32 32,168,148\nxrb 160,2,0\nxx3 10,10,105,106\nxx3 10,10,107,108\nxx3 10,10,109,110\nxm32 48,172,148\nxrb 164,2,1024\nv_max_f32 v10,v10,v111\n"
"xad 8,v121,8\nxad 7,v122,7\nxwl 4\nxm32 64,176,148\nxrb 168,2,2048\nv_max_f32 v9,v9,v10\nxad 8,v123,8\nxad 7,v124,7\nxm32 80,180,148\nxrb 172,2,3072\n"
"xad 8,v125,8\nxad 7,v126,7\nv_cmp_lt_f32 vcc,0x41000000,v9\nxwl 4\nxm32 96,184,148\nxrb 176,2,4096\nxad 8,v127,8\nxad 7,v128,7\ns_add_u32 s69,s50,2\n"
"xm32 112,188,148\ns_waitcnt vmcnt(0)\ns_barrier\nxrb 180,2,5120\nxad 8,v129,8\nxad 7,v130,7\ns_add_u32 s48,s48,0x8000\ns_mov_b32 m0,s42\nxwl 4\n"
"xm32 0,160,152\nxdl1 3,16,s40,0\nxrb 184,2,6144\nxad 8,v131,8\nxm32 16,164,152\nxrb 188,2,7168\nxad 7,v132,7\nxad 8,v133,8\nxwl 4\nxm32 32,168,152\n"
"xdl1 3,16,s40,1024\nxrb 160,2,8192\nxad 7,v134,7\nxad 8,v135,8\nxm32 48,172,152\nxrb 164,2,9216\nxad 7,v136,7\nxad 8,v137,8\nxwl 4\nxm32 64,176,152\n"
"xdl1 3,16,s40,2048\nxrb 168,2,10240\nxad 7,v138,7\nxad 8,v139,8\nxm32 80,180,152\nxrb 172,2,11264\nxad 7,v140,7\nxad 8,v141,8\nxwl 4\n"
"xm32 96,184,152\nxdl1 3,16,s40,3072\nxrb 176,2,12288\nxad 7,v142,7\nxad 8,v143,8\nxm32 112,188,152\nxrb 180,2,13312\ns_cmp_lt_u32 s69,s36\n"
"s_cselect_b32 s43,s65,0x40000000\ns_add_u32 s44,s43,0x4000\nxam 42,0x1000\nxwl 4\nxm32 0,160,156\nxdl1 3,16,s41,0\nxrb 184,2,14336\n"
"s_add_u32 s65,s65,0x8000\nxm32 16,164,156\nxrb 188,2,15360\ns_add_u32 s46,s45,s55\ns_add_u32 s45,s45,0x4000\nxwl 4\nxm32 32,168,156\n"
"xdl1 3,16,s41,1024\nxrb 160,0,0\ns_cmp_ge_u32 s45,0x14000\ns_cselect_b32 s69,0x14000,0\nxm32 48,172,156\nxrb 164,0,16384\ns_sub_u32 s45,s45,s69\n"
"s_add_u32 s47,s45,s55\ns_add_u32 s45,s45,0x4000\nxwl 4\nxm32 64,176,156\nxdl1 3,16,s41,2048\nxrb 168,0,1024\ns_cmp_ge_u32 s45,0x14000\n"
"s_cselect_b32 s69,0x14000,0\nxm32 80,180,156\nxrb 172,0,17408\ns_sub_u32 s45,s45,s69\ns_cmp_ge_u32 s48,0x14000\ns_cselect_b32 s71,0x14000,0\nxwl 4\n"
"xm32 96,184,156\nxdl1 3,16,s41,3072\nxrb 176,0,2048\ns_sub_u32 s48,s48,s71\nv_add_u32 v1,s48,v5\nxm32 112,188,156\nxrb 180,0,18432\n"
"s_xor_b32 s42,s42,0x8000\ns_add_u32 s50,s50,1\ns_sub_u32 s51,s51,1\ns_cbranch_vccz Lnoslow_63_%=\ns_nop 15\nv_mov_b32 v13,v9\nv_mov_b32 v14,v9\n"
"s_nop 1\nv_permlane32_swap_b32 v13,v14\nv_max_f32 v13,v13,v14\nv_max_f32 v13,0,v13\nv_sub_f32 v14,0,v13\nxex 14,14\nv_sub_f32 v80,v80,v13\n"
"v_sub_f32 v81,v81,v13\nv_sub_f32 v82,v82,v13\nv_sub_f32 v83,v83,v13\nv_sub_f32 v84,v84,v13\nv_sub_f32 v85,v85,v13\nv_sub_f32 v86,v86,v13\n"
"v_sub_f32 v87,v87,v13\nv_sub_f32 v88,v88,v13\nv_sub_f32 v89,v89,v13\nv_sub_f32 v90,v90,v13\nv_sub_f32 v91,v91,v13\nv_sub_f32 v92,v92,v13\n"
"v_sub_f32 v93,v93,v13\nv_sub_f32 v94,v94,v13\nv_sub_f32 v95,v95,v13\nv_sub_f32 v96,v96,v13\nv_sub_f32 v97,v97,v13\nv_sub_f32 v98,v98,v13\n"
"v_sub_f32 v99,v99,v13\nv_sub_f32 v100,v100,v13\nv_sub_f32 v101,v101,v13\nv_sub_f32 v102,v102,v13\nv_sub_f32 v103,v103,v13\nv_sub_f32 v104,v104,v13\n"
"v_sub_f32 v105,v105,v13\nv_sub_f32 v106,v106,v13\nv_sub_f32 v107,v107,v13\nv_sub_f32 v108,v108,v13\nv_sub_f32 v109,v109,v13\nv_sub_f32 v110,v110,v13\n"
"v_sub_f32 v111,v111,v13\nxad 6,v6,13\nv_sub_f32 v192,0,v6\nv_sub_f32 v193,0,v6\nv_sub_f32 v194,0,v6\nv_sub_f32 v195,0,v6\nv_sub_f32 v196,0,v6\n"
"v_sub_f32 v197,0,v6\nv_sub_f32 v198,0,v6\nv_sub_f32 v199,0,v6\nv_sub_f32 v200,0,v6\nv_sub_f32 v201,0,v6\nv_sub_f32 v202,0,v6\nv_sub_f32 v203,0,v6\n"
"v_sub_f32 v204,0,v6\nv_sub_f32 v205,0,v6\nv_sub_f32 v206,0,v6\nv_sub_f32 v207,0,v6\nxmu 7,v7,14\nxmu 8,v8,14\nxar 208,0\nxar 209,1\nxar 210,2\n"
"xar 211,3\nxar 212,4\nxar 213,5\nxar 214,6\nxar 215,7\nxmu 208,v208,14\nxmu 209,v209,14\nxmu 210,v210,14\nxmu 211,v211,14\nxmu 212,v212,14\n"
"xmu 213,v213,14\nxmu 214,v214,14\nxmu 215,v215,14\nv_accvgpr_write_b32 a0,v208\nv_accvgpr_write_b32 a1,v209\nv_accvgpr_write_b32 a2,v210\n"
"v_accvgpr_write_b32 a3,v211\nv_accvgpr_write_b32 a4,v212\nv_accvgpr_write_b32 a5,v213\nv_accvgpr_write_b32 a6,v214\nv_accvgpr_write_b32 a7,v215\n"
"xar 208,8\nxar 209,9\nxar 210,10\nxar 211,11\nxar 212,12\nxar 213,13\nxar 214,14\nxar 215,15\nxmu 208,v208,14\nxmu 209,v209,14\nxmu 210,v210,14\n"
"xmu 211,v211,14\nxmu 212,v212,14\nxmu 213,v213,14\nxmu 214,v214,14\nxmu 215,v215,14\nv_accvgpr_write_b32 a8,v208\nv_accvgpr_write_b32 a9,v209\n"
"v_accvgpr_write_b32 a10,v210\nv_accvgpr_write_b32 a11,v211\nv_accvgpr_write_b32 a12,v212\nv_accvgpr_write_b32 a13,v213\nv_accvgpr_write_b32 a14,v214\n"
"v_accvgpr_write_b32 a15,v215\nxar 208,16\nxar 209,17\nxar 210,18\nxar 211,19\nxar 212,20\nxar 213,21\nxar 214,22\nxar 215,23\nxmu 208,v208,14\n"
"xmu 209,v209,14\nxmu 210,v210,14\nxmu 211,v211,14\nxmu 212,v212,14\nxmu 213,v213,14\nxmu 214,v214,14\nxmu 215,v215,14\nv_accvgpr_write_b32 a16,v208\n"
"v_accvgpr_write_b32 a17,v209\nv_accvgpr_write_b32 a18,v210\nv_accvgpr_write_b32 a19,v211\nv_accvgpr_write_b32 a20,v212\nv_accvgpr_write_b32 a21,v213\n"
"v_accvgpr_write_b32 a22,v214\nv_accvgpr_write_b32 a23,v215\nxar 208,24\nxar 209,25\nxar 210,26\nxar 211,27\nxar 212,28\nxar 213,29\nxar 214,30\n"
"xar 215,31\nxmu 208,v208,14\nxmu 209,v209,14\nxmu 210,v210,14\nxmu 211,v211,14\nxmu 212,v212,14\nxmu 213,v213,14\nxmu 214,v214,14\nxmu 215,v215,14\n"
"v_accvgpr_write_b32 a24,v208\nv_accvgpr_write_b32 a25,v209\nv_accvgpr_write_b32 a26,v210\nv_accvgpr_write_b32 a27,v211\nv_accvgpr_write_b32 a28,v212\n"
"v_accvgpr_write_b32 a29,v213\nv_accvgpr_write_b32 a30,v214\nv_accvgpr_write_b32 a31,v215\nxar 208,32\nxar 209,33\nxar 210,34\nxar 211,35\nxar 212,36\n"
"xar 213,37\nxar 214,38\nxar 215,39\nxmu 208,v208,14\nxmu 209,v209,14\nxmu 210,v210,14\nxmu 211,v211,14\nxmu 212,v212,14\nxmu 213,v213,14\n"
"xmu 214,v214,14\nxmu 215,v215,14\nv_accvgpr_write_b32 a32,v208\nv_accvgpr_write_b32 a33,v209\nv_accvgpr_write_b32 a34,v210\n"
"v_accvgpr_write_b32 a35,v211\nv_accvgpr_write_b32 a36,v212\nv_accvgpr_write_b32 a37,v213\nv_accvgpr_write_b32 a38,v214\nv_accvgpr_write_b32 a39,v215\n"
"xar 208,40\nxar 209,41\nxar 210,42\nxar 211,43\nxar 212,44\nxar 213,45\nxar 214,46\nxar 215,47\nxmu 208,v208,14\nxmu 209,v209,14\nxmu 210,v210,14\n"
"xmu 211,v211,14\nxmu 212,v212,14\nxmu 213,v213,14\nxmu 214,v214,14\nxmu 215,v215,14\nv_accvgpr_write_b32 a40,v208\nv_accvgpr_write_b32 a41,v209\n"
"v_accvgpr_write_b32 a42,v210\nv_accvgpr_write_b32 a43,v211\nv_accvgpr_write_b32 a44,v212\nv_accvgpr_write_b32 a45,v213\nv_accvgpr_write_b32 a46,v214\n"
"v_accvgpr_write_b32 a47,v215\nxar 208,48\nxar 209,49\nxar 210,50\nxar 211,51\nxar 212,52\nxar 213,53\nxar 214,54\nxar 215,55\nxmu 208,v208,14\n"
"xmu 209,v209,14\nxmu 210,v210,14\nxmu 211,v211,14\nxmu 212,v212,14\nxmu 213,v213,14\nxmu 214,v214,14\nxmu 215,v215,14\nv_accvgpr_write_b32 a48,v208\n"
"v_accvgpr_write_b32 a49,v209\nv_accvgpr_write_b32 a50,v210\nv_accvgpr_write_b32 a51,v211\nv_accvgpr_write_b32 a52,v212\nv_accvgpr_write_b32 a53,v213\n"
"v_accvgpr_write_b32 a54,v214\nv_accvgpr_write_b32 a55,v215\nxar 208,56\nxar 209,57\nxar 210,58\nxar 211,59\nxar 212,60\nxar 213,61\nxar 214,62\n"
"xar 215,63\nxmu 208,v208,14\nxmu 209,v209,14\nxmu 210,v210,14\nxmu 211,v211,14\nxmu 212,v212,14\nxmu 213,v213,14\nxmu 214,v214,14\nxmu 215,v215,14\n"
"v_accvgpr_write_b32 a56,v208\nv_accvgpr_write_b32 a57,v209\nv_accvgpr_write_b32 a58,v210\nv_accvgpr_write_b32 a59,v211\nv_accvgpr_write_b32 a60,v212\n"
"v_accvgpr_write_b32 a61,v213\nv_accvgpr_write_b32 a62,v214\nv_accvgpr_write_b32 a63,v215\nxar 208,64\nxar 209,65\nxar 210,66\nxar 211,67\nxar 212,68\n"
"xar 213,69\nxar 214,70\nxar 215,71\nxmu 208,v208,14\nxmu 209,v209,14\nxmu 210,v210,14\nxmu 211,v211,14\nxmu 212,v212,14\nxmu 213,v213,14\n"
"xmu 214,v214,14\nxmu 215,v215,14\nv_accvgpr_write_b32 a64,v208\nv_accvgpr_write_b32 a65,v209\nv_accvgpr_write_b32 a66,v210\n"
"v_accvgpr_write_b32 a67,v211\nv_accvgpr_write_b32 a68,v212\nv_accvgpr_write_b32 a69,v213\nv_accvgpr_write_b32 a70,v214\nv_accvgpr_write_b32 a71,v215\n"
"xar 208,72\nxar 209,73\nxar 210,74\nxar 211,75\nxar 212,76\nxar 213,77\nxar 214,78\nxar 215,79\nxmu 208,v208,14\nxmu 209,v209,14\nxmu 210,v210,14\n"
"xmu 211,v211,14\nxmu 212,v212,14\nxmu 213,v213,14\nxmu 214,v214,14\nxmu 215,v215,14\nv_accvgpr_write_b32 a72,v208\nv_accvgpr_write_b32 a73,v209\n"
"v_accvgpr_write_b32 a74,v210\nv_accvgpr_write_b32 a75,v211\nv_accvgpr_write_b32 a76,v212\nv_accvgpr_write_b32 a77,v213\nv_accvgpr_write_b32 a78,v214\n"
"v_accvgpr_write_b32 a79,v215\nxar 208,80\nxar 209,81\nxar 210,82\nxar 211,83\nxar 212,84\nxar 213,85\nxar 214,86\nxar 215,87\nxmu 208,v208,14\n"
"xmu 209,v209,14\nxmu 210,v210,14\nxmu 211,v211,14\nxmu 212,v212,14\nxmu 213,v213,14\nxmu 214,v214,14\nxmu 215,v215,14\nv_accvgpr_write_b32 a80,v208\n"
"v_accvgpr_write_b32 a81,v209\nv_accvgpr_write_b32 a82,v210\nv_accvgpr_write_b32 a83,v211\nv_accvgpr_write_b32 a84,v212\nv_accvgpr_write_b32 a85,v213\n"
"v_accvgpr_write_b32 a86,v214\nv_accvgpr_write_b32 a87,v215\nxar 208,88\nxar 209,89\nxar 210,90\nxar 211,91\nxar 212,92\nxar 213,93\nxar 214,94\n"
"xar 215,95\nxmu 208,v208,14\nxmu 209,v209,14\nxmu 210,v210,14\nxmu 211,v211,14\nxmu 212,v212,14\nxmu 213,v213,14\nxmu 214,v214,14\nxmu 215,v215,14\n"
"v_accvgpr_write_b32 a88,v208\nv_accvgpr_write_b32 a89,v209\nv_accvgpr_write_b32 a90,v210\nv_accvgpr_write_b32 a91,v211\nv_accvgpr_write_b32 a92,v212\n"
"v_accvgpr_write_b32 a93,v213\nv_accvgpr_write_b32 a94,v214\nv_accvgpr_write_b32 a95,v215\nxar 208,96\nxar 209,97\nxar 210,98\nxar 211,99\n"
"xar 212,100\nxar 213,101\nxar 214,102\nxar 215,103\nxmu 208,v208,14\nxmu 209,v209,14\nxmu 210,v210,14\nxmu 211,v211,14\nxmu 212,v212,14\n"
"xmu 213,v213,14\nxmu 214,v214,14\nxmu 215,v215,14\nv_accvgpr_write_b32 a96,v208\nv_accvgpr_write_b32 a97,v209\nv_accvgpr_write_b32 a98,v210\n"
"v_accvgpr_write_b32 a99,v211\nv_accvgpr_write_b32 a100,v212\nv_accvgpr_write_b32 a101,v213\nv_accvgpr_write_b32 a102,v214\n"
"v_accvgpr_write_b32 a103,v215\nxar 208,104\nxar 209,105\nxar 210,106\nxar 211,107\nxar 212,108\nxar 213,109\nxar 214,110\nxar 215,111\n"
"xmu 208,v208,14\nxmu 209,v209,14\nxmu 210,v210,14\nxmu 211,v211,14\nxmu 212,v212,14\nxmu 213,v213,14\nxmu 214,v214,14\nxmu 215,v215,14\n"
"v_accvgpr_write_b32 a104,v208\nv_accvgpr_write_b32 a105,v209\nv_accvgpr_write_b32 a106,v210\nv_accvgpr_write_b32 a107,v211\n"
"v_accvgpr_write_b32 a108,v212\nv_accvgpr_write_b32 a109,v213\nv_accvgpr_write_b32 a110,v214\nv_accvgpr_write_b32 a111,v215\nxar 208,112\nxar 209,113\n"
"xar 210,114\nxar 211,115\nxar 212,116\nxar 213,117\nxar 214,118\nxar 215,119\nxmu 208,v208,14\nxmu 209,v209,14\nxmu 210,v210,14\nxmu 211,v211,14\n"
"xmu 212,v212,14\nxmu 213,v213,14\nxmu 214,v214,14\nxmu 215,v215,14\nv_accvgpr_write_b32 a112,v208\nv_accvgpr_write_b32 a113,v209\n"
"v_accvgpr_write_b32 a114,v210\nv_accvgpr_write_b32 a115,v211\nv_accvgpr_write_b32 a116,v212\nv_accvgpr_write_b32 a117,v213\n"
"v_accvgpr_write_b32 a118,v214\nv_accvgpr_write_b32 a119,v215\nxar 208,120\nxar 209,121\nxar 210,122\nxar 211,123\nxar 212,124\nxar 213,125\n"
"xar 214,126\nxar 215,127\nxmu 208,v208,14\nxmu 209,v209,14\nxmu 210,v210,14\nxmu 211,v211,14\nxmu 212,v212,14\nxmu 213,v213,14\nxmu 214,v214,14\n"
"xmu 215,v215,14\nv_accvgpr_write_b32 a120,v208\nv_accvgpr_write_b32 a121,v209\nv_accvgpr_write_b32 a122,v210\nv_accvgpr_write_b32 a123,v211\n"
"v_accvgpr_write_b32 a124,v212\nv_accvgpr_write_b32 a125,v213\nv_accvgpr_write_b32 a126,v214\nv_accvgpr_write_b32 a127,v215\ns_nop 3\nLnoslow_63_%=:\n"
"s_cmp_eq_u32 s51,0\ns_cbranch_scc0 LbodyA_56_%=\nLtail0_57_%=:\nv_add_u32 v2,s49,v5\nxwl 5\nxrb 160,1,0\nxwl 5\nxrb 164,1,1024\nxwl 5\n"
"xrb 168,1,2048\nxwl 5\nxrb 172,1,3072\nxwl 5\nxrb 176,1,4096\nxwl 5\nxrb 180,1,5120\nxex 80,80\nxex 81,81\nxex 82,82\nxex 83,83\nxex 84,84\n"
"xex 85,85\nxex 86,86\nxex 87,87\nxex 88,88\nxex 89,89\nxex 90,90\nxex 91,91\nxex 92,92\nxex 93,93\nxex 94,94\nxex 95,95\nxad 7,v80,7\nxad 8,v81,8\n"
"xad 7,v82,7\nxad 8,v83,8\nxad 7,v84,7\nxad 8,v85,8\nxad 7,v86,7\nxad 8,v87,8\nxad 7,v88,7\nxad 8,v89,8\nxad 7,v90,7\nxad 8,v91,8\nxad 7,v92,7\n"
"xad 8,v93,8\nxad 7,v94,7\nxad 8,v95,8\nxcv 144,80,81\nxcv 145,82,83\nxcv 146,84,85\nxcv 147,86,87\nxcv 148,88,89\nxcv 149,90,91\nxcv 150,92,93\n"
"xcv 151,94,95\nxwl 5\nxm32 0,160,144\nxrb 184,1,6144\nxex 96,96\nxex 97,97\nxwl 5\nxm32 16,164,144\nxrb 188,1,7168\nxad 7,v96,7\nxad 8,v97,8\n"
"xcv 152,96,97\nxex 98,98\nxwl 5\nxm32 32,168,144\nxrb 160,1,8192\nxad 7,v98,7\nxex 99,99\nxwl 5\nxm32 48,172,144\nxrb 164,1,9216\nxad 8,v99,8\n"
"xcv 153,98,99\nxex 100,100\nxwl 5\nxm32 64,176,144\nxrb 168,1,10240\nxad 7,v100,7\nxex 101,101\nxex 102,102\nxwl 5\nxm32 80,180,144\nxrb 172,1,11264\n"
"xad 8,v101,8\nxcv 154,100,101\nxad 7,v102,7\nxex 103,103\nxwl 5\nxm32 96,184,144\nxrb 176,1,12288\nxad 8,v103,8\nxcv 155,102,103\nxex 104,104\nxwl 5\n"
"xm32 112,188,144\nxrb 180,1,13312\nxad 7,v104,7\nxex 105,105\nxwl 5\nxm32 0,160,148\nxrb 184,1,14336\nxad 8,v105,8\nxcv 156,104,105\nxex 106,106\n"
"xex 107,107\nxwl 5\nxm32 16,164,148\nxrb 188,1,15360\nxad 7,v106,7\nxad 8,v107,8\nxcv 157,106,107\nxex 108,108\nxwl 5\nxm32 32,168,148\nxrb 160,2,0\n"
"xad 7,v108,7\nxex 109,109\nxwl 5\nxm32 48,172,148\nxrb 164,2,1024\nxad 8,v109,8\nxcv 158,108,109\nxex 110,110\nxwl 5\nxm32 64,176,148\n"
"xrb 168,2,2048\nxad 7,v110,7\nxex 111,111\nxwl 5\nxm32 80,180,148\nxrb 172,2,3072\nxad 8,v111,8\nxcv 159,110,111\nxwl 5\nxm32 96,184,148\n"
"xrb 176,2,4096\nxwl 5\nxm32 112,188,148\nxrb 180,2,5120\nxwl 5\nxm32 0,160,152\nxrb 184,2,6144\nxwl 5\nxm32 16,164,152\nxrb 188,2,7168\nxwl 5\n"
"xm32 32,168,152\nxrb 160,2,8192\nxwl 5\nxm32 48,172,152\nxrb 164,2,9216\nxwl 5\nxm32 64,176,152\nxrb 168,2,10240\nxwl 5\nxm32 80,180,152\n"
"xrb 172,2,11264\nxwl 5\nxm32 96,184,152\nxrb 176,2,12288\nxwl 5\nxm32 112,188,152\nxrb 180,2,13312\nxwl 5\nxm32 0,160,156\nxrb 184,2,14336\nxwl 5\n"
"xm32 16,164,156\nxrb 188,2,15360\nxwl 5\nxm32 32,168,156\nxwl 4\nxm32 48,172,156\nxwl 3\nxm32 64,176,156\nxwl 2\nxm32 80,180,156\nxwl 1\n"
"xm32 96,184,156\nxwl 0\nxm32 112,188,156\ns_branch Lend_59_%=\nLtail1_58_%=:\nv_add_u32 v2,s49,v5\nxwl 5\nxrb 160,1,0\nxwl 5\nxrb 164,1,1024\nxwl 5\n"
"xrb 168,1,2048\nxwl 5\nxrb 172,1,3072\nxwl 5\nxrb 176,1,4096\nxwl 5\nxrb 180,1,5120\nxex 112,112\nxex 113,113\nxex 114,114\nxex 115,115\nxex 116,116\n"
"xex 117,117\nxex 118,118\nxex 119,119\nxex 120,120\nxex 121,121\nxex 122,122\nxex 123,123\nxex 124,124\nxex 125,125\nxex 126,126\nxex 127,127\n"
"xad 7,v112,7\nxad 8,v113,8\nxad 7,v114,7\nxad 8,v115,8\nxad 7,v116,7\nxad 8,v117,8\nxad 7,v118,7\nxad 8,v119,8\nxad 7,v120,7\nxad 8,v121,8\n"
"xad 7,v122,7\nxad 8,v123,8\nxad 7,v124,7\nxad 8,v125,8\nxad 7,v126,7\nxad 8,v127,8\nxcv 144,112,113\nxcv 145,114,115\nxcv 146,116,117\n"
"xcv 147,118,119\nxcv 148,120,121\nxcv 149,122,123\nxcv 150,124,125\nxcv 151,126,127\nxwl 5\nxm32 0,160,144\nxrb 184,1,6144\nxex 128,128\nxex 129,129\n"
"xwl 5\nxm32 16,164,144\nxrb 188,1,7168\nxad 7,v128,7\nxad 8,v129,8\nxcv 152,128,129\nxex 130,130\nxwl 5\nxm32 32,168,144\nxrb 160,1,8192\n"
"xad 7,v130,7\nxex 131,131\nxwl 5\nxm32 48,172,144\nxrb 164,1,9216\nxad 8,v131,8\nxcv 153,130,131\nxex 132,132\nxwl 5\nxm32 64,176,144\n"
"xrb 168,1,10240\nxad 7,v132,7\nxex 133,133\nxex 134,134\nxwl 5\nxm32 80,180,144\nxrb 172,1,11264\nxad 8,v133,8\nxcv 154,132,133\nxad 7,v134,7\n"
"xex 135,135\nxwl 5\nxm32 96,184,144\nxrb 176,1,12288\nxad 8,v135,8\nxcv 155,134,135\nxex 136,136\nxwl 5\nxm32 112,188,144\nxrb 180,1,13312\n"
"xad 7,v136,7\nxex 137,137\nxwl 5\nxm32 0,160,148\nxrb 184,1,14336\nxad 8,v137,8\nxcv 156,136,137\nxex 138,138\nxex 139,139\nxwl 5\nxm32 16,164,148\n"
"xrb 188,1,15360\nxad 7,v138,7\nxad 8,v139,8\nxcv 157,138,139\nxex 140,140\nxwl 5\nxm32 32,168,148\nxrb 160,2,0\nxad 7,v140,7\nxex 141,141\nxwl 5\n"
"xm32 48,172,148\nxrb 164,2,1024\nxad 8,v141,8\nxcv 158,140,141\nxex 142,142\nxwl 5\nxm32 64,176,148\nxrb 168,2,2048\nxad 7,v142,7\nxex 143,143\n"
"xwl 5\nxm32 80,180,148\nxrb 172,2,3072\nxad 8,v143,8\nxcv 159,142,143\nxwl 5\nxm32 96,184,148\nxrb 176,2,4096\nxwl 5\nxm32 112,188,148\n"
"xrb 180,2,5120\nxwl 5\nxm32 0,160,152\nxrb 184,2,6144\nxwl 5\nxm32 16,164,152\nxrb 188,2,7168\nxwl 5\nxm32 32,168,152\nxrb 160,2,8192\nxwl 5\n"
"xm32 48,172,152\nxrb 164,2,9216\nxwl 5\nxm32 64,176,152\nxrb 168,2,10240\nxwl 5\nxm32 80,180,152\nxrb 172,2,11264\nxwl 5\nxm32 96,184,152\n"
"xrb 176,2,12288\nxwl 5\nxm32 112,188,152\nxrb 180,2,13312\nxwl 5\nxm32 0,160,156\nxrb 184,2,14336\nxwl 5\nxm32 16,164,156\nxrb 188,2,15360\nxwl 5\n"
"xm32 32,168,156\nxwl 4\nxm32 48,172,156\nxwl 3\nxm32 64,176,156\nxwl 2\nxm32 80,180,156\nxwl 1\nxm32 96,184,156\nxwl 0\nxm32 112,188,156\n"
"Lend_59_%=:\nxad 7,v7,8\nv_mov_b32 v8,v7\ns_nop 1\nv_permlane32_swap_b32 v7,v8\nxad 7,v7,8\ns_waitcnt vmcnt(0) lgkmcnt(0)\ns_barrier\ns_nop 15\n"
"s_lshr_b32 s98,s100,28\ns_and_b32 s98,s98,1\ns_or_b32 s98,s101,s98\ns_cmp_lg_u32 s98,0\ns_cbranch_scc1 Lfin_44_%=\ns_nop 4\n"
"v_mbcnt_lo_u32_b32 v252,-1,0\nv_mbcnt_hi_u32_b32 v252,-1,v252\ns_and_b32 s95,s100,15\ns_lshl_b32 s95,s95,3\ns_lshr_b32 s94,s100,25\n"
"s_and_b32 s94,s94,7\ns_add_u32 s95,s95,s94\ns_lshl_b32 s95,s95,17\ns_add_u32 s95,0x5800000,s95\ns_add_u32 s16,s4,s95\ns_addc_u32 s17,s5,0\n"
"s_and_b32 s17,s17,0xffff\ns_mov_b32 s18,0x20000\ns_mov_b32 s19,0x20000\ns_and_b32 s95,s100,15\ns_lshl_b32 s95,s95,3\ns_lshr_b32 s94,s100,25\n"
"s_and_b32 s94,s94,7\ns_add_u32 s95,s95,s94\ns_lshl_b32 s95,s95,11\ns_add_u32 s95,0x6a00000,s95\ns_add_u32 s20,s4,s95\ns_addc_u32 s21,s5,0\n"
"s_and_b32 s21,s21,0xffff\ns_mov_b32 s22,0x800\ns_mov_b32 s23,0x20000\nv_lshlrev_b32 v0,4,v252\ns_lshl_b32 s95,s3,15\nv_add_u32 v0,s95,v0\n"
"v_lshlrev_b32 v1,3,v252\ns_lshl_b32 s95,s3,9\nv_add_u32 v1,s95,v1\ns_mov_b32 s39,s3\ns_and_b32 s74,s100,15\ns_lshl_b32 s74,s74,3\n"
"s_lshr_b32 s95,s100,25\ns_and_b32 s95,s95,7\ns_add_u32 s74,s74,s95\ns_lshl_b32 s74,s74,3\ns_add_u32 s74,0x4000,s74\ns_add_u32 s76,s4,0x6c00000\n"
"s_addc_u32 s77,s5,0\ns_and_b32 s77,s77,0xffff\ns_mov_b32 s78,0x8000\ns_mov_b32 s79,0x20000\ns_mov_b32 s52,0\n"
"buffer_store_dwordx4 a[0:3],v0,s[16:19],s52 offen offset:0 sc1\nbuffer_store_dwordx4 a[4:7],v0,s[16:19],s52 offen offset:1024 sc1\n"
"buffer_store_dwordx4 a[8:11],v0,s[16:19],s52 offen offset:2048 sc1\nbuffer_store_dwordx4 a[12:15],v0,s[16:19],s52 offen offset:3072 sc1\n"
"s_mov_b32 s52,0x1000\nbuffer_store_dwordx4 a[16:19],v0,s[16:19],s52 offen offset:0 sc1\n"
"buffer_store_dwordx4 a[20:23],v0,s[16:19],s52 offen offset:1024 sc1\nbuffer_store_dwordx4 a[24:27],v0,s[16:19],s52 offen offset:2048 sc1\n"
"buffer_store_dwordx4 a[28:31],v0,s[16:19],s52 offen offset:3072 sc1\ns_mov_b32 s52,0x2000\n"
"buffer_store_dwordx4 a[32:35],v0,s[16:19],s52 offen offset:0 sc1\nbuffer_store_dwordx4 a[36:39],v0,s[16:19],s52 offen offset:1024 sc1\n"
"buffer_store_dwordx4 a[40:43],v0,s[16:19],s52 offen offset:2048 sc1\nbuffer_store_dwordx4 a[44:47],v0,s[16:19],s52 offen offset:3072 sc1\n"
"s_mov_b32 s52,0x3000\nbuffer_store_dwordx4 a[48:51],v0,s[16:19],s52 offen offset:0 sc1\n"
"buffer_store_dwordx4 a[52:55],v0,s[16:19],s52 offen offset:1024 sc1\nbuffer_store_dwordx4 a[56:59],v0,s[16:19],s52 offen offset:2048 sc1\n"
"buffer_store_dwordx4 a[60:63],v0,s[16:19],s52 offen offset:3072 sc1\ns_mov_b32 s52,0x4000\n"
"buffer_store_dwordx4 a[64:67],v0,s[16:19],s52 offen offset:0 sc1\nbuffer_store_dwordx4 a[68:71],v0,s[16:19],s52 offen offset:1024 sc1\n"
"buffer_store_dwordx4 a[72:75],v0,s[16:19],s52 offen offset:2048 sc1\nbuffer_store_dwordx4 a[76:79],v0,s[16:19],s52 offen offset:3072 sc1\n"
"s_mov_b32 s52,0x5000\nbuffer_store_dwordx4 a[80:83],v0,s[16:19],s52 offen offset:0 sc1\n"
"buffer_store_dwordx4 a[84:87],v0,s[16:19],s52 offen offset:1024 sc1\nbuffer_store_dwordx4 a[88:91],v0,s[16:19],s52 offen offset:2048 sc1\n"
"buffer_store_dwordx4 a[92:95],v0,s[16:19],s52 offen offset:3072 sc1\ns_mov_b32 s52,0x6000\n"
"buffer_store_dwordx4 a[96:99],v0,s[16:19],s52 offen offset:0 sc1\nbuffer_store_dwordx4 a[100:103],v0,s[16:19],s52 offen offset:1024 sc1\n"
"buffer_store_dwordx4 a[104:107],v0,s[16:19],s52 offen offset:2048 sc1\nbuffer_store_dwordx4 a[108:111],v0,s[16:19],s52 offen offset:3072 sc1\n"
"s_mov_b32 s52,0x7000\nbuffer_store_dwordx4 a[112:115],v0,s[16:19],s52 offen offset:0 sc1\n"
"buffer_store_dwordx4 a[116:119],v0,s[16:19],s52 offen offset:1024 sc1\nbuffer_store_dwordx4 a[120:123],v0,s[16:19],s52 offen offset:2048 sc1\n"
"buffer_store_dwordx4 a[124:127],v0,s[16:19],s52 offen offset:3072 sc1\nbuffer_store_dwordx2 v[6:7],v1,s[20:23],0 offen offset:0 sc1\n"
"s_waitcnt vmcnt(0)\ns_barrier\ns_cmp_eq_u32 s39,0\ns_cbranch_scc0 Lnosig_64_%=\nv_mov_b32 v216,s6\nv_mov_b32 v217,s7\nv_mov_b32 v219,0\n"
"s_mov_b64 exec,1\nbuffer_store_dwordx2 v[216:217],v219,s[76:79],s74 offen offset:0 sc1\ns_mov_b64 exec,-1\nLnosig_64_%=:\ns_nop 1\ns_mov_b32 s101,1\n"
"s_branch Lseg_43_%=\nLfin_44_%=:\ns_lshr_b32 s98,s100,28\ns_and_b32 s98,s98,1\ns_cmp_lg_u32 s98,0\ns_cbranch_scc1 Lfm_45_%=\ns_nop 4\n"
"v_mbcnt_lo_u32_b32 v252,-1,0\nv_mbcnt_hi_u32_b32 v252,-1,v252\ns_lshr_b32 s82,s100,4\ns_and_b32 s82,s82,15\nv_lshrrev_b32 v2,4,v252\n"
"v_and_b32 v2,1,v2\ns_lshl_b32 s95,s3,1\nv_add_u32 v2,s95,v2\nv_mul_u32_u24 v2,0x27000,v2\nv_lshrrev_b32 v247,5,v252\nv_lshlrev_b32 v247,8,v247\n"
"v_add_u32 v2,v2,v247\nv_and_b32 v247,15,v252\nv_lshlrev_b32 v247,4,v247\nv_add_u32 v2,v2,v247\nv_and_b32 v4,31,v252\ns_lshl_b32 s95,s3,5\n"
"v_add_u32 v4,s95,v4\nv_lshlrev_b32 v4,2,v4\ns_mul_i32 s95,s82,0x138000\ns_add_u32 s95,0x3000000,s95\ns_and_b32 s94,s100,15\ns_lshl_b32 s94,s94,13\n"
"s_add_u32 s95,s95,s94\ns_add_u32 s28,s4,s95\ns_addc_u32 s29,s5,0\ns_and_b32 s29,s29,0xffff\ns_and_b32 s30,s100,15\ns_lshl_b32 s30,s30,13\n"
"s_sub_u32 s30,0x138000,s30\ns_mov_b32 s31,0x20000\ns_lshl_b32 s95,s82,9\ns_add_u32 s95,0x5400000,s95\ns_add_u32 s32,s4,s95\ns_addc_u32 s33,s5,0\n"
"s_and_b32 s33,s33,0xffff\ns_mov_b32 s34,0x200\ns_mov_b32 s35,0x20000\ns_mov_b32 s39,s3\ns_and_b32 s74,s100,15\ns_lshl_b32 s74,s74,3\n"
"s_lshr_b32 s95,s100,25\ns_and_b32 s95,s95,7\ns_add_u32 s74,s74,s95\ns_lshl_b32 s74,s74,3\ns_add_u32 s74,0x4000,s74\ns_lshl_b32 s75,s82,7\n"
"s_add_u32 s75,0x1000,s75\ns_add_u32 s76,s4,0x6c00000\ns_addc_u32 s77,s5,0\ns_and_b32 s77,s77,0xffff\ns_mov_b32 s78,0x8000\ns_mov_b32 s79,0x20000\n"
"s_lshl_b32 s80,s82,4\ns_and_b32 s95,s100,15\ns_add_u32 s80,s80,s95\ns_lshl_b32 s80,s80,3\ns_add_u32 s80,0x6000,s80\ns_mov_b32 s53,0x30d40\n"
"v_mbcnt_lo_u32_b32 v218,-1,0\nv_mbcnt_hi_u32_b32 v218,-1,v218\nv_lshlrev_b32 v218,3,v218\n"
"buffer_load_dwordx2 v[216:217],v218,s[76:79],s75 offen offset:0 sc1\nLspin_65_%=:\ns_waitcnt vmcnt(0)\nv_cmp_eq_u64 vcc,s[6:7],v[216:217]\ns_nop 3\n"
"s_and_b32 s52,vcc_lo,0xffff\ns_cmp_eq_u32 s52,0xffff\ns_cbranch_scc1 Lspun_66_%=\ns_sleep 2\n"
"buffer_load_dwordx2 v[216:217],v218,s[76:79],s75 offen offset:0 sc1\ns_sub_u32 s53,s53,1\ns_cmp_gt_i32 s53,0\ns_cbranch_scc1 Lspin_65_%=\n"
"s_waitcnt vmcnt(0)\nLspun_66_%=:\ns_nop 1\nbuffer_load_dword v3,v4,s[32:35],0 offen offset:0 sc1\ns_waitcnt vmcnt(0)\nxrc 8,7\ns_nop 0\nxmu 8,v8,3\n"
"xar 16,0\nxar 17,1\nxar 18,2\nxar 19,3\nxar 20,8\nxar 21,9\nxar 22,10\nxar 23,11\nxmu 16,v16,8\nxmu 17,v17,8\nxmu 18,v18,8\nxmu 19,v19,8\n"
"xmu 20,v20,8\nxmu 21,v21,8\nxmu 22,v22,8\nxmu 23,v23,8\nxcv 48,16,17\nxcv 49,18,19\nxcv 50,20,21\nxcv 51,22,23\ns_mov_b32 s52,0\nxst1 48,2,28,s52,0\n"
"xar 24,4\nxar 25,5\nxar 26,6\nxar 27,7\nxar 28,12\nxar 29,13\nxar 30,14\nxar 31,15\nxmu 24,v24,8\nxmu 25,v25,8\nxmu 26,v26,8\nxmu 27,v27,8\n"
"xmu 28,v28,8\nxmu 29,v29,8\nxmu 30,v30,8\nxmu 31,v31,8\nxcv 52,24,25\nxcv 53,26,27\nxcv 54,28,29\nxcv 55,30,31\nxst1 52,2,28,s52,512\nxar 16,16\n"
"xar 17,17\nxar 18,18\nxar 19,19\nxar 20,24\nxar 21,25\nxar 22,26\nxar 23,27\nxmu 16,v16,8\nxmu 17,v17,8\nxmu 18,v18,8\nxmu 19,v19,8\nxmu 20,v20,8\n"
"xmu 21,v21,8\nxmu 22,v22,8\nxmu 23,v23,8\nxcv 56,16,17\nxcv 57,18,19\nxcv 58,20,21\nxcv 59,22,23\nxst1 56,2,28,s52,1024\nxar 24,20\nxar 25,21\n"
"xar 26,22\nxar 27,23\nxar 28,28\nxar 29,29\nxar 30,30\nxar 31,31\nxmu 24,v24,8\nxmu 25,v25,8\nxmu 26,v26,8\nxmu 27,v27,8\nxmu 28,v28,8\nxmu 29,v29,8\n"
"xmu 30,v30,8\nxmu 31,v31,8\nxcv 60,24,25\nxcv 61,26,27\nxcv 62,28,29\nxcv 63,30,31\nxst1 60,2,28,s52,1536\nxar 16,32\nxar 17,33\nxar 18,34\n"
"xar 19,35\nxar 20,40\nxar 21,41\nxar 22,42\nxar 23,43\nxmu 16,v16,8\nxmu 17,v17,8\nxmu 18,v18,8\nxmu 19,v19,8\nxmu 20,v20,8\nxmu 21,v21,8\n"
"xmu 22,v22,8\nxmu 23,v23,8\nxcv 48,16,17\nxcv 49,18,19\nxcv 50,20,21\nxcv 51,22,23\nxst1 48,2,28,s52,2048\nxar 24,36\nxar 25,37\nxar 26,38\n"
"xar 27,39\nxar 28,44\nxar 29,45\nxar 30,46\nxar 31,47\nxmu 24,v24,8\nxmu 25,v25,8\nxmu 26,v26,8\nxmu 27,v27,8\nxmu 28,v28,8\nxmu 29,v29,8\n"
"xmu 30,v30,8\nxmu 31,v31,8\nxcv 52,24,25\nxcv 53,26,27\nxcv 54,28,29\nxcv 55,30,31\nxst1 52,2,28,s52,2560\nxar 16,48\nxar 17,49\nxar 18,50\n"
"xar 19,51\nxar 20,56\nxar 21,57\nxar 22,58\nxar 23,59\nxmu 16,v16,8\nxmu 17,v17,8\nxmu 18,v18,8\nxmu 19,v19,8\nxmu 20,v20,8\nxmu 21,v21,8\n"
"xmu 22,v22,8\nxmu 23,v23,8\nxcv 56,16,17\nxcv 57,18,19\nxcv 58,20,21\nxcv 59,22,23\nxst1 56,2,28,s52,3072\nxar 24,52\nxar 25,53\nxar 26,54\n"
"xar 27,55\nxar 28,60\nxar 29,61\nxar 30,62\nxar 31,63\nxmu 24,v24,8\nxmu 25,v25,8\nxmu 26,v26,8\nxmu 27,v27,8\nxmu 28,v28,8\nxmu 29,v29,8\n"
"xmu 30,v30,8\nxmu 31,v31,8\nxcv 60,24,25\nxcv 61,26,27\nxcv 62,28,29\nxcv 63,30,31\nxst1 60,2,28,s52,3584\nxar 16,64\nxar 17,65\nxar 18,66\n"
"xar 19,67\nxar 20,72\nxar 21,73\nxar 22,74\nxar 23,75\nxmu 16,v16,8\nxmu 17,v17,8\nxmu 18,v18,8\nxmu 19,v19,8\nxmu 20,v20,8\nxmu 21,v21,8\n"
"xmu 22,v22,8\nxmu 23,v23,8\nxcv 48,16,17\nxcv 49,18,19\nxcv 50,20,21\nxcv 51,22,23\ns_mov_b32 s52,0x1000\nxst1 48,2,28,s52,0\nxar 24,68\nxar 25,69\n"
"xar 26,70\nxar 27,71\nxar 28,76\nxar 29,77\nxar 30,78\nxar 31,79\nxmu 24,v24,8\nxmu 25,v25,8\nxmu 26,v26,8\nxmu 27,v27,8\nxmu 28,v28,8\nxmu 29,v29,8\n"
"xmu 30,v30,8\nxmu 31,v31,8\nxcv 52,24,25\nxcv 53,26,27\nxcv 54,28,29\nxcv 55,30,31\nxst1 52,2,28,s52,512\nxar 16,80\nxar 17,81\nxar 18,82\nxar 19,83\n"
"xar 20,88\nxar 21,89\nxar 22,90\nxar 23,91\nxmu 16,v16,8\nxmu 17,v17,8\nxmu 18,v18,8\nxmu 19,v19,8\nxmu 20,v20,8\nxmu 21,v21,8\nxmu 22,v22,8\n"
"xmu 23,v23,8\nxcv 56,16,17\nxcv 57,18,19\nxcv 58,20,21\nxcv 59,22,23\nxst1 56,2,28,s52,1024\nxar 24,84\nxar 25,85\nxar 26,86\nxar 27,87\nxar 28,92\n"
"xar 29,93\nxar 30,94\nxar 31,95\nxmu 24,v24,8\nxmu 25,v25,8\nxmu 26,v26,8\nxmu 27,v27,8\nxmu 28,v28,8\nxmu 29,v29,8\nxmu 30,v30,8\nxmu 31,v31,8\n"
"xcv 60,24,25\nxcv 61,26,27\nxcv 62,28,29\nxcv 63,30,31\nxst1 60,2,28,s52,1536\nxar 16,96\nxar 17,97\nxar 18,98\nxar 19,99\nxar 20,104\nxar 21,105\n"
"xar 22,106\nxar 23,107\nxmu 16,v16,8\nxmu 17,v17,8\nxmu 18,v18,8\nxmu 19,v19,8\nxmu 20,v20,8\nxmu 21,v21,8\nxmu 22,v22,8\nxmu 23,v23,8\nxcv 48,16,17\n"
"xcv 49,18,19\nxcv 50,20,21\nxcv 51,22,23\nxst1 48,2,28,s52,2048\nxar 24,100\nxar 25,101\nxar 26,102\nxar 27,103\nxar 28,108\nxar 29,109\nxar 30,110\n"
"xar 31,111\nxmu 24,v24,8\nxmu 25,v25,8\nxmu 26,v26,8\nxmu 27,v27,8\nxmu 28,v28,8\nxmu 29,v29,8\nxmu 30,v30,8\nxmu 31,v31,8\nxcv 52,24,25\n"
"xcv 53,26,27\nxcv 54,28,29\nxcv 55,30,31\nxst1 52,2,28,s52,2560\nxar 16,112\nxar 17,113\nxar 18,114\nxar 19,115\nxar 20,120\nxar 21,121\nxar 22,122\n"
"xar 23,123\nxmu 16,v16,8\nxmu 17,v17,8\nxmu 18,v18,8\nxmu 19,v19,8\nxmu 20,v20,8\nxmu 21,v21,8\nxmu 22,v22,8\nxmu 23,v23,8\nxcv 56,16,17\n"
"xcv 57,18,19\nxcv 58,20,21\nxcv 59,22,23\nxst1 56,2,28,s52,3072\nxar 24,116\nxar 25,117\nxar 26,118\nxar 27,119\nxar 28,124\nxar 29,125\nxar 30,126\n"
"xar 31,127\nxmu 24,v24,8\nxmu 25,v25,8\nxmu 26,v26,8\nxmu 27,v27,8\nxmu 28,v28,8\nxmu 29,v29,8\nxmu 30,v30,8\nxmu 31,v31,8\nxcv 60,24,25\n"
"xcv 61,26,27\nxcv 62,28,29\nxcv 63,30,31\nxst1 60,2,28,s52,3584\ns_waitcnt vmcnt(0)\ns_barrier\ns_cmp_eq_u32 s39,0\ns_cbranch_scc0 Lnosig_67_%=\n"
"v_mov_b32 v216,s6\nv_mov_b32 v217,s7\nv_mov_b32 v219,0\ns_mov_b64 exec,1\nbuffer_store_dwordx2 v[216:217],v219,s[76:79],s80 offen offset:0 sc1\n"
"s_mov_b64 exec,-1\nLnosig_67_%=:\ns_nop 1\ns_branch Lout_47_%=\nLfm_45_%=:\ns_nop 4\nv_mbcnt_lo_u32_b32 v252,-1,0\nv_mbcnt_hi_u32_b32 v252,-1,v252\n"
"s_and_b32 s95,s100,15\ns_lshl_b32 s95,s95,3\ns_lshr_b32 s94,s100,25\ns_and_b32 s94,s94,7\ns_add_u32 s95,s95,s94\ns_lshl_b32 s95,s95,17\n"
"s_add_u32 s95,0x5800000,s95\ns_add_u32 s16,s4,s95\ns_addc_u32 s17,s5,0\ns_and_b32 s17,s17,0xffff\ns_mov_b32 s18,0x20000\ns_mov_b32 s19,0x20000\n"
"s_and_b32 s95,s100,15\ns_lshl_b32 s95,s95,3\ns_lshr_b32 s94,s100,25\ns_and_b32 s94,s94,7\ns_add_u32 s95,s95,s94\ns_lshl_b32 s95,s95,11\n"
"s_add_u32 s95,0x6a00000,s95\ns_add_u32 s20,s4,s95\ns_addc_u32 s21,s5,0\ns_and_b32 s21,s21,0xffff\ns_mov_b32 s22,0x800\ns_mov_b32 s23,0x20000\n"
"v_lshlrev_b32 v0,4,v252\ns_lshl_b32 s95,s3,15\nv_add_u32 v0,s95,v0\nv_lshlrev_b32 v1,3,v252\ns_lshl_b32 s95,s3,9\nv_add_u32 v1,s95,v1\n"
"s_lshr_b32 s82,s100,4\ns_and_b32 s82,s82,15\nv_lshrrev_b32 v2,4,v252\nv_and_b32 v2,1,v2\ns_lshl_b32 s95,s3,1\nv_add_u32 v2,s95,v2\n"
"v_mul_u32_u24 v2,0x27000,v2\nv_lshrrev_b32 v247,5,v252\nv_lshlrev_b32 v247,8,v247\nv_add_u32 v2,v2,v247\nv_and_b32 v247,15,v252\n"
"v_lshlrev_b32 v247,4,v247\nv_add_u32 v2,v2,v247\nv_and_b32 v4,31,v252\ns_lshl_b32 s95,s3,5\nv_add_u32 v4,s95,v4\nv_lshlrev_b32 v4,2,v4\n"
"s_mul_i32 s95,s82,0x138000\ns_add_u32 s95,0x3000000,s95\ns_and_b32 s94,s100,15\ns_lshl_b32 s94,s94,13\ns_add_u32 s95,s95,s94\ns_add_u32 s28,s4,s95\n"
"s_addc_u32 s29,s5,0\ns_and_b32 s29,s29,0xffff\ns_and_b32 s30,s100,15\ns_lshl_b32 s30,s30,13\ns_sub_u32 s30,0x138000,s30\ns_mov_b32 s31,0x20000\n"
"s_lshl_b32 s95,s82,9\ns_add_u32 s95,0x5400000,s95\ns_add_u32 s32,s4,s95\ns_addc_u32 s33,s5,0\ns_and_b32 s33,s33,0xffff\ns_mov_b32 s34,0x200\n"
"s_mov_b32 s35,0x20000\ns_mov_b32 s39,s3\ns_and_b32 s74,s100,15\ns_lshl_b32 s74,s74,3\ns_lshr_b32 s95,s100,25\ns_and_b32 s95,s95,7\n"
"s_add_u32 s74,s74,s95\ns_lshl_b32 s74,s74,3\ns_add_u32 s74,0x4000,s74\ns_lshl_b32 s75,s82,7\ns_add_u32 s75,0x1000,s75\ns_add_u32 s76,s4,0x6c00000\n"
"s_addc_u32 s77,s5,0\ns_and_b32 s77,s77,0xffff\ns_mov_b32 s78,0x8000\ns_mov_b32 s79,0x20000\ns_lshl_b32 s80,s82,4\ns_and_b32 s95,s100,15\n"
"s_add_u32 s80,s80,s95\ns_lshl_b32 s80,s80,3\ns_add_u32 s80,0x6000,s80\ns_mov_b32 s53,0x30d40\nv_mbcnt_lo_u32_b32 v218,-1,0\n"
"v_mbcnt_hi_u32_b32 v218,-1,v218\nv_lshlrev_b32 v218,3,v218\nbuffer_load_dwordx2 v[216:217],v218,s[76:79],s75 offen offset:0 sc1\nLspin_68_%=:\n"
"s_waitcnt vmcnt(0)\nv_cmp_eq_u64 vcc,s[6:7],v[216:217]\ns_nop 3\ns_and_b32 s52,vcc_lo,0xffff\ns_cmp_eq_u32 s52,0xffff\ns_cbranch_scc1 Lspun_69_%=\n"
"s_sleep 2\nbuffer_load_dwordx2 v[216:217],v218,s[76:79],s75 offen offset:0 sc1\ns_sub_u32 s53,s53,1\ns_cmp_gt_i32 s53,0\ns_cbranch_scc1 Lspin_68_%=\n"
"s_waitcnt vmcnt(0)\nLspun_69_%=:\ns_nop 1\nbuffer_load_dword v3,v4,s[32:35],0 offen offset:0 sc1\ns_mov_b32 s53,0x30d40\n"
"v_mbcnt_lo_u32_b32 v218,-1,0\nv_mbcnt_hi_u32_b32 v218,-1,v218\nv_lshlrev_b32 v218,3,v218\n"
"buffer_load_dwordx2 v[216:217],v218,s[76:79],s74 offen offset:0 sc1\nLspin_70_%=:\ns_waitcnt vmcnt(0)\nv_cmp_eq_u64 vcc,s[6:7],v[216:217]\ns_nop 3\n"
"s_and_b32 s52,vcc_lo,1\ns_cmp_eq_u32 s52,1\ns_cbranch_scc1 Lspun_71_%=\ns_sleep 2\n"
"buffer_load_dwordx2 v[216:217],v218,s[76:79],s74 offen offset:0 sc1\ns_sub_u32 s53,s53,1\ns_cmp_gt_i32 s53,0\ns_cbranch_scc1 Lspin_70_%=\n"
"s_waitcnt vmcnt(0)\nLspun_71_%=:\ns_nop 1\nbuffer_load_dwordx2 v[10:11],v1,s[20:23],0 offen offset:0 sc1\ns_mov_b32 s52,0\n"
"buffer_load_dwordx4 a[128:131],v0,s[16:19],s52 offen offset:0 sc1\nbuffer_load_dwordx4 a[132:135],v0,s[16:19],s52 offen offset:1024 sc1\n"
"buffer_load_dwordx4 a[136:139],v0,s[16:19],s52 offen offset:2048 sc1\nbuffer_load_dwordx4 a[140:143],v0,s[16:19],s52 offen offset:3072 sc1\n"
"s_mov_b32 s52,0x1000\nbuffer_load_dwordx4 a[144:147],v0,s[16:19],s52 offen offset:0 sc1\n"
"buffer_load_dwordx4 a[148:151],v0,s[16:19],s52 offen offset:1024 sc1\nbuffer_load_dwordx4 a[152:155],v0,s[16:19],s52 offen offset:2048 sc1\n"
"buffer_load_dwordx4 a[156:159],v0,s[16:19],s52 offen offset:3072 sc1\ns_mov_b32 s52,0x2000\n"
"buffer_load_dwordx4 a[160:163],v0,s[16:19],s52 offen offset:0 sc1\nbuffer_load_dwordx4 a[164:167],v0,s[16:19],s52 offen offset:1024 sc1\n"
"buffer_load_dwordx4 a[168:171],v0,s[16:19],s52 offen offset:2048 sc1\nbuffer_load_dwordx4 a[172:175],v0,s[16:19],s52 offen offset:3072 sc1\n"
"s_mov_b32 s52,0x3000\nbuffer_load_dwordx4 a[176:179],v0,s[16:19],s52 offen offset:0 sc1\n"
"buffer_load_dwordx4 a[180:183],v0,s[16:19],s52 offen offset:1024 sc1\nbuffer_load_dwordx4 a[184:187],v0,s[16:19],s52 offen offset:2048 sc1\n"
"buffer_load_dwordx4 a[188:191],v0,s[16:19],s52 offen offset:3072 sc1\ns_mov_b32 s52,0x4000\n"
"buffer_load_dwordx4 a[192:195],v0,s[16:19],s52 offen offset:0 sc1\nbuffer_load_dwordx4 a[196:199],v0,s[16:19],s52 offen offset:1024 sc1\n"
"buffer_load_dwordx4 a[200:203],v0,s[16:19],s52 offen offset:2048 sc1\nbuffer_load_dwordx4 a[204:207],v0,s[16:19],s52 offen offset:3072 sc1\n"
"s_mov_b32 s52,0x5000\nbuffer_load_dwordx4 a[208:211],v0,s[16:19],s52 offen offset:0 sc1\n"
"buffer_load_dwordx4 a[212:215],v0,s[16:19],s52 offen offset:1024 sc1\nbuffer_load_dwordx4 a[216:219],v0,s[16:19],s52 offen offset:2048 sc1\n"
"buffer_load_dwordx4 a[220:223],v0,s[16:19],s52 offen offset:3072 sc1\ns_mov_b32 s52,0x6000\n"
"buffer_load_dwordx4 a[224:227],v0,s[16:19],s52 offen offset:0 sc1\nbuffer_load_dwordx4 a[228:231],v0,s[16:19],s52 offen offset:1024 sc1\n"
"buffer_load_dwordx4 a[232:235],v0,s[16:19],s52 offen offset:2048 sc1\nbuffer_load_dwordx4 a[236:239],v0,s[16:19],s52 offen offset:3072 sc1\n"
"s_mov_b32 s52,0x7000\nbuffer_load_dwordx4 a[240:243],v0,s[16:19],s52 offen offset:0 sc1\n"
"buffer_load_dwordx4 a[244:247],v0,s[16:19],s52 offen offset:1024 sc1\nbuffer_load_dwordx4 a[248:251],v0,s[16:19],s52 offen offset:2048 sc1\n"
"buffer_load_dwordx4 a[252:255],v0,s[16:19],s52 offen offset:3072 sc1\ns_waitcnt vmcnt(32)\nv_max_f32 v13,v6,v10\nv_sub_f32 v8,v6,v13\n"
"v_sub_f32 v9,v10,v13\nxex 8,8\nxex 9,9\nxmu 14,v7,8\nxfm 14,v11,v9,v14\nxrc 14,14\ns_nop 0\nxmu 14,v14,3\nxmu 8,v8,14\nxmu 9,v9,14\n"
"s_waitcnt vmcnt(24)\nxar 16,0\nxar 17,1\nxar 18,2\nxar 19,3\nxar 20,8\nxar 21,9\nxar 22,10\nxar 23,11\nxar 32,128\nxar 33,129\nxar 34,130\n"
"xar 35,131\nxar 36,136\nxar 37,137\nxar 38,138\nxar 39,139\nxmu 16,v16,8\nxmu 17,v17,8\nxmu 18,v18,8\nxmu 19,v19,8\nxmu 20,v20,8\nxmu 21,v21,8\n"
"xmu 22,v22,8\nxmu 23,v23,8\nxfm 16,v32,v9,v16\nxfm 17,v33,v9,v17\nxfm 18,v34,v9,v18\nxfm 19,v35,v9,v19\nxfm 20,v36,v9,v20\nxfm 21,v37,v9,v21\n"
"xfm 22,v38,v9,v22\nxfm 23,v39,v9,v23\nxcv 48,16,17\nxcv 49,18,19\nxcv 50,20,21\nxcv 51,22,23\ns_mov_b32 s52,0\nxst1 48,2,28,s52,0\nxar 24,4\n"
"xar 25,5\nxar 26,6\nxar 27,7\nxar 28,12\nxar 29,13\nxar 30,14\nxar 31,15\nxar 40,132\nxar 41,133\nxar 42,134\nxar 43,135\nxar 44,140\nxar 45,141\n"
"xar 46,142\nxar 47,143\nxmu 24,v24,8\nxmu 25,v25,8\nxmu 26,v26,8\nxmu 27,v27,8\nxmu 28,v28,8\nxmu 29,v29,8\nxmu 30,v30,8\nxmu 31,v31,8\n"
"xfm 24,v40,v9,v24\nxfm 25,v41,v9,v25\nxfm 26,v42,v9,v26\nxfm 27,v43,v9,v27\nxfm 28,v44,v9,v28\nxfm 29,v45,v9,v29\nxfm 30,v46,v9,v30\n"
"xfm 31,v47,v9,v31\nxcv 52,24,25\nxcv 53,26,27\nxcv 54,28,29\nxcv 55,30,31\nxst1 52,2,28,s52,512\nxar 16,16\nxar 17,17\nxar 18,18\nxar 19,19\n"
"xar 20,24\nxar 21,25\nxar 22,26\nxar 23,27\nxar 32,144\nxar 33,145\nxar 34,146\nxar 35,147\nxar 36,152\nxar 37,153\nxar 38,154\nxar 39,155\n"
"xmu 16,v16,8\nxmu 17,v17,8\nxmu 18,v18,8\nxmu 19,v19,8\nxmu 20,v20,8\nxmu 21,v21,8\nxmu 22,v22,8\nxmu 23,v23,8\nxfm 16,v32,v9,v16\nxfm 17,v33,v9,v17\n"
"xfm 18,v34,v9,v18\nxfm 19,v35,v9,v19\nxfm 20,v36,v9,v20\nxfm 21,v37,v9,v21\nxfm 22,v38,v9,v22\nxfm 23,v39,v9,v23\nxcv 56,16,17\nxcv 57,18,19\n"
"xcv 58,20,21\nxcv 59,22,23\nxst1 56,2,28,s52,1024\nxar 24,20\nxar 25,21\nxar 26,22\nxar 27,23\nxar 28,28\nxar 29,29\nxar 30,30\nxar 31,31\n"
"xar 40,148\nxar 41,149\nxar 42,150\nxar 43,151\nxar 44,156\nxar 45,157\nxar 46,158\nxar 47,159\nxmu 24,v24,8\nxmu 25,v25,8\nxmu 26,v26,8\n"
"xmu 27,v27,8\nxmu 28,v28,8\nxmu 29,v29,8\nxmu 30,v30,8\nxmu 31,v31,8\nxfm 24,v40,v9,v24\nxfm 25,v41,v9,v25\nxfm 26,v42,v9,v26\nxfm 27,v43,v9,v27\n"
"xfm 28,v44,v9,v28\nxfm 29,v45,v9,v29\nxfm 30,v46,v9,v30\nxfm 31,v47,v9,v31\nxcv 60,24,25\nxcv 61,26,27\nxcv 62,28,29\nxcv 63,30,31\n"
"xst1 60,2,28,s52,1536\ns_waitcnt vmcnt(20)\nxar 16,32\nxar 17,33\nxar 18,34\nxar 19,35\nxar 20,40\nxar 21,41\nxar 22,42\nxar 23,43\nxar 32,160\n"
"xar 33,161\nxar 34,162\nxar 35,163\nxar 36,168\nxar 37,169\nxar 38,170\nxar 39,171\nxmu 16,v16,8\nxmu 17,v17,8\nxmu 18,v18,8\nxmu 19,v19,8\n"
"xmu 20,v20,8\nxmu 21,v21,8\nxmu 22,v22,8\nxmu 23,v23,8\nxfm 16,v32,v9,v16\nxfm 17,v33,v9,v17\nxfm 18,v34,v9,v18\nxfm 19,v35,v9,v19\n"
"xfm 20,v36,v9,v20\nxfm 21,v37,v9,v21\nxfm 22,v38,v9,v22\nxfm 23,v39,v9,v23\nxcv 48,16,17\nxcv 49,18,19\nxcv 50,20,21\nxcv 51,22,23\n"
"xst1 48,2,28,s52,2048\nxar 24,36\nxar 25,37\nxar 26,38\nxar 27,39\nxar 28,44\nxar 29,45\nxar 30,46\nxar 31,47\nxar 40,164\nxar 41,165\nxar 42,166\n"
"xar 43,167\nxar 44,172\nxar 45,173\nxar 46,174\nxar 47,175\nxmu 24,v24,8\nxmu 25,v25,8\nxmu 26,v26,8\nxmu 27,v27,8\nxmu 28,v28,8\nxmu 29,v29,8\n"
"xmu 30,v30,8\nxmu 31,v31,8\nxfm 24,v40,v9,v24\nxfm 25,v41,v9,v25\nxfm 26,v42,v9,v26\nxfm 27,v43,v9,v27\nxfm 28,v44,v9,v28\nxfm 29,v45,v9,v29\n"
"xfm 30,v46,v9,v30\nxfm 31,v47,v9,v31\nxcv 52,24,25\nxcv 53,26,27\nxcv 54,28,29\nxcv 55,30,31\nxst1 52,2,28,s52,2560\nxar 16,48\nxar 17,49\nxar 18,50\n"
"xar 19,51\nxar 20,56\nxar 21,57\nxar 22,58\nxar 23,59\nxar 32,176\nxar 33,177\nxar 34,178\nxar 35,179\nxar 36,184\nxar 37,185\nxar 38,186\n"
"xar 39,187\nxmu 16,v16,8\nxmu 17,v17,8\nxmu 18,v18,8\nxmu 19,v19,8\nxmu 20,v20,8\nxmu 21,v21,8\nxmu 22,v22,8\nxmu 23,v23,8\nxfm 16,v32,v9,v16\n"
"xfm 17,v33,v9,v17\nxfm 18,v34,v9,v18\nxfm 19,v35,v9,v19\nxfm 20,v36,v9,v20\nxfm 21,v37,v9,v21\nxfm 22,v38,v9,v22\nxfm 23,v39,v9,v23\nxcv 56,16,17\n"
"xcv 57,18,19\nxcv 58,20,21\nxcv 59,22,23\nxst1 56,2,28,s52,3072\nxar 24,52\nxar 25,53\nxar 26,54\nxar 27,55\nxar 28,60\nxar 29,61\nxar 30,62\n"
"xar 31,63\nxar 40,180\nxar 41,181\nxar 42,182\nxar 43,183\nxar 44,188\nxar 45,189\nxar 46,190\nxar 47,191\nxmu 24,v24,8\nxmu 25,v25,8\nxmu 26,v26,8\n"
"xmu 27,v27,8\nxmu 28,v28,8\nxmu 29,v29,8\nxmu 30,v30,8\nxmu 31,v31,8\nxfm 24,v40,v9,v24\nxfm 25,v41,v9,v25\nxfm 26,v42,v9,v26\nxfm 27,v43,v9,v27\n"
"xfm 28,v44,v9,v28\nxfm 29,v45,v9,v29\nxfm 30,v46,v9,v30\nxfm 31,v47,v9,v31\nxcv 60,24,25\nxcv 61,26,27\nxcv 62,28,29\nxcv 63,30,31\n"
"xst1 60,2,28,s52,3584\ns_waitcnt vmcnt(16)\nxar 16,64\nxar 17,65\nxar 18,66\nxar 19,67\nxar 20,72\nxar 21,73\nxar 22,74\nxar 23,75\nxar 32,192\n"
"xar 33,193\nxar 34,194\nxar 35,195\nxar 36,200\nxar 37,201\nxar 38,202\nxar 39,203\nxmu 16,v16,8\nxmu 17,v17,8\nxmu 18,v18,8\nxmu 19,v19,8\n"
"xmu 20,v20,8\nxmu 21,v21,8\nxmu 22,v22,8\nxmu 23,v23,8\nxfm 16,v32,v9,v16\nxfm 17,v33,v9,v17\nxfm 18,v34,v9,v18\nxfm 19,v35,v9,v19\n"
"xfm 20,v36,v9,v20\nxfm 21,v37,v9,v21\nxfm 22,v38,v9,v22\nxfm 23,v39,v9,v23\nxcv 48,16,17\nxcv 49,18,19\nxcv 50,20,21\nxcv 51,22,23\n"
"s_mov_b32 s52,0x1000\nxst1 48,2,28,s52,0\nxar 24,68\nxar 25,69\nxar 26,70\nxar 27,71\nxar 28,76\nxar 29,77\nxar 30,78\nxar 31,79\nxar 40,196\n"
"xar 41,197\nxar 42,198\nxar 43,199\nxar 44,204\nxar 45,205\nxar 46,206\nxar 47,207\nxmu 24,v24,8\nxmu 25,v25,8\nxmu 26,v26,8\nxmu 27,v27,8\n"
"xmu 28,v28,8\nxmu 29,v29,8\nxmu 30,v30,8\nxmu 31,v31,8\nxfm 24,v40,v9,v24\nxfm 25,v41,v9,v25\nxfm 26,v42,v9,v26\nxfm 27,v43,v9,v27\n"
"xfm 28,v44,v9,v28\nxfm 29,v45,v9,v29\nxfm 30,v46,v9,v30\nxfm 31,v47,v9,v31\nxcv 52,24,25\nxcv 53,26,27\nxcv 54,28,29\nxcv 55,30,31\n"
"xst1 52,2,28,s52,512\nxar 16,80\nxar 17,81\nxar 18,82\nxar 19,83\nxar 20,88\nxar 21,89\nxar 22,90\nxar 23,91\nxar 32,208\nxar 33,209\nxar 34,210\n"
"xar 35,211\nxar 36,216\nxar 37,217\nxar 38,218\nxar 39,219\nxmu 16,v16,8\nxmu 17,v17,8\nxmu 18,v18,8\nxmu 19,v19,8\nxmu 20,v20,8\nxmu 21,v21,8\n"
"xmu 22,v22,8\nxmu 23,v23,8\nxfm 16,v32,v9,v16\nxfm 17,v33,v9,v17\nxfm 18,v34,v9,v18\nxfm 19,v35,v9,v19\nxfm 20,v36,v9,v20\nxfm 21,v37,v9,v21\n"
"xfm 22,v38,v9,v22\nxfm 23,v39,v9,v23\nxcv 56,16,17\nxcv 57,18,19\nxcv 58,20,21\nxcv 59,22,23\nxst1 56,2,28,s52,1024\nxar 24,84\nxar 25,85\nxar 26,86\n"
"xar 27,87\nxar 28,92\nxar 29,93\nxar 30,94\nxar 31,95\nxar 40,212\nxar 41,213\nxar 42,214\nxar 43,215\nxar 44,220\nxar 45,221\nxar 46,222\n"
"xar 47,223\nxmu 24,v24,8\nxmu 25,v25,8\nxmu 26,v26,8\nxmu 27,v27,8\nxmu 28,v28,8\nxmu 29,v29,8\nxmu 30,v30,8\nxmu 31,v31,8\nxfm 24,v40,v9,v24\n"
"xfm 25,v41,v9,v25\nxfm 26,v42,v9,v26\nxfm 27,v43,v9,v27\nxfm 28,v44,v9,v28\nxfm 29,v45,v9,v29\nxfm 30,v46,v9,v30\nxfm 31,v47,v9,v31\nxcv 60,24,25\n"
"xcv 61,26,27\nxcv 62,28,29\nxcv 63,30,31\nxst1 60,2,28,s52,1536\ns_waitcnt vmcnt(12)\nxar 16,96\nxar 17,97\nxar 18,98\nxar 19,99\nxar 20,104\n"
"xar 21,105\nxar 22,106\nxar 23,107\nxar 32,224\nxar 33,225\nxar 34,226\nxar 35,227\nxar 36,232\nxar 37,233\nxar 38,234\nxar 39,235\nxmu 16,v16,8\n"
"xmu 17,v17,8\nxmu 18,v18,8\nxmu 19,v19,8\nxmu 20,v20,8\nxmu 21,v21,8\nxmu 22,v22,8\nxmu 23,v23,8\nxfm 16,v32,v9,v16\nxfm 17,v33,v9,v17\n"
"xfm 18,v34,v9,v18\nxfm 19,v35,v9,v19\nxfm 20,v36,v9,v20\nxfm 21,v37,v9,v21\nxfm 22,v38,v9,v22\nxfm 23,v39,v9,v23\nxcv 48,16,17\nxcv 49,18,19\n"
"xcv 50,20,21\nxcv 51,22,23\nxst1 48,2,28,s52,2048\nxar 24,100\nxar 25,101\nxar 26,102\nxar 27,103\nxar 28,108\nxar 29,109\nxar 30,110\nxar 31,111\n"
"xar 40,228\nxar 41,229\nxar 42,230\nxar 43,231\nxar 44,236\nxar 45,237\nxar 46,238\nxar 47,239\nxmu 24,v24,8\nxmu 25,v25,8\nxmu 26,v26,8\n"
"xmu 27,v27,8\nxmu 28,v28,8\nxmu 29,v29,8\nxmu 30,v30,8\nxmu 31,v31,8\nxfm 24,v40,v9,v24\nxfm 25,v41,v9,v25\nxfm 26,v42,v9,v26\nxfm 27,v43,v9,v27\n"
"xfm 28,v44,v9,v28\nxfm 29,v45,v9,v29\nxfm 30,v46,v9,v30\nxfm 31,v47,v9,v31\nxcv 52,24,25\nxcv 53,26,27\nxcv 54,28,29\nxcv 55,30,31\n"
"xst1 52,2,28,s52,2560\nxar 16,112\nxar 17,113\nxar 18,114\nxar 19,115\nxar 20,120\nxar 21,121\nxar 22,122\nxar 23,123\nxar 32,240\nxar 33,241\n"
"xar 34,242\nxar 35,243\nxar 36,248\nxar 37,249\nxar 38,250\nxar 39,251\nxmu 16,v16,8\nxmu 17,v17,8\nxmu 18,v18,8\nxmu 19,v19,8\nxmu 20,v20,8\n"
"xmu 21,v21,8\nxmu 22,v22,8\nxmu 23,v23,8\nxfm 16,v32,v9,v16\nxfm 17,v33,v9,v17\nxfm 18,v34,v9,v18\nxfm 19,v35,v9,v19\nxfm 20,v36,v9,v20\n"
"xfm 21,v37,v9,v21\nxfm 22,v38,v9,v22\nxfm 23,v39,v9,v23\nxcv 56,16,17\nxcv 57,18,19\nxcv 58,20,21\nxcv 59,22,23\nxst1 56,2,28,s52,3072\nxar 24,116\n"
"xar 25,117\nxar 26,118\nxar 27,119\nxar 28,124\nxar 29,125\nxar 30,126\nxar 31,127\nxar 40,244\nxar 41,245\nxar 42,246\nxar 43,247\nxar 44,252\n"
"xar 45,253\nxar 46,254\nxar 47,255\nxmu 24,v24,8\nxmu 25,v25,8\nxmu 26,v26,8\nxmu 27,v27,8\nxmu 28,v28,8\nxmu 29,v29,8\nxmu 30,v30,8\nxmu 31,v31,8\n"
"xfm 24,v40,v9,v24\nxfm 25,v41,v9,v25\nxfm 26,v42,v9,v26\nxfm 27,v43,v9,v27\nxfm 28,v44,v9,v28\nxfm 29,v45,v9,v29\nxfm 30,v46,v9,v30\n"
"xfm 31,v47,v9,v31\nxcv 60,24,25\nxcv 61,26,27\nxcv 62,28,29\nxcv 63,30,31\nxst1 60,2,28,s52,3584\ns_waitcnt vmcnt(0)\ns_barrier\ns_cmp_eq_u32 s39,0\n"
"s_cbranch_scc0 Lnosig_72_%=\nv_mov_b32 v216,s6\nv_mov_b32 v217,s7\nv_mov_b32 v219,0\ns_mov_b64 exec,1\n"
"buffer_store_dwordx2 v[216:217],v219,s[76:79],s80 offen offset:0 sc1\ns_mov_b64 exec,-1\nLnosig_72_%=:\ns_nop 1\nLout_47_%=:\ns_lshr_b32 s98,s100,29\n"
"s_and_b32 s98,s98,1\ns_cmp_eq_u32 s98,0\ns_cbranch_scc1 Lnoout_73_%=\ns_nop 4\nv_mbcnt_lo_u32_b32 v252,-1,0\nv_mbcnt_hi_u32_b32 v252,-1,v252\n"
"s_load_dwordx2 s[76:77],s[0:1],40\ns_load_dwordx2 s[78:79],s[0:1],72\ns_load_dwordx2 s[80:81],s[0:1],88\nxwl 0\ns_and_b32 s74,s2,7\n"
"s_lshl_b32 s74,s74,2\ns_lshr_b32 s99,s2,3\ns_mul_i32 s99,s99,37\ns_lshr_b32 s99,s99,8\ns_add_u32 s74,s74,s99\ns_lshr_b32 s75,s2,3\n"
"s_lshr_b32 s99,s2,3\ns_mul_i32 s99,s99,37\ns_lshr_b32 s99,s99,8\ns_mul_i32 s99,s99,7\ns_sub_u32 s75,s75,s99\ns_mul_i32 s99,s74,0x9c000\n"
"s_add_u32 s99,0x3000000,s99\ns_add_u32 s99,s99,0x20000\ns_add_u32 s16,s4,s99\ns_addc_u32 s17,s5,0\ns_and_b32 s17,s17,0xffff\ns_mov_b32 s18,0x7c000\n"
"s_mov_b32 s19,0x20000\ns_lshl_b32 s99,s75,7\ns_add_u32 s20,s78,s99\ns_addc_u32 s21,s79,0\ns_and_b32 s21,s21,0xffff\ns_lshl_b32 s22,s75,7\n"
"s_sub_u32 s22,0xc4000,s22\ns_mov_b32 s23,0x20000\ns_mul_i32 s99,s74,0x9c000\ns_add_u32 s99,0x3000000,s99\ns_add_u32 s28,s4,s99\ns_addc_u32 s29,s5,0\n"
"s_and_b32 s29,s29,0xffff\ns_mov_b32 s30,0x9c000\ns_mov_b32 s31,0x20000\ns_lshl_b32 s99,s75,7\ns_add_u32 s32,s76,s99\ns_addc_u32 s33,s77,0\n"
"s_and_b32 s33,s33,0xffff\ns_lshl_b32 s34,s75,7\ns_sub_u32 s34,0x380000,s34\ns_mov_b32 s35,0x20000\ns_mul_i32 s99,s74,0xe000\ns_lshl_b32 s98,s75,7\n"
"s_add_u32 s99,s99,s98\ns_add_u32 s40,s80,s99\ns_addc_u32 s41,s81,0\ns_and_b32 s41,s41,0xffff\ns_lshl_b32 s42,s75,7\ns_sub_u32 s42,0xe000,s42\n"
"s_mov_b32 s43,0x20000\ns_add_u32 s44,s4,0x6c00000\ns_addc_u32 s45,s5,0\ns_and_b32 s45,s45,0xffff\ns_mov_b32 s46,0x8000\ns_mov_b32 s47,0x20000\n"
"v_lshlrev_b32 v0,4,v252\ns_mul_i32 s99,s3,0x27000\nv_add_u32 v0,s99,v0\nv_lshrrev_b32 v2,3,v252\ns_lshl_b32 s99,s3,4\nv_add_u32 v2,s99,v2\n"
"v_mul_u32_u24 v2,0x380,v2\nv_and_b32 v247,7,v252\nv_lshrrev_b32 v247,1,v247\nv_lshrrev_b32 v246,3,v252\nv_lshrrev_b32 v246,1,v246\n"
"v_and_b32 v246,3,v246\nv_xor_b32 v247,v247,v246\nv_lshlrev_b32 v247,1,v247\nv_and_b32 v246,7,v252\nv_and_b32 v246,1,v246\nv_or_b32 v247,v247,v246\n"
"v_lshlrev_b32 v247,4,v247\nv_add_u32 v2,v2,v247\nv_lshlrev_b32 v6,4,v252\ns_lshr_b32 s99,s3,1\ns_lshl_b32 s99,s99,13\nv_add_u32 v6,s99,v6\n"
"v_lshrrev_b32 v8,4,v252\nv_lshlrev_b32 v8,2,v8\nv_and_b32 v247,15,v252\nv_lshrrev_b32 v247,2,v247\nv_add_u32 v8,v8,v247\nv_lshlrev_b32 v8,7,v8\n"
"v_lshrrev_b32 v247,4,v252\nv_lshlrev_b32 v247,2,v247\nv_and_b32 v246,15,v252\nv_lshrrev_b32 v246,2,v246\nv_add_u32 v247,v247,v246\n"
"v_lshrrev_b32 v247,1,v247\nv_and_b32 v247,3,v247\ns_and_b32 s99,s3,1\ns_lshl_b32 s99,s99,1\nv_xor_b32 v247,s99,v247\nv_lshlrev_b32 v247,5,v247\n"
"v_add_u32 v8,v8,v247\nv_and_b32 v247,3,v252\nv_lshlrev_b32 v247,3,v247\nv_add_u32 v8,v8,v247\nv_add_u32 v8,0x14000,v8\nv_lshrrev_b32 v9,4,v252\n"
"v_lshlrev_b32 v9,2,v9\nv_and_b32 v247,15,v252\nv_lshrrev_b32 v247,2,v247\nv_add_u32 v9,v9,v247\nv_lshlrev_b32 v9,7,v9\nv_lshrrev_b32 v247,4,v252\n"
"v_lshlrev_b32 v247,2,v247\nv_and_b32 v246,15,v252\nv_lshrrev_b32 v246,2,v246\nv_add_u32 v247,v247,v246\nv_lshrrev_b32 v247,1,v247\n"
"v_and_b32 v247,3,v247\ns_and_b32 s99,s3,1\ns_lshl_b32 s99,s99,1\ns_add_u32 s99,s99,1\nv_xor_b32 v247,s99,v247\nv_lshlrev_b32 v247,5,v247\n"
"v_add_u32 v9,v9,v247\nv_and_b32 v247,3,v252\nv_lshlrev_b32 v247,3,v247\nv_add_u32 v9,v9,v247\nv_add_u32 v9,0x14000,v9\nv_and_b32 v30,15,v252\n"
"s_lshr_b32 s99,s3,1\ns_lshl_b32 s99,s99,5\nv_add_u32 v30,s99,v30\nv_mul_u32_u24 v30,0x380,v30\ns_and_b32 s99,s3,1\ns_lshl_b32 s99,s99,6\n"
"v_add_u32 v30,s99,v30\nv_lshrrev_b32 v247,4,v252\nv_lshlrev_b32 v247,3,v247\nv_add_u32 v30,v30,v247\ns_lshr_b32 s52,s74,1\ns_lshl_b32 s52,s52,7\n"
"s_add_u32 s52,0x5000,s52\ns_lshr_b32 s53,s74,1\ns_lshl_b32 s53,s53,7\ns_add_u32 s53,0x6000,s53\ns_mov_b32 s70,0x30d40\nv_mbcnt_lo_u32_b32 v26,-1,0\n"
"v_mbcnt_hi_u32_b32 v26,-1,v26\nv_lshlrev_b32 v26,3,v26\nbuffer_load_dwordx2 v[24:25],v26,s[44:47],s52 offen offset:0 sc1\nLspin_74_%=:\n"
"s_waitcnt vmcnt(0)\nv_cmp_eq_u64 vcc,s[6:7],v[24:25]\ns_nop 3\ns_and_b32 s69,vcc_lo,0x3fff\ns_cmp_eq_u32 s69,0x3fff\ns_cbranch_scc1 Lspun_75_%=\n"
"s_sleep 2\nbuffer_load_dwordx2 v[24:25],v26,s[44:47],s52 offen offset:0 sc1\ns_sub_u32 s70,s70,1\ns_cmp_gt_i32 s70,0\ns_cbranch_scc1 Lspin_74_%=\n"
"s_waitcnt vmcnt(0)\nLspun_75_%=:\ns_nop 1\ns_mul_i32 s62,s3,0x1000\ns_mov_b32 s74,0x1c00\ns_mov_b32 s75,0xe000\ns_mov_b32 s76,0xfc00\n"
"s_mul_i32 s63,s3,0x800\nxam 62,0\ns_nop 0\nxdl1 0,16,0,0\nxam 62,0\ns_nop 0\nxdl1 0,16,0,1024\nxam 62,0\ns_nop 0\nxdl1 0,16,0,2048\nxam 62,0\n"
"s_nop 0\nxdl1 0,16,0,3072\ns_add_u32 s16,s16,0x1000\ns_addc_u32 s17,s17,0\ns_sub_u32 s18,s18,0x1000\nxam 63,0x14000\ns_nop 0\nxdl 2,20,0,0\n"
"xam 63,0x14400\ns_nop 0\nxdl 2,20,s74,0\nxam 63,0x16000\ns_nop 0\nxdl 2,20,s75,0\nxam 63,0x16400\ns_nop 0\nxdl 2,20,s76,0\ns_add_u32 s20,s20,0x1c000\n"
"s_addc_u32 s21,s21,0\ns_sub_u32 s22,s22,0x1c000\nxam 62,0x4000\ns_nop 0\nxdl1 0,16,0,0\nxam 62,0x4000\ns_nop 0\nxdl1 0,16,0,1024\nxam 62,0x4000\n"
"s_nop 0\nxdl1 0,16,0,2048\nxam 62,0x4000\ns_nop 0\nxdl1 0,16,0,3072\ns_add_u32 s16,s16,0x1000\ns_addc_u32 s17,s17,0\ns_sub_u32 s18,s18,0x1000\n"
"xam 63,0x18000\ns_nop 0\nxdl 2,20,0,0\nxam 63,0x18400\ns_nop 0\nxdl 2,20,s74,0\nxam 63,0x1a000\ns_nop 0\nxdl 2,20,s75,0\nxam 63,0x1a400\ns_nop 0\n"
"xdl 2,20,s76,0\ns_add_u32 s20,s20,0x1c000\ns_addc_u32 s21,s21,0\ns_sub_u32 s22,s22,0x1c000\nxam 62,0x8000\ns_nop 0\nxdl1 0,16,0,0\nxam 62,0x8000\n"
"s_nop 0\nxdl1 0,16,0,1024\nxam 62,0x8000\ns_nop 0\nxdl1 0,16,0,2048\nxam 62,0x8000\ns_nop 0\nxdl1 0,16,0,3072\ns_add_u32 s16,s16,0x1000\n"
"s_addc_u32 s17,s17,0\ns_sub_u32 s18,s18,0x1000\nxam 63,0x1c000\ns_nop 0\nxdl 2,20,0,0\nxam 63,0x1c400\ns_nop 0\nxdl 2,20,s74,0\nxam 63,0x1e000\n"
"s_nop 0\nxdl 2,20,s75,0\nxam 63,0x1e400\ns_nop 0\nxdl 2,20,s76,0\ns_add_u32 s20,s20,0x1c000\ns_addc_u32 s21,s21,0\ns_sub_u32 s22,s22,0x1c000\n"
"xam 62,0xc000\ns_nop 0\nxdl1 0,16,0,0\nxam 62,0xc000\ns_nop 0\nxdl1 0,16,0,1024\nxam 62,0xc000\ns_nop 0\nxdl1 0,16,0,2048\nxam 62,0xc000\ns_nop 0\n"
"xdl1 0,16,0,3072\ns_add_u32 s16,s16,0x1000\ns_addc_u32 s17,s17,0\ns_sub_u32 s18,s18,0x1000\nxam 63,0x20000\ns_nop 0\nxdl 2,20,0,0\nxam 63,0x20400\n"
"s_nop 0\nxdl 2,20,s74,0\nxam 63,0x22000\ns_nop 0\nxdl 2,20,s75,0\nxam 63,0x22400\ns_nop 0\nxdl 2,20,s76,0\ns_add_u32 s20,s20,0x1c000\n"
"s_addc_u32 s21,s21,0\ns_sub_u32 s22,s22,0x1c000\nxam 62,0x10000\ns_nop 0\nxdl1 0,16,0,0\nxam 62,0x10000\ns_nop 0\nxdl1 0,16,0,1024\nxam 62,0x10000\n"
"s_nop 0\nxdl1 0,16,0,2048\nxam 62,0x10000\ns_nop 0\nxdl1 0,16,0,3072\ns_add_u32 s16,s16,0x1000\ns_addc_u32 s17,s17,0\ns_sub_u32 s18,s18,0x1000\n"
"xam 63,0x24000\ns_nop 0\nxdl 2,20,0,0\nxam 63,0x24400\ns_nop 0\nxdl 2,20,s74,0\nxam 63,0x26000\ns_nop 0\nxdl 2,20,s75,0\nxam 63,0x26400\ns_nop 0\n"
"xdl 2,20,s76,0\ns_add_u32 s20,s20,0x1c000\ns_addc_u32 s21,s21,0\ns_sub_u32 s22,s22,0x1c000\nv_add_u32 v20,0x10000,v6\nv_add_u32 v21,0x10000,v8\n"
"v_add_u32 v22,0x10000,v9\ns_waitcnt vmcnt(32)\ns_barrier\nxrt 40,8,0\nxrt 42,8,2048\nxrt 44,9,0\nxrt 46,9,2048\nxrb 32,6,0\nxrb 36,6,4096\n"
"xrt 56,8,4096\nxrt 58,8,6144\nxrt 60,9,4096\nxrt 62,9,6144\nxrb 48,6,1024\nxrb 52,6,5120\nxrt 72,8,8192\nxrt 74,8,10240\nxrt 76,9,8192\n"
"xrt 78,9,10240\nxrb 64,6,2048\nxrb 68,6,6144\nxrt 88,8,12288\nxrt 90,8,14336\nxrt 92,9,12288\nxrt 94,9,14336\nxrb 80,6,3072\nxrb 84,6,7168\n"
"s_waitcnt vmcnt(24) lgkmcnt(0)\ns_barrier\nxam 62,0\nxm16z 0,40,32\nxdl1 0,16,0,0\nxrt 104,8,16384\nxrt 106,8,18432\nxam 62,0\nxm16z 4,44,32\n"
"xdl1 0,16,0,1024\nxrt 108,9,16384\nxrt 110,9,18432\nxm16z 8,40,36\nxrb 96,6,16384\nxrb 100,6,20480\nxam 62,0\nxm16z 12,44,36\nxdl1 0,16,0,2048\n"
"xrt 120,8,20480\nxrt 122,8,22528\nxm16 0,56,48\nxrt 124,9,20480\nxrt 126,9,22528\nxam 62,0\nxm16 4,60,48\nxdl1 0,16,0,3072\ns_add_u32 s16,s16,0x1000\n"
"s_addc_u32 s17,s17,0\ns_sub_u32 s18,s18,0x1000\nxrb 112,6,17408\nxrb 116,6,21504\nxm16 8,56,52\nxrt 136,8,24576\nxrt 138,8,26624\nxam 63,0x14000\n"
"xm16 12,60,52\nxdl 2,20,0,0\nxrt 140,9,24576\nxrt 142,9,26624\nxam 63,0x14400\nxm16 0,72,64\nxdl 2,20,s74,0\nxrb 128,6,18432\nxrb 132,6,22528\n"
"xm16 4,76,64\nxrt 152,8,28672\nxrt 154,8,30720\nxam 63,0x16000\nxm16 8,72,68\nxdl 2,20,s75,0\nxrt 156,9,28672\nxrt 158,9,30720\nxm16 12,76,68\n"
"xrb 144,6,19456\nxrb 148,6,23552\nxam 63,0x16400\nxm16 0,88,80\nxdl 2,20,s76,0\ns_add_u32 s20,s20,0x1c000\ns_addc_u32 s21,s21,0\n"
"s_sub_u32 s22,s22,0x1c000\nxm16 4,92,80\nxm16 8,88,84\nxm16 12,92,84\ns_waitcnt vmcnt(24) lgkmcnt(0)\ns_barrier\nxam 62,0x4000\nxm16 0,104,96\n"
"xdl1 0,16,0,0\nxrt 40,8,32768\nxrt 42,8,34816\nxam 62,0x4000\nxm16 4,108,96\nxdl1 0,16,0,1024\nxrt 44,9,32768\nxrt 46,9,34816\nxm16 8,104,100\n"
"xrb 32,6,32768\nxrb 36,6,36864\nxam 62,0x4000\nxm16 12,108,100\nxdl1 0,16,0,2048\nxrt 56,8,36864\nxrt 58,8,38912\nxm16 0,120,112\nxrt 60,9,36864\n"
"xrt 62,9,38912\nxam 62,0x4000\nxm16 4,124,112\nxdl1 0,16,0,3072\ns_mov_b32 s16,s28\ns_mov_b32 s17,s29\ns_mov_b32 s18,s30\nxrb 48,6,33792\n"
"xrb 52,6,37888\nxm16 8,120,116\nxrt 72,8,40960\nxrt 74,8,43008\nxam 63,0x18000\nxm16 12,124,116\nxdl 2,20,0,0\nxrt 76,9,40960\nxrt 78,9,43008\n"
"xam 63,0x18400\nxm16 0,136,128\nxdl 2,20,s74,0\nxrb 64,6,34816\nxrb 68,6,38912\nxm16 4,140,128\nxrt 88,8,45056\nxrt 90,8,47104\nxam 63,0x1a000\n"
"xm16 8,136,132\nxdl 2,20,s75,0\nxrt 92,9,45056\nxrt 94,9,47104\nxm16 12,140,132\nxrb 80,6,35840\nxrb 84,6,39936\nxam 63,0x1a400\nxm16 0,152,144\n"
"xdl 2,20,s76,0\ns_mov_b32 s20,s32\ns_mov_b32 s21,s33\ns_mov_b32 s22,s34\nxm16 4,156,144\nxm16 8,152,148\nxm16 12,156,148\ns_mov_b32 s70,0x30d40\n"
"v_mbcnt_lo_u32_b32 v26,-1,0\nv_mbcnt_hi_u32_b32 v26,-1,v26\nv_lshlrev_b32 v26,3,v26\n"
"buffer_load_dwordx2 v[24:25],v26,s[44:47],s53 offen offset:0 sc1\nLspin_76_%=:\ns_waitcnt vmcnt(0)\nv_cmp_eq_u64 vcc,s[6:7],v[24:25]\ns_nop 3\n"
"s_and_b32 s69,vcc_lo,0xffff\ns_cmp_eq_u32 s69,0xffff\ns_cbranch_scc1 Lspun_77_%=\ns_sleep 2\n"
"buffer_load_dwordx2 v[24:25],v26,s[44:47],s53 offen offset:0 sc1\ns_sub_u32 s70,s70,1\ns_cmp_gt_i32 s70,0\ns_cbranch_scc1 Lspin_76_%=\n"
"s_waitcnt vmcnt(0)\nLspun_77_%=:\ns_nop 1\ns_waitcnt vmcnt(24) lgkmcnt(0)\ns_barrier\nxam 62,0x8000\nxm16 0,40,32\nxdl1 0,16,0,0\nxrt 104,8,49152\n"
"xrt 106,8,51200\nxam 62,0x8000\nxm16 4,44,32\nxdl1 0,16,0,1024\nxrt 108,9,49152\nxrt 110,9,51200\nxm16 8,40,36\nxrb 96,6,49152\nxrb 100,6,53248\n"
"xam 62,0x8000\nxm16 12,44,36\nxdl1 0,16,0,2048\nxrt 120,8,53248\nxrt 122,8,55296\nxm16 0,56,48\nxrt 124,9,53248\nxrt 126,9,55296\nxam 62,0x8000\n"
"xm16 4,60,48\nxdl1 0,16,0,3072\ns_add_u32 s16,s16,0x1000\ns_addc_u32 s17,s17,0\ns_sub_u32 s18,s18,0x1000\nxrb 112,6,50176\nxrb 116,6,54272\n"
"xm16 8,56,52\nxrt 136,8,57344\nxrt 138,8,59392\nxam 63,0x1c000\nxm16 12,60,52\nxdl 2,20,0,0\nxrt 140,9,57344\nxrt 142,9,59392\nxam 63,0x1c400\n"
"xm16 0,72,64\nxdl 2,20,s74,0\nxrb 128,6,51200\nxrb 132,6,55296\nxm16 4,76,64\nxrt 152,8,61440\nxrt 154,8,63488\nxam 63,0x1e000\nxm16 8,72,68\n"
"xdl 2,20,s75,0\nxrt 156,9,61440\nxrt 158,9,63488\nxm16 12,76,68\nxrb 144,6,52224\nxrb 148,6,56320\nxam 63,0x1e400\nxm16 0,88,80\nxdl 2,20,s76,0\n"
"s_add_u32 s20,s20,0x1c000\ns_addc_u32 s21,s21,0\ns_sub_u32 s22,s22,0x1c000\nxm16 4,92,80\nxm16 8,88,84\nxm16 12,92,84\ns_mov_b32 s71,3\nLloop_78_%=:\n"
"s_waitcnt vmcnt(24) lgkmcnt(0)\ns_barrier\nxam 62,0xc000\nxm16 0,104,96\nxdl1 0,16,0,0\nxrt 40,21,0\nxrt 42,21,2048\nxam 62,0xc000\nxm16 4,108,96\n"
"xdl1 0,16,0,1024\nxrt 44,22,0\nxrt 46,22,2048\nxm16 8,104,100\nxrb 32,20,0\nxrb 36,20,4096\nxam 62,0xc000\nxm16 12,108,100\nxdl1 0,16,0,2048\n"
"xrt 56,21,4096\nxrt 58,21,6144\nxm16 0,120,112\nxrt 60,22,4096\nxrt 62,22,6144\nxam 62,0xc000\nxm16 4,124,112\nxdl1 0,16,0,3072\n"
"s_add_u32 s16,s16,0x1000\ns_addc_u32 s17,s17,0\ns_sub_u32 s18,s18,0x1000\nxrb 48,20,1024\nxrb 52,20,5120\nxm16 8,120,116\nxrt 72,21,8192\n"
"xrt 74,21,10240\nxam 63,0x20000\nxm16 12,124,116\nxdl 2,20,0,0\nxrt 76,22,8192\nxrt 78,22,10240\nxam 63,0x20400\nxm16 0,136,128\nxdl 2,20,s74,0\n"
"xrb 64,20,2048\nxrb 68,20,6144\nxm16 4,140,128\nxrt 88,21,12288\nxrt 90,21,14336\nxam 63,0x22000\nxm16 8,136,132\nxdl 2,20,s75,0\nxrt 92,22,12288\n"
"xrt 94,22,14336\nxm16 12,140,132\nxrb 80,20,3072\nxrb 84,20,7168\nxam 63,0x22400\nxm16 0,152,144\nxdl 2,20,s76,0\ns_add_u32 s20,s20,0x1c000\n"
"s_addc_u32 s21,s21,0\ns_sub_u32 s22,s22,0x1c000\nxm16 4,156,144\nxm16 8,152,148\nxm16 12,156,148\ns_waitcnt vmcnt(24) lgkmcnt(0)\ns_barrier\n"
"xam 62,0x10000\nxm16 0,40,32\nxdl1 0,16,0,0\nxrt 104,8,0\nxrt 106,8,2048\nxam 62,0x10000\nxm16 4,44,32\nxdl1 0,16,0,1024\nxrt 108,9,0\n"
"xrt 110,9,2048\nxm16 8,40,36\nxrb 96,6,0\nxrb 100,6,4096\nxam 62,0x10000\nxm16 12,44,36\nxdl1 0,16,0,2048\nxrt 120,8,4096\nxrt 122,8,6144\n"
"xm16 0,56,48\nxrt 124,9,4096\nxrt 126,9,6144\nxam 62,0x10000\nxm16 4,60,48\nxdl1 0,16,0,3072\ns_add_u32 s16,s16,0x1000\ns_addc_u32 s17,s17,0\n"
"s_sub_u32 s18,s18,0x1000\nxrb 112,6,1024\nxrb 116,6,5120\nxm16 8,56,52\nxrt 136,8,8192\nxrt 138,8,10240\nxam 63,0x24000\nxm16 12,60,52\nxdl 2,20,0,0\n"
"xrt 140,9,8192\nxrt 142,9,10240\nxam 63,0x24400\nxm16 0,72,64\nxdl 2,20,s74,0\nxrb 128,6,2048\nxrb 132,6,6144\nxm16 4,76,64\nxrt 152,8,12288\n"
"xrt 154,8,14336\nxam 63,0x26000\nxm16 8,72,68\nxdl 2,20,s75,0\nxrt 156,9,12288\nxrt 158,9,14336\nxm16 12,76,68\nxrb 144,6,3072\nxrb 148,6,7168\n"
"xam 63,0x26400\nxm16 0,88,80\nxdl 2,20,s76,0\ns_add_u32 s20,s20,0x1c000\ns_addc_u32 s21,s21,0\ns_sub_u32 s22,s22,0x1c000\nxm16 4,92,80\nxm16 8,88,84\n"
"xm16 12,92,84\ns_waitcnt vmcnt(24) lgkmcnt(0)\ns_barrier\nxam 62,0\nxm16 0,104,96\nxdl1 0,16,0,0\nxrt 40,8,16384\nxrt 42,8,18432\nxam 62,0\n"
"xm16 4,108,96\nxdl1 0,16,0,1024\nxrt 44,9,16384\nxrt 46,9,18432\nxm16 8,104,100\nxrb 32,6,16384\nxrb 36,6,20480\nxam 62,0\nxm16 12,108,100\n"
"xdl1 0,16,0,2048\nxrt 56,8,20480\nxrt 58,8,22528\nxm16 0,120,112\nxrt 60,9,20480\nxrt 62,9,22528\nxam 62,0\nxm16 4,124,112\nxdl1 0,16,0,3072\n"
"s_add_u32 s16,s16,0x1000\ns_addc_u32 s17,s17,0\ns_sub_u32 s18,s18,0x1000\nxrb 48,6,17408\nxrb 52,6,21504\nxm16 8,120,116\nxrt 72,8,24576\n"
"xrt 74,8,26624\nxam 63,0x14000\nxm16 12,124,116\nxdl 2,20,0,0\nxrt 76,9,24576\nxrt 78,9,26624\nxam 63,0x14400\nxm16 0,136,128\nxdl 2,20,s74,0\n"
"xrb 64,6,18432\nxrb 68,6,22528\nxm16 4,140,128\nxrt 88,8,28672\nxrt 90,8,30720\nxam 63,0x16000\nxm16 8,136,132\nxdl 2,20,s75,0\nxrt 92,9,28672\n"
"xrt 94,9,30720\nxm16 12,140,132\nxrb 80,6,19456\nxrb 84,6,23552\nxam 63,0x16400\nxm16 0,152,144\nxdl 2,20,s76,0\ns_add_u32 s20,s20,0x1c000\n"
"s_addc_u32 s21,s21,0\ns_sub_u32 s22,s22,0x1c000\nxm16 4,156,144\nxm16 8,152,148\nxm16 12,156,148\ns_waitcnt vmcnt(24) lgkmcnt(0)\ns_barrier\n"
"xam 62,0x4000\nxm16 0,40,32\nxdl1 0,16,0,0\nxrt 104,8,32768\nxrt 106,8,34816\nxam 62,0x4000\nxm16 4,44,32\nxdl1 0,16,0,1024\nxrt 108,9,32768\n"
"xrt 110,9,34816\nxm16 8,40,36\nxrb 96,6,32768\nxrb 100,6,36864\nxam 62,0x4000\nxm16 12,44,36\nxdl1 0,16,0,2048\nxrt 120,8,36864\nxrt 122,8,38912\n"
"xm16 0,56,48\nxrt 124,9,36864\nxrt 126,9,38912\nxam 62,0x4000\nxm16 4,60,48\nxdl1 0,16,0,3072\ns_add_u32 s16,s16,0x1000\ns_addc_u32 s17,s17,0\n"
"s_sub_u32 s18,s18,0x1000\nxrb 112,6,33792\nxrb 116,6,37888\nxm16 8,56,52\nxrt 136,8,40960\nxrt 138,8,43008\nxam 63,0x18000\nxm16 12,60,52\n"
"xdl 2,20,0,0\nxrt 140,9,40960\nxrt 142,9,43008\nxam 63,0x18400\nxm16 0,72,64\nxdl 2,20,s74,0\nxrb 128,6,34816\nxrb 132,6,38912\nxm16 4,76,64\n"
"xrt 152,8,45056\nxrt 154,8,47104\nxam 63,0x1a000\nxm16 8,72,68\nxdl 2,20,s75,0\nxrt 156,9,45056\nxrt 158,9,47104\nxm16 12,76,68\nxrb 144,6,35840\n"
"xrb 148,6,39936\nxam 63,0x1a400\nxm16 0,88,80\nxdl 2,20,s76,0\ns_add_u32 s20,s20,0x1c000\ns_addc_u32 s21,s21,0\ns_sub_u32 s22,s22,0x1c000\n"
"xm16 4,92,80\nxm16 8,88,84\nxm16 12,92,84\ns_waitcnt vmcnt(24) lgkmcnt(0)\ns_barrier\nxam 62,0x8000\nxm16 0,104,96\nxdl1 0,16,0,0\nxrt 40,8,49152\n"
"xrt 42,8,51200\nxam 62,0x8000\nxm16 4,108,96\nxdl1 0,16,0,1024\nxrt 44,9,49152\nxrt 46,9,51200\nxm16 8,104,100\nxrb 32,6,49152\nxrb 36,6,53248\n"
"xam 62,0x8000\nxm16 12,108,100\nxdl1 0,16,0,2048\nxrt 56,8,53248\nxrt 58,8,55296\nxm16 0,120,112\nxrt 60,9,53248\nxrt 62,9,55296\nxam 62,0x8000\n"
"xm16 4,124,112\nxdl1 0,16,0,3072\ns_add_u32 s16,s16,0x1000\ns_addc_u32 s17,s17,0\ns_sub_u32 s18,s18,0x1000\nxrb 48,6,50176\nxrb 52,6,54272\n"
"xm16 8,120,116\nxrt 72,8,57344\nxrt 74,8,59392\nxam 63,0x1c000\nxm16 12,124,116\nxdl 2,20,0,0\nxrt 76,9,57344\nxrt 78,9,59392\nxam 63,0x1c400\n"
"xm16 0,136,128\nxdl 2,20,s74,0\nxrb 64,6,51200\nxrb 68,6,55296\nxm16 4,140,128\nxrt 88,8,61440\nxrt 90,8,63488\nxam 63,0x1e000\nxm16 8,136,132\n"
"xdl 2,20,s75,0\nxrt 92,9,61440\nxrt 94,9,63488\nxm16 12,140,132\nxrb 80,6,52224\nxrb 84,6,56320\nxam 63,0x1e400\nxm16 0,152,144\nxdl 2,20,s76,0\n"
"s_add_u32 s20,s20,0x1c000\ns_addc_u32 s21,s21,0\ns_sub_u32 s22,s22,0x1c000\nxm16 4,156,144\nxm16 8,152,148\nxm16 12,156,148\n"
"s_waitcnt vmcnt(24) lgkmcnt(0)\ns_barrier\nxam 62,0xc000\nxm16 0,40,32\nxdl1 0,16,0,0\nxrt 104,21,0\nxrt 106,21,2048\nxam 62,0xc000\nxm16 4,44,32\n"
"xdl1 0,16,0,1024\nxrt 108,22,0\nxrt 110,22,2048\nxm16 8,40,36\nxrb 96,20,0\nxrb 100,20,4096\nxam 62,0xc000\nxm16 12,44,36\nxdl1 0,16,0,2048\n"
"xrt 120,21,4096\nxrt 122,21,6144\nxm16 0,56,48\nxrt 124,22,4096\nxrt 126,22,6144\nxam 62,0xc000\nxm16 4,60,48\nxdl1 0,16,0,3072\n"
"s_add_u32 s16,s16,0x1000\ns_addc_u32 s17,s17,0\ns_sub_u32 s18,s18,0x1000\nxrb 112,20,1024\nxrb 116,20,5120\nxm16 8,56,52\nxrt 136,21,8192\n"
"xrt 138,21,10240\nxam 63,0x20000\nxm16 12,60,52\nxdl 2,20,0,0\nxrt 140,22,8192\nxrt 142,22,10240\nxam 63,0x20400\nxm16 0,72,64\nxdl 2,20,s74,0\n"
"xrb 128,20,2048\nxrb 132,20,6144\nxm16 4,76,64\nxrt 152,21,12288\nxrt 154,21,14336\nxam 63,0x22000\nxm16 8,72,68\nxdl 2,20,s75,0\nxrt 156,22,12288\n"
"xrt 158,22,14336\nxm16 12,76,68\nxrb 144,20,3072\nxrb 148,20,7168\nxam 63,0x22400\nxm16 0,88,80\nxdl 2,20,s76,0\ns_add_u32 s20,s20,0x1c000\n"
"s_addc_u32 s21,s21,0\ns_sub_u32 s22,s22,0x1c000\nxm16 4,92,80\nxm16 8,88,84\nxm16 12,92,84\ns_waitcnt vmcnt(24) lgkmcnt(0)\ns_barrier\n"
"xam 62,0x10000\nxm16 0,104,96\nxdl1 0,16,0,0\nxrt 40,8,0\nxrt 42,8,2048\nxam 62,0x10000\nxm16 4,108,96\nxdl1 0,16,0,1024\nxrt 44,9,0\nxrt 46,9,2048\n"
"xm16 8,104,100\nxrb 32,6,0\nxrb 36,6,4096\nxam 62,0x10000\nxm16 12,108,100\nxdl1 0,16,0,2048\nxrt 56,8,4096\nxrt 58,8,6144\nxm16 0,120,112\n"
"xrt 60,9,4096\nxrt 62,9,6144\nxam 62,0x10000\nxm16 4,124,112\nxdl1 0,16,0,3072\ns_add_u32 s16,s16,0x1000\ns_addc_u32 s17,s17,0\n"
"s_sub_u32 s18,s18,0x1000\nxrb 48,6,1024\nxrb 52,6,5120\nxm16 8,120,116\nxrt 72,8,8192\nxrt 74,8,10240\nxam 63,0x24000\nxm16 12,124,116\nxdl 2,20,0,0\n"
"xrt 76,9,8192\nxrt 78,9,10240\nxam 63,0x24400\nxm16 0,136,128\nxdl 2,20,s74,0\nxrb 64,6,2048\nxrb 68,6,6144\nxm16 4,140,128\nxrt 88,8,12288\n"
"xrt 90,8,14336\nxam 63,0x26000\nxm16 8,136,132\nxdl 2,20,s75,0\nxrt 92,9,12288\nxrt 94,9,14336\nxm16 12,140,132\nxrb 80,6,3072\nxrb 84,6,7168\n"
"xam 63,0x26400\nxm16 0,152,144\nxdl 2,20,s76,0\ns_add_u32 s20,s20,0x1c000\ns_addc_u32 s21,s21,0\ns_sub_u32 s22,s22,0x1c000\nxm16 4,156,144\n"
"xm16 8,152,148\nxm16 12,156,148\ns_waitcnt vmcnt(24) lgkmcnt(0)\ns_barrier\nxam 62,0\nxm16 0,40,32\nxdl1 0,16,0,0\nxrt 104,8,16384\nxrt 106,8,18432\n"
"xam 62,0\nxm16 4,44,32\nxdl1 0,16,0,1024\nxrt 108,9,16384\nxrt 110,9,18432\nxm16 8,40,36\nxrb 96,6,16384\nxrb 100,6,20480\nxam 62,0\nxm16 12,44,36\n"
"xdl1 0,16,0,2048\nxrt 120,8,20480\nxrt 122,8,22528\nxm16 0,56,48\nxrt 124,9,20480\nxrt 126,9,22528\nxam 62,0\nxm16 4,60,48\nxdl1 0,16,0,3072\n"
"s_add_u32 s16,s16,0x1000\ns_addc_u32 s17,s17,0\ns_sub_u32 s18,s18,0x1000\nxrb 112,6,17408\nxrb 116,6,21504\nxm16 8,56,52\nxrt 136,8,24576\n"
"xrt 138,8,26624\nxam 63,0x14000\nxm16 12,60,52\nxdl 2,20,0,0\nxrt 140,9,24576\nxrt 142,9,26624\nxam 63,0x14400\nxm16 0,72,64\nxdl 2,20,s74,0\n"
"xrb 128,6,18432\nxrb 132,6,22528\nxm16 4,76,64\nxrt 152,8,28672\nxrt 154,8,30720\nxam 63,0x16000\nxm16 8,72,68\nxdl 2,20,s75,0\nxrt 156,9,28672\n"
"xrt 158,9,30720\nxm16 12,76,68\nxrb 144,6,19456\nxrb 148,6,23552\nxam 63,0x16400\nxm16 0,88,80\nxdl 2,20,s76,0\ns_add_u32 s20,s20,0x1c000\n"
"s_addc_u32 s21,s21,0\ns_sub_u32 s22,s22,0x1c000\nxm16 4,92,80\nxm16 8,88,84\nxm16 12,92,84\ns_waitcnt vmcnt(24) lgkmcnt(0)\ns_barrier\nxam 62,0x4000\n"
"xm16 0,104,96\nxdl1 0,16,0,0\nxrt 40,8,32768\nxrt 42,8,34816\nxam 62,0x4000\nxm16 4,108,96\nxdl1 0,16,0,1024\nxrt 44,9,32768\nxrt 46,9,34816\n"
"xm16 8,104,100\nxrb 32,6,32768\nxrb 36,6,36864\nxam 62,0x4000\nxm16 12,108,100\nxdl1 0,16,0,2048\nxrt 56,8,36864\nxrt 58,8,38912\nxm16 0,120,112\n"
"xrt 60,9,36864\nxrt 62,9,38912\nxam 62,0x4000\nxm16 4,124,112\nxdl1 0,16,0,3072\ns_add_u32 s16,s16,0x1000\ns_addc_u32 s17,s17,0\n"
"s_sub_u32 s18,s18,0x1000\nxrb 48,6,33792\nxrb 52,6,37888\nxm16 8,120,116\nxrt 72,8,40960\nxrt 74,8,43008\nxam 63,0x18000\nxm16 12,124,116\n"
"xdl 2,20,0,0\nxrt 76,9,40960\nxrt 78,9,43008\nxam 63,0x18400\nxm16 0,136,128\nxdl 2,20,s74,0\nxrb 64,6,34816\nxrb 68,6,38912\nxm16 4,140,128\n"
"xrt 88,8,45056\nxrt 90,8,47104\nxam 63,0x1a000\nxm16 8,136,132\nxdl 2,20,s75,0\nxrt 92,9,45056\nxrt 94,9,47104\nxm16 12,140,132\nxrb 80,6,35840\n"
"xrb 84,6,39936\nxam 63,0x1a400\nxm16 0,152,144\nxdl 2,20,s76,0\ns_add_u32 s20,s20,0x1c000\ns_addc_u32 s21,s21,0\ns_sub_u32 s22,s22,0x1c000\n"
"xm16 4,156,144\nxm16 8,152,148\nxm16 12,156,148\ns_waitcnt vmcnt(24) lgkmcnt(0)\ns_barrier\nxam 62,0x8000\nxm16 0,40,32\nxdl1 0,16,0,0\n"
"xrt 104,8,49152\nxrt 106,8,51200\nxam 62,0x8000\nxm16 4,44,32\nxdl1 0,16,0,1024\nxrt 108,9,49152\nxrt 110,9,51200\nxm16 8,40,36\nxrb 96,6,49152\n"
"xrb 100,6,53248\nxam 62,0x8000\nxm16 12,44,36\nxdl1 0,16,0,2048\nxrt 120,8,53248\nxrt 122,8,55296\nxm16 0,56,48\nxrt 124,9,53248\nxrt 126,9,55296\n"
"xam 62,0x8000\nxm16 4,60,48\nxdl1 0,16,0,3072\ns_add_u32 s16,s16,0x1000\ns_addc_u32 s17,s17,0\ns_sub_u32 s18,s18,0x1000\nxrb 112,6,50176\n"
"xrb 116,6,54272\nxm16 8,56,52\nxrt 136,8,57344\nxrt 138,8,59392\nxam 63,0x1c000\nxm16 12,60,52\nxdl 2,20,0,0\nxrt 140,9,57344\nxrt 142,9,59392\n"
"xam 63,0x1c400\nxm16 0,72,64\nxdl 2,20,s74,0\nxrb 128,6,51200\nxrb 132,6,55296\nxm16 4,76,64\nxrt 152,8,61440\nxrt 154,8,63488\nxam 63,0x1e000\n"
"xm16 8,72,68\nxdl 2,20,s75,0\nxrt 156,9,61440\nxrt 158,9,63488\nxm16 12,76,68\nxrb 144,6,52224\nxrb 148,6,56320\nxam 63,0x1e400\nxm16 0,88,80\n"
"xdl 2,20,s76,0\ns_add_u32 s20,s20,0x1c000\ns_addc_u32 s21,s21,0\ns_sub_u32 s22,s22,0x1c000\nxm16 4,92,80\nxm16 8,88,84\nxm16 12,92,84\n"
"s_sub_u32 s71,s71,1\ns_cmp_gt_i32 s71,0\ns_cbranch_scc1 Lloop_78_%=\ns_waitcnt vmcnt(24) lgkmcnt(0)\ns_barrier\nxam 62,0xc000\nxm16 0,104,96\n"
"xdl1 0,16,0,0\nxrt 40,21,0\nxrt 42,21,2048\nxam 62,0xc000\nxm16 4,108,96\nxdl1 0,16,0,1024\nxrt 44,22,0\nxrt 46,22,2048\nxm16 8,104,100\nxrb 32,20,0\n"
"xrb 36,20,4096\nxam 62,0xc000\nxm16 12,108,100\nxdl1 0,16,0,2048\nxrt 56,21,4096\nxrt 58,21,6144\nxm16 0,120,112\nxrt 60,22,4096\nxrt 62,22,6144\n"
"xam 62,0xc000\nxm16 4,124,112\nxdl1 0,16,0,3072\ns_add_u32 s16,s16,0x1000\ns_addc_u32 s17,s17,0\ns_sub_u32 s18,s18,0x1000\nxrb 48,20,1024\n"
"xrb 52,20,5120\nxm16 8,120,116\nxrt 72,21,8192\nxrt 74,21,10240\nxam 63,0x20000\nxm16 12,124,116\nxdl 2,20,0,0\nxrt 76,22,8192\nxrt 78,22,10240\n"
"xam 63,0x20400\nxm16 0,136,128\nxdl 2,20,s74,0\nxrb 64,20,2048\nxrb 68,20,6144\nxm16 4,140,128\nxrt 88,21,12288\nxrt 90,21,14336\nxam 63,0x22000\n"
"xm16 8,136,132\nxdl 2,20,s75,0\nxrt 92,22,12288\nxrt 94,22,14336\nxm16 12,140,132\nxrb 80,20,3072\nxrb 84,20,7168\nxam 63,0x22400\nxm16 0,152,144\n"
"xdl 2,20,s76,0\ns_add_u32 s20,s20,0x1c000\ns_addc_u32 s21,s21,0\ns_sub_u32 s22,s22,0x1c000\nxm16 4,156,144\nxm16 8,152,148\nxm16 12,156,148\n"
"s_waitcnt vmcnt(24) lgkmcnt(0)\ns_barrier\nxm16 0,40,32\nxrt 104,8,0\nxrt 106,8,2048\nxm16 4,44,32\nxrt 108,9,0\nxrt 110,9,2048\nxm16 8,40,36\n"
"xrb 96,6,0\nxrb 100,6,4096\nxm16 12,44,36\nxrt 120,8,4096\nxrt 122,8,6144\nxm16 0,56,48\nxrt 124,9,4096\nxrt 126,9,6144\nxm16 4,60,48\n"
"xrb 112,6,1024\nxrb 116,6,5120\nxm16 8,56,52\nxrt 136,8,8192\nxrt 138,8,10240\nxm16 12,60,52\nxrt 140,9,8192\nxrt 142,9,10240\nxm16 0,72,64\n"
"xrb 128,6,2048\nxrb 132,6,6144\nxm16 4,76,64\nxrt 152,8,12288\nxrt 154,8,14336\nxm16 8,72,68\nxrt 156,9,12288\nxrt 158,9,14336\nxm16 12,76,68\n"
"xrb 144,6,3072\nxrb 148,6,7168\nxm16 0,88,80\nxm16 4,92,80\nxm16 8,88,84\nxm16 12,92,84\ns_waitcnt vmcnt(16) lgkmcnt(0)\ns_barrier\nxm16 0,104,96\n"
"xrt 40,8,16384\nxrt 42,8,18432\nxm16 4,108,96\nxrt 44,9,16384\nxrt 46,9,18432\nxm16 8,104,100\nxrb 32,6,16384\nxrb 36,6,20480\nxm16 12,108,100\n"
"xrt 56,8,20480\nxrt 58,8,22528\nxm16 0,120,112\nxrt 60,9,20480\nxrt 62,9,22528\nxm16 4,124,112\nxrb 48,6,17408\nxrb 52,6,21504\nxm16 8,120,116\n"
"xrt 72,8,24576\nxrt 74,8,26624\nxm16 12,124,116\nxrt 76,9,24576\nxrt 78,9,26624\nxm16 0,136,128\nxrb 64,6,18432\nxrb 68,6,22528\nxm16 4,140,128\n"
"xrt 88,8,28672\nxrt 90,8,30720\nxm16 8,136,132\nxrt 92,9,28672\nxrt 94,9,30720\nxm16 12,140,132\nxrb 80,6,19456\nxrb 84,6,23552\nxm16 0,152,144\n"
"xm16 4,156,144\nxm16 8,152,148\nxm16 12,156,148\ns_waitcnt vmcnt(8) lgkmcnt(0)\ns_barrier\nxm16 0,40,32\nxrt 104,8,32768\nxrt 106,8,34816\n"
"xm16 4,44,32\nxrt 108,9,32768\nxrt 110,9,34816\nxm16 8,40,36\nxrb 96,6,32768\nxrb 100,6,36864\nxm16 12,44,36\nxrt 120,8,36864\nxrt 122,8,38912\n"
"xm16 0,56,48\nxrt 124,9,36864\nxrt 126,9,38912\nxm16 4,60,48\nxrb 112,6,33792\nxrb 116,6,37888\nxm16 8,56,52\nxrt 136,8,40960\nxrt 138,8,43008\n"
"xm16 12,60,52\nxrt 140,9,40960\nxrt 142,9,43008\nxm16 0,72,64\nxrb 128,6,34816\nxrb 132,6,38912\nxm16 4,76,64\nxrt 152,8,45056\nxrt 154,8,47104\n"
"xm16 8,72,68\nxrt 156,9,45056\nxrt 158,9,47104\nxm16 12,76,68\nxrb 144,6,35840\nxrb 148,6,39936\nxm16 0,88,80\nxm16 4,92,80\nxm16 8,88,84\n"
"xm16 12,92,84\ns_waitcnt vmcnt(0) lgkmcnt(0)\ns_barrier\nxm16 0,104,96\nxrt 40,8,49152\nxrt 42,8,51200\nxm16 4,108,96\nxrt 44,9,49152\n"
"xrt 46,9,51200\nxm16 8,104,100\nxrb 32,6,49152\nxrb 36,6,53248\nxm16 12,108,100\nxrt 56,8,53248\nxrt 58,8,55296\nxm16 0,120,112\nxrt 60,9,53248\n"
"xrt 62,9,55296\nxm16 4,124,112\nxrb 48,6,50176\nxrb 52,6,54272\nxm16 8,120,116\nxrt 72,8,57344\nxrt 74,8,59392\nxm16 12,124,116\nxrt 76,9,57344\n"
"xrt 78,9,59392\nxm16 0,136,128\nxrb 64,6,51200\nxrb 68,6,55296\nxm16 4,140,128\nxrt 88,8,61440\nxrt 90,8,63488\nxm16 8,136,132\nxrt 92,9,61440\n"
"xrt 94,9,63488\nxm16 12,140,132\nxrb 80,6,52224\nxrb 84,6,56320\nxm16 0,152,144\nxm16 4,156,144\nxm16 8,152,148\nxm16 12,156,148\nxwl 15\n"
"xm16 0,40,32\nxm16 4,44,32\nxm16 8,40,36\nxm16 12,44,36\nxwl 13\nxm16 0,56,48\nxm16 4,60,48\nxwl 12\nxm16 8,56,52\nxm16 12,60,52\nxwl 7\n"
"xm16 0,72,64\nxm16 4,76,64\nxwl 6\nxm16 8,72,68\nxm16 12,76,68\nxwl 1\nxm16 0,88,80\nxm16 4,92,80\nxwl 0\nxm16 8,88,84\nxm16 12,92,84\ns_nop 7\n"
"xar 160,0\nxar 161,1\nxar 162,2\nxar 163,3\nxcv 168,160,161\nxcv 169,162,163\ns_mov_b32 s68,0\n"
"buffer_store_dwordx2 v[168:169],v30,s[40:43],s68 offen offset:0\nxar 164,4\nxar 165,5\nxar 166,6\nxar 167,7\nxcv 170,164,165\nxcv 171,166,167\n"
"s_mov_b32 s68,0\nbuffer_store_dwordx2 v[170:171],v30,s[40:43],s68 offen offset:32\ns_nop 1\nxar 160,8\nxar 161,9\nxar 162,10\nxar 163,11\n"
"xcv 168,160,161\nxcv 169,162,163\ns_mov_b32 s68,0x3800\nbuffer_store_dwordx2 v[168:169],v30,s[40:43],s68 offen offset:0\nxar 164,12\nxar 165,13\n"
"xar 166,14\nxar 167,15\nxcv 170,164,165\nxcv 171,166,167\ns_mov_b32 s68,0x3800\nbuffer_store_dwordx2 v[170:171],v30,s[40:43],s68 offen offset:32\n"
"s_nop 1\ns_waitcnt vmcnt(0)\nLnoout_73_%=:\ns_mov_b32 s98,s2\ns_cmp_lg_u32 s98,0\ns_cbranch_scc1 Lend_4_%=\ns_nop 4\ns_add_u32 s44,s4,0x6c00000\n"
"s_addc_u32 s45,s5,0\ns_and_b32 s45,s45,0xffff\ns_mov_b32 s46,0x8000\ns_mov_b32 s47,0x20000\ns_mov_b32 s73,0x1000\ns_mov_b32 s70,0x30d40\n"
"v_mbcnt_lo_u32_b32 v26,-1,0\nv_mbcnt_hi_u32_b32 v26,-1,v26\nv_lshlrev_b32 v26,3,v26\n"
"buffer_load_dwordx2 v[24:25],v26,s[44:47],s73 offen offset:0 sc1\nLspin_79_%=:\ns_waitcnt vmcnt(0)\nv_cmp_eq_u64 vcc,s[6:7],v[24:25]\ns_nop 3\n"
"s_cmp_eq_u64 vcc,-1\ns_cbranch_scc1 Lspun_80_%=\ns_sleep 2\nbuffer_load_dwordx2 v[24:25],v26,s[44:47],s73 offen offset:0 sc1\ns_sub_u32 s70,s70,1\n"
"s_cmp_gt_i32 s70,0\ns_cbranch_scc1 Lspin_79_%=\ns_waitcnt vmcnt(0)\nLspun_80_%=:\ns_nop 1\ns_mov_b32 s73,0x1200\ns_mov_b32 s70,0x30d40\n"
"v_mbcnt_lo_u32_b32 v26,-1,0\nv_mbcnt_hi_u32_b32 v26,-1,v26\nv_lshlrev_b32 v26,3,v26\n"
"buffer_load_dwordx2 v[24:25],v26,s[44:47],s73 offen offset:0 sc1\nLspin_81_%=:\ns_waitcnt vmcnt(0)\nv_cmp_eq_u64 vcc,s[6:7],v[24:25]\ns_nop 3\n"
"s_cmp_eq_u64 vcc,-1\ns_cbranch_scc1 Lspun_82_%=\ns_sleep 2\nbuffer_load_dwordx2 v[24:25],v26,s[44:47],s73 offen offset:0 sc1\ns_sub_u32 s70,s70,1\n"
"s_cmp_gt_i32 s70,0\ns_cbranch_scc1 Lspin_81_%=\ns_waitcnt vmcnt(0)\nLspun_82_%=:\ns_nop 1\ns_mov_b32 s73,0x1400\ns_mov_b32 s70,0x30d40\n"
"v_mbcnt_lo_u32_b32 v26,-1,0\nv_mbcnt_hi_u32_b32 v26,-1,v26\nv_lshlrev_b32 v26,3,v26\n"
"buffer_load_dwordx2 v[24:25],v26,s[44:47],s73 offen offset:0 sc1\nLspin_83_%=:\ns_waitcnt vmcnt(0)\nv_cmp_eq_u64 vcc,s[6:7],v[24:25]\ns_nop 3\n"
"s_cmp_eq_u64 vcc,-1\ns_cbranch_scc1 Lspun_84_%=\ns_sleep 2\nbuffer_load_dwordx2 v[24:25],v26,s[44:47],s73 offen offset:0 sc1\ns_sub_u32 s70,s70,1\n"
"s_cmp_gt_i32 s70,0\ns_cbranch_scc1 Lspin_83_%=\ns_waitcnt vmcnt(0)\nLspun_84_%=:\ns_nop 1\ns_mov_b32 s73,0x1600\ns_mov_b32 s70,0x30d40\n"
"v_mbcnt_lo_u32_b32 v26,-1,0\nv_mbcnt_hi_u32_b32 v26,-1,v26\nv_lshlrev_b32 v26,3,v26\n"
"buffer_load_dwordx2 v[24:25],v26,s[44:47],s73 offen offset:0 sc1\nLspin_85_%=:\ns_waitcnt vmcnt(0)\nv_cmp_eq_u64 vcc,s[6:7],v[24:25]\ns_nop 3\n"
"s_cmp_eq_u64 vcc,-1\ns_cbranch_scc1 Lspun_86_%=\ns_sleep 2\nbuffer_load_dwordx2 v[24:25],v26,s[44:47],s73 offen offset:0 sc1\ns_sub_u32 s70,s70,1\n"
"s_cmp_gt_i32 s70,0\ns_cbranch_scc1 Lspin_85_%=\ns_waitcnt vmcnt(0)\nLspun_86_%=:\ns_nop 1\ns_mov_b32 s73,0\ns_cmp_eq_u32 s3,0\n"
"s_cbranch_scc0 Lnosig_87_%=\nv_mov_b32 v24,s6\nv_mov_b32 v25,s7\nv_mov_b32 v27,0\ns_mov_b64 exec,1\n"
"buffer_store_dwordx2 v[24:25],v27,s[44:47],s73 offen offset:0 sc1\ns_mov_b64 exec,-1\nLnosig_87_%=:\ns_nop 1\ns_waitcnt vmcnt(0)\nLend_4_%=:\n"
"s_waitcnt vmcnt(0) lgkmcnt(0)\n.purgem xad\n.purgem xam\n.purgem xar\n.purgem xcv\n.purgem xdl\n.purgem xdl1\n.purgem xex\n.purgem xfm\n.purgem xm16\n"
".purgem xm16z\n.purgem xm32\n.purgem xm32v\n.purgem xmu\n.purgem xrb\n.purgem xrc\n.purgem xrt\n.purgem xst1\n.purgem xwl\n.purgem xx3\n"
  : "+{v0}"(tid)
  : "{s[0:1]}"(ka), "{s2}"(bid)
  : "memory", "scc", "vcc", "v1", "v2", "v3", "v4", "v5", "v6", "v7", "v8", "v9", "v10", "v11", "v12", "v13", "v14", "v15", "v16", "v17", "v18", "v19", "v20", "v21", "v22", "v23", "v24", "v25", "v26", "v27", "v28", "v29", "v30", "v31", "v32", "v33", "v34", "v35", "v36", "v37", "v38", "v39", "v40", "v41", "v42", "v43", "v44", "v45", "v46", "v47", "v48", "v49", "v50", "v51", "v52", "v53", "v54", "v55", "v56", "v57", "v58", "v59", "v60", "v61", "v62", "v63", "v64", "v65", "v66", "v67", "v68", "v69", "v70", "v71", "v72", "v73", "v74", "v75", "v76", "v77", "v78", "v79", "v80", "v81", "v82", "v83", "v84", "v85", "v86", "v87", "v88", "v89", "v90", "v91", "v92", "v93", "v94", "v95", "v96", "v97", "v98", "v99", "v100", "v101", "v102", "v103", "v104", "v105", "v106", "v107", "v108", "v109", "v110", "v111", "v112", "v113", "v114", "v115", "v116", "v117", "v118", "v119", "v120", "v121", "v122", "v123", "v124", "v125", "v126", "v127", "v128", "v129", "v130", "v131", "v132", "v133", "v134", "v135", "v136", "v137", "v138", "v139", "v140", "v141", "v142", "v143", "v144", "v145", "v146", "v147", "v148", "v149", "v150", "v151", "v152", "v153", "v154", "v155", "v156", "v157", "v158", "v159", "v160", "v161", "v162", "v163", "v164", "v165", "v166", "v167", "v168", "v169", "v170", "v171", "v172", "v173", "v174", "v175", "v176", "v177", "v178", "v179", "v180", "v181", "v182", "v183", "v184", "v185", "v186", "v187", "v188", "v189", "v190", "v191", "v192", "v193", "v194", "v195", "v196", "v197", "v198", "v199", "v200", "v201", "v202", "v203", "v204", "v205", "v206", "v207", "v208", "v209", "v210", "v211", "v212", "v213", "v214", "v215", "v216", "v217", "v218", "v219", "v220", "v221", "v222", "v223", "v224", "v225", "v226", "v227", "v228", "v229", "v230", "v231", "v232", "v233", "v234", "v235", "v236", "v237", "v238", "v239", "v240", "v241", "v242", "v243", "v244", "v245", "v246", "v247", "v248", "v249", "v250", "v251", "v252", "v253", "v254", "v255", "s3", "s4", "s5", "s6", "s7", "s8", "s9", "s10", "s11", "s12", "s13", "s14", "s15", "s16", "s17", "s18", "s19", "s20", "s21", "s22", "s23", "s24", "s25", "s26", "s27", "s28", "s29", "s30", "s31", "s32", "s33", "s34", "s35", "s36", "s37", "s38", "s39", "s40", "s41", "s42", "s43", "s44", "s45", "s46", "s47", "s48", "s49", "s50", "s51", "s52", "s53", "s54", "s55", "s56", "s57", "s58", "s59", "s60", "s61", "s62", "s63", "s64", "s65", "s66", "s67", "s68", "s69", "s70", "s71", "s72", "s73", "s74", "s75", "s76", "s77", "s78", "s79", "s80", "s81", "s82", "s83", "s84", "s85", "s86", "s87", "s88", "s89", "s90", "s91", "s92", "s93", "s94", "s95", "s96", "s97", "s98", "s99", "s100", "s101", "a0", "a1", "a2", "a3", "a4", "a5", "a6", "a7", "a8", "a9", "a10", "a11", "a12", "a13", "a14", "a15", "a16", "a17", "a18", "a19", "a20", "a21", "a22", "a23", "a24", "a25", "a26", "a27", "a28", "a29", "a30", "a31", "a32", "a33", "a34", "a35", "a36", "a37", "a38", "a39", "a40", "a41", "a42", "a43", "a44", "a45", "a46", "a47", "a48", "a49", "a50", "a51", "a52", "a53", "a54", "a55", "a56", "a57", "a58", "a59", "a60", "a61", "a62", "a63", "a64", "a65", "a66", "a67", "a68", "a69", "a70", "a71", "a72", "a73", "a74", "a75", "a76", "a77", "a78", "a79", "a80", "a81", "a82", "a83", "a84", "a85", "a86", "a87", "a88", "a89", "a90", "a91", "a92", "a93", "a94", "a95", "a96", "a97", "a98", "a99", "a100", "a101", "a102", "a103", "a104", "a105", "a106", "a107", "a108", "a109", "a110", "a111", "a112", "a113", "a114", "a115", "a116", "a117", "a118", "a119", "a120", "a121", "a122", "a123", "a124", "a125", "a126", "a127", "a128", "a129", "a130", "a131", "a132", "a133", "a134", "a135", "a136", "a137", "a138", "a139", "a140", "a141", "a142", "a143", "a144", "a145", "a146", "a147", "a148", "a149", "a150", "a151", "a152", "a153", "a154", "a155", "a156", "a157", "a158", "a159", "a160", "a161", "a162", "a163", "a164", "a165", "a166", "a167", "a168", "a169", "a170", "a171", "a172", "a173", "a174", "a175", "a176", "a177", "a178", "a179", "a180", "a181", "a182", "a183", "a184", "a185", "a186", "a187", "a188", "a189", "a190", "a191", "a192", "a193", "a194", "a195", "a196", "a197", "a198", "a199", "a200", "a201", "a202", "a203", "a204", "a205", "a206", "a207", "a208", "a209", "a210", "a211", "a212", "a213", "a214", "a215", "a216", "a217", "a218", "a219", "a220", "a221", "a222", "a223", "a224", "a225", "a226", "a227", "a228", "a229", "a230", "a231", "a232", "a233", "a234", "a235", "a236", "a237", "a238", "a239", "a240", "a241", "a242", "a243", "a244", "a245", "a246", "a247", "a248", "a249", "a250", "a251", "a252", "a253", "a254", "a255");
}

extern "C" void kernel_launch(void* const* d_in, const int* in_sizes, int n_in, void* d_out, int out_size, void* d_ws, size_t ws_size, hipStream_t stream) {
  Params p;
  p.x = (const char*)d_in[0]; p.ckv = (const char*)d_in[1]; p.wq = (const char*)d_in[2]; p.wuk = (const char*)d_in[3]; p.wuv = (const char*)d_in[4];
  p.wo = (const char*)d_in[5]; p.win = (const char*)d_in[7]; p.wsp = (const char*)d_in[8]; p.bsp = (const char*)d_in[9]; p.wout = (const char*)d_in[10];
  p.gatew = (const char*)d_in[11]; p.out = (char*)d_out; p.ws = (char*)d_ws;
  hipFuncSetAttribute((const void*)fused_block, hipFuncAttributeMaxDynamicSharedMemorySize, 163840);
  fused_block<<<256, 256, 163840, stream>>>(p);
}
```
